```python
import torch

def _softmax(x, axis=-1):
    return torch.softmax(x, dim=axis)
def _gelu(x):
    return torch.nn.functional.gelu(x, approximate="tanh")
def _silu(x):
    return torch.nn.functional.silu(x)
def _sigmoid(x):
    return torch.sigmoid(x)
def _layernorm(x, axis=-1, eps=1e-5):
    m = x.mean(dim=axis, keepdim=True)
    v = x.var(dim=axis, keepdim=True, unbiased=False)
    return (x - m) / torch.sqrt(v + eps)
def _scatter_add(tgt, idx, src, axis=0):
    return tgt.clone().index_add_(axis, idx, src)
def _segment_sum(src, seg, n):
    out = torch.zeros((n,) + src.shape[1:], dtype=src.dtype)
    return out.index_add_(0, seg, src)
def _pad(x, pw):
    flat = tuple(v for pair in reversed(pw) for v in pair)
    return torch.nn.functional.pad(x, flat)

kernel_name = 'concat_heads__gqa_block_cached__sliding_block_cached__decode'

def setup_inputs():

    _w = (lambda a: (lambda t: t.to(torch.bfloat16) if t.is_floating_point() else t)(torch.as_tensor(a)))
    def _A_setup():
        B, Sq, D = 1, 1, 512
        Hq, Hkv, Sk, Dh = 32, 8, 65536, 128
        assert Hq % Hkv == 0, (Hq, Hkv)
        return {
            "x": _w((lambda *s: torch.randn(*s, dtype=torch.bfloat16) * 0.4)(B, Sq, D)),
            "k_cache": _w((lambda *s: torch.randn(*s, dtype=torch.bfloat16) * 3.2)(B, Hkv, Sk, Dh)),
            "v_cache": _w((lambda *s: torch.randn(*s, dtype=torch.bfloat16) * 0.8)(B, Hkv, Sk, Dh)),
            "wq": _w((lambda *s: torch.randn(*s, dtype=torch.bfloat16) * 3.2)(D, Hq * Dh) * (D ** -0.5)),
            "wo": _w((lambda *s: torch.randn(*s, dtype=torch.bfloat16) * 0.8)(Hq * Dh, D) * ((Hq * Dh) ** -0.5)),
        }
    _A_in = _A_setup()

    def _B_setup():
        B, Sq, D = 1, 1, 512
        Hq, Hkv, Sk, Dh = 32, 8, 65536, 128
        assert Hq % Hkv == 0, (Hq, Hkv)
        return {
            "x": _w((lambda *s: torch.randn(*s, dtype=torch.bfloat16) * 0.2)(B, Sq, D)),
            "k_cache": _w((lambda *s: torch.randn(*s, dtype=torch.bfloat16) * 3.2)(B, Hkv, Sk, Dh)),
            "v_cache": _w((lambda *s: torch.randn(*s, dtype=torch.bfloat16) * 1.6)(B, Hkv, Sk, Dh)),
            "wq": _w((lambda *s: torch.randn(*s, dtype=torch.bfloat16) * 3.2)(D, Hq * Dh) * (D ** -0.5)),
            "wo": _w((lambda *s: torch.randn(*s, dtype=torch.bfloat16) * 1.6)(Hq * Dh, D) * ((Hq * Dh) ** -0.5)),
        }
    _B_in = _B_setup()

    return {
        "A_x": _A_in["x"],
        "A_k_cache": _A_in["k_cache"],
        "A_v_cache": _A_in["v_cache"],
        "A_wq": _A_in["wq"],
        "A_wo": _A_in["wo"],
        "B_x": _B_in["x"],
        "B_k_cache": _B_in["k_cache"],
        "B_v_cache": _B_in["v_cache"],
        "B_wq": _B_in["wq"],
        "B_wo": _B_in["wo"],
    }

def reference(A_x, A_k_cache, A_v_cache, A_wq, A_wo, B_x, B_k_cache, B_v_cache, B_wq, B_wo):
    def _A_ref(x, k_cache, v_cache, wq, wo):
        B, Sq, D = x.shape
        Hq, Dh = 32, 128
        Hkv = k_cache.shape[1]
        rep = Hq // Hkv
        q = (lambda x, *d: x.permute(*d))((x @ wq).reshape(B, Sq, Hq, Dh), 0, 2, 1, 3)
        k = (lambda x, n, axis: torch.repeat_interleave(x, n, dim=axis))(k_cache, rep, axis=1)
        v = (lambda x, n, axis: torch.repeat_interleave(x, n, dim=axis))(v_cache, rep, axis=1)
        scores = torch.einsum("bhqd,bhkd->bhqk", q, k) * (Dh ** -0.5)
        p = _softmax(scores, axis=-1)
        o = torch.einsum("bhqk,bhkd->bhqd", p, v)
        return (lambda x, *d: x.permute(*d))(o, 0, 2, 1, 3).reshape(B, Sq, Hq * Dh) @ wo

    def _B_ref(x, k_cache, v_cache, wq, wo):
        B, Sq, D = x.shape
        Hq, Dh = 32, 128
        Hkv, Sk = k_cache.shape[1], k_cache.shape[2]
        W = min(4096, max(256, Sk // 4))
        rep = Hq // Hkv
        q = (lambda x, *d: x.permute(*d))((x @ wq).reshape(B, Sq, Hq, Dh), 0, 2, 1, 3)
        k = (lambda x, n, axis: torch.repeat_interleave(x, n, dim=axis))(k_cache[:, :, Sk - W:, :], rep, axis=1)
        v = (lambda x, n, axis: torch.repeat_interleave(x, n, dim=axis))(v_cache[:, :, Sk - W:, :], rep, axis=1)
        scores = torch.einsum("bhqd,bhkd->bhqk", q, k) * (Dh ** -0.5)
        p = _softmax(scores, axis=-1)
        o = torch.einsum("bhqk,bhkd->bhqd", p, v)
        return (lambda x, *d: x.permute(*d))(o, 0, 2, 1, 3).reshape(B, Sq, Hq * Dh) @ wo

    a = _A_ref(A_x, A_k_cache, A_v_cache, A_wq, A_wo)
    b = _B_ref(B_x, B_k_cache, B_v_cache, B_wq, B_wo)
    return torch.concatenate([a, b], axis=-1)
```

```cpp
#include <hip/hip_runtime.h>
#include <hip/hip_bf16.h>
#include <stdint.h>

#define GAS __attribute__((address_space(1)))
#define LAS __attribute__((address_space(3)))
typedef unsigned u32x4 __attribute__((ext_vector_type(4)));
typedef unsigned u32x2 __attribute__((ext_vector_type(2)));
typedef float f32x4 __attribute__((ext_vector_type(4)));
typedef float f32x2 __attribute__((ext_vector_type(2)));
typedef __bf16 bf16x8 __attribute__((ext_vector_type(8)));
typedef short s16x4 __attribute__((ext_vector_type(4)));

constexpr int D = 512, HQ = 32, HKV = 8, SK = 65536, DH = 128, WIN = 4096;
constexpr int NWG = 256, NTHR = 512;
constexpr float LOG2E = 1.4426950408889634f;
constexpr float SCALE = 0.08838834764831845f;

constexpr size_t WS_Q = 0;
constexpr size_t WS_PART = 65536;
constexpr int PREC = 4 * 132;
constexpr size_t WS_Y = WS_PART + (size_t)NWG * 2 * PREC * 4;

__device__ __forceinline__ float bf2f(unsigned short b) { return __builtin_bit_cast(float, (unsigned)b << 16); }
typedef __bf16 bf16x2 __attribute__((ext_vector_type(2)));
__device__ __forceinline__ unsigned cvtpk(float lo, float hi) { f32x2 f = {lo, hi}; return __builtin_bit_cast(unsigned, __builtin_convertvector(f, bf16x2)); }
__device__ __forceinline__ float rbf(float x) { return __builtin_bit_cast(float, cvtpk(x, x) << 16); }
__device__ __forceinline__ float lo16(unsigned u) { return __builtin_bit_cast(float, u << 16); }
__device__ __forceinline__ float hi16(unsigned u) { return __builtin_bit_cast(float, u & 0xffff0000u); }

__global__ void __launch_bounds__(NTHR, 2) k_qproj(const unsigned short* xA, const unsigned short* wqA, const unsigned short* xB, const unsigned short* wqB, unsigned char* ws) {
  __shared__ float red[8][32];
  const int b = blockIdx.x, h = b & 7, j = b >> 3, br = j >> 4;
  const int tid = threadIdx.x, lane = tid & 63, w = tid >> 6;
  const unsigned short* x = br ? xB : xA;
  const unsigned short* wq = br ? wqB : wqA;
  const int col0 = h * 512 + 32 * (j & 15);
  const int c4 = lane & 3, rr = lane >> 2;
  float acc[8];
#pragma unroll
  for (int k = 0; k < 8; ++k) acc[k] = 0.f;
#pragma unroll
  for (int i = 0; i < 4; ++i) {
    const int row = w * 64 + i * 16 + rr;
    const u32x4 v = *(const u32x4*)(wq + (size_t)row * 4096 + col0 + 8 * c4);
    const float xv = bf2f(x[row]);
#pragma unroll
    for (int k = 0; k < 4; ++k) { acc[2 * k] = fmaf(xv, lo16(v[k]), acc[2 * k]); acc[2 * k + 1] = fmaf(xv, hi16(v[k]), acc[2 * k + 1]); }
  }
#pragma unroll
  for (int k = 0; k < 8; ++k) {
#pragma unroll
    for (int m = 4; m < 64; m <<= 1) acc[k] += __shfl_xor(acc[k], m);
  }
  if (lane < 4) {
#pragma unroll
    for (int k = 0; k < 8; ++k) red[w][8 * lane + k] = acc[k];
  }
  __syncthreads();
  if (tid < 32) {
    float s = 0.f;
#pragma unroll
    for (int ww = 0; ww < 8; ++ww) s += red[ww][tid];
    ((unsigned short*)(ws + WS_Q))[br * 4096 + col0 + tid] = (unsigned short)(cvtpk(s, s) & 0xffffu);
  }
}

struct St { float m, l; f32x4 a0, a1; };

template <bool MIMIC>
__device__ __forceinline__ void tile(St& st, const u32x4 (&K)[4], const u32x4 (&V)[4], const u32x4 (&q)[4], unsigned mask0, unsigned mask1, int lane) {
  f32x4 s = {0.f, 0.f, 0.f, 0.f};
#pragma unroll
  for (int i = 0; i < 4; ++i) s = __builtin_amdgcn_mfma_f32_16x16x32_bf16(__builtin_bit_cast(bf16x8, K[i]), __builtin_bit_cast(bf16x8, q[i]), s, 0, 0, 0);
  float t[4];
#pragma unroll
  for (int r = 0; r < 4; ++r) {
    if (MIMIC) t[r] = rbf(rbf(s[r]) * SCALE) * LOG2E; else t[r] = s[r] * (SCALE * LOG2E);
  }
  float mx = fmaxf(fmaxf(t[0], t[1]), fmaxf(t[2], t[3]));
  mx = fmaxf(mx, __shfl_xor(mx, 16));
  mx = fmaxf(mx, __shfl_xor(mx, 32));
  const float mn = fmaxf(st.m, mx);
  const float alpha = __builtin_amdgcn_exp2f(st.m - mn);
  st.m = mn;
  float p[4];
#pragma unroll
  for (int r = 0; r < 4; ++r) p[r] = __builtin_amdgcn_exp2f(t[r] - mn);
  st.l = st.l * alpha + ((p[0] + p[1]) + (p[2] + p[3]));
  const float al = __shfl(alpha, (lane >> 4) * 4);
  st.a0 *= al; st.a1 *= al;
#pragma unroll
  for (int r = 0; r < 4; ++r) {
    const unsigned x = cvtpk(p[r], p[r]);
    u32x2 a = {x & mask0, x & mask1};
    u32x2 v0 = {V[r][0], V[r][1]}, v1 = {V[r][2], V[r][3]};
    st.a0 = __builtin_amdgcn_mfma_f32_16x16x16bf16_1k(__builtin_bit_cast(s16x4, a), __builtin_bit_cast(s16x4, v0), st.a0, 0, 0, 0);
    st.a1 = __builtin_amdgcn_mfma_f32_16x16x16bf16_1k(__builtin_bit_cast(s16x4, a), __builtin_bit_cast(s16x4, v1), st.a1, 0, 0, 0);
  }
}

template <bool MIMIC>
__global__ void __launch_bounds__(NTHR, 2) k_attn(const unsigned short* kA, const unsigned short* vA, const unsigned short* kB, const unsigned short* vB, unsigned char* ws) {
  __shared__ float mrg[8][4][132];
  const int b = blockIdx.x, h = b & 7, j = b >> 3;
  const int tid = threadIdx.x, lane = tid & 63, w = __builtin_amdgcn_readfirstlane(tid >> 6);
  const int hh = lane >> 4, i16 = lane & 15, g = i16 >> 2, jj = lane & 3;
  const unsigned mask0 = jj == 0 ? 0x0000ffffu : jj == 1 ? 0xffff0000u : 0u;
  const unsigned mask1 = jj == 2 ? 0x0000ffffu : jj == 3 ? 0xffff0000u : 0u;
  const unsigned short* qb = (const unsigned short*)(ws + WS_Q);
  float* part = (float*)(ws + WS_PART);
  for (int br = 0; br < 2; ++br) {
    u32x4 q[4];
#pragma unroll
    for (int s = 0; s < 4; ++s) q[s] = *(const u32x4*)(qb + br * 4096 + (h * 4 + g) * 128 + 32 * s + 8 * hh);
    const unsigned short* kp = (br ? kB : kA) + (size_t)h * SK * DH;
    const unsigned short* vp = (br ? vB : vA) + (size_t)h * SK * DH;
    const int nt = br ? 1 : 16;
    const int key0 = br ? (SK - WIN + j * 128) : j * 2048;
    St st; st.m = -INFINITY; st.l = 0.f; st.a0 = f32x4{0, 0, 0, 0}; st.a1 = f32x4{0, 0, 0, 0};
    for (int it = 0; it < nt; ++it) {
      const int t0 = key0 + (it * 8 + w) * 16;
      u32x4 K[4], V[4];
#pragma unroll
      for (int s = 0; s < 4; ++s) K[s] = __builtin_nontemporal_load((const u32x4*)(kp + (size_t)(t0 + i16) * DH + 32 * s + 8 * hh));
#pragma unroll
      for (int m = 0; m < 4; ++m) V[m] = __builtin_nontemporal_load((const u32x4*)(vp + (size_t)(t0 + 4 * hh + m) * DH + 8 * i16));
      tile<MIMIC>(st, K, V, q, mask0, mask1, lane);
    }
    float l = st.l; l += __shfl_xor(l, 16); l += __shfl_xor(l, 32);
    __syncthreads();
#pragma unroll
    for (int r = 0; r < 4; ++r) { mrg[w][hh][4 + 8 * i16 + r] = st.a0[r]; mrg[w][hh][4 + 8 * i16 + 4 + r] = st.a1[r]; }
    if (hh == 0 && jj == 0) { mrg[w][g][0] = st.m; mrg[w][g][1] = l; }
    __syncthreads();
    {
      const int gg = tid >> 7, d = tid & 127;
      float M = -INFINITY;
#pragma unroll
      for (int ww = 0; ww < 8; ++ww) M = fmaxf(M, mrg[ww][gg][0]);
      float L = 0.f, o = 0.f;
#pragma unroll
      for (int ww = 0; ww < 8; ++ww) { const float wt = __builtin_amdgcn_exp2f(mrg[ww][gg][0] - M); L = fmaf(wt, mrg[ww][gg][1], L); o = fmaf(wt, mrg[ww][gg][4 + d], o); }
      float* rec = part + (size_t)(b * 2 + br) * PREC + gg * 132;
      rec[4 + d] = o;
      if (d == 0) { rec[0] = M; rec[1] = L; }
    }
  }
}

__global__ void __launch_bounds__(NTHR, 2) k_comb(const unsigned short* woA, const unsigned short* woB, unsigned char* ws) {
  __shared__ float osh[2][16];
  __shared__ float red[8][512];
  const int b = blockIdx.x, h = b & 7, j = b >> 3;
  const int tid = threadIdx.x, lane = tid & 63, w = tid >> 6;
  const float* part = (const float*)(ws + WS_PART);
  const int g = j >> 3, d0 = 16 * (j & 7);
  if (tid < 32) {
    const int br = tid >> 4, d = d0 + (tid & 15);
    float M = -INFINITY;
    for (int p = 0; p < 32; ++p) M = fmaxf(M, part[(size_t)((p * 8 + h) * 2 + br) * PREC + g * 132]);
    float L = 0.f, o = 0.f;
    for (int p = 0; p < 32; ++p) { const float* rec = part + (size_t)((p * 8 + h) * 2 + br) * PREC + g * 132; const float wt = __builtin_amdgcn_exp2f(rec[0] - M); L = fmaf(wt, rec[1], L); o = fmaf(wt, rec[4 + d], o); }
    osh[br][tid & 15] = rbf(o / L);
  }
  __syncthreads();
  const int br = w >> 2, kq = w & 3;
  const unsigned short* wo = (br ? woB : woA) + (size_t)(h * 512 + 16 * j + 4 * kq) * 512;
  float y[8];
#pragma unroll
  for (int k = 0; k < 8; ++k) y[k] = 0.f;
#pragma unroll
  for (int r = 0; r < 4; ++r) {
    const u32x4 v = *(const u32x4*)(wo + r * 512 + 8 * lane);
    const float o = osh[br][4 * kq + r];
#pragma unroll
    for (int k = 0; k < 4; ++k) { y[2 * k] = fmaf(o, lo16(v[k]), y[2 * k]); y[2 * k + 1] = fmaf(o, hi16(v[k]), y[2 * k + 1]); }
  }
#pragma unroll
  for (int k = 0; k < 8; ++k) red[w][8 * lane + k] = y[k];
  __syncthreads();
  float* Y = (float*)(ws + WS_Y) + (size_t)b * 1024;
  for (int u = tid; u < 1024; u += NTHR) { const int bb = u >> 9, c = u & 511; Y[u] = (red[bb * 4][c] + red[bb * 4 + 1][c]) + (red[bb * 4 + 2][c] + red[bb * 4 + 3][c]); }
}

__global__ void __launch_bounds__(256) k_red(const unsigned char* ws, unsigned short* out) {
  __shared__ float red[8][32];
  const int c = blockIdx.x * 32 + (threadIdx.x & 31), pg = threadIdx.x >> 5;
  const float* Y = (const float*)(ws + WS_Y);
  float s = 0.f;
  for (int p = pg; p < 256; p += 8) s += Y[(size_t)p * 1024 + c];
  red[pg][threadIdx.x & 31] = s;
  __syncthreads();
  if (threadIdx.x < 32) { float t = 0.f; for (int k = 0; k < 8; ++k) t += red[k][threadIdx.x]; out[c] = (unsigned short)(cvtpk(t, t) & 0xffffu); }
}

#define MIMIC_ROUND 0
extern "C" void kernel_launch(void* const* d_in, const int* in_sizes, int n_in, void* d_out, int out_size, void* d_ws, size_t ws_size, hipStream_t stream) {
  const unsigned short* xA = (const unsigned short*)d_in[0]; const unsigned short* kA = (const unsigned short*)d_in[1]; const unsigned short* vA = (const unsigned short*)d_in[2];
  const unsigned short* wqA = (const unsigned short*)d_in[3]; const unsigned short* woA = (const unsigned short*)d_in[4];
  const unsigned short* xB = (const unsigned short*)d_in[5]; const unsigned short* kB = (const unsigned short*)d_in[6]; const unsigned short* vB = (const unsigned short*)d_in[7];
  const unsigned short* wqB = (const unsigned short*)d_in[8]; const unsigned short* woB = (const unsigned short*)d_in[9];
  unsigned char* ws = (unsigned char*)d_ws;
  k_qproj<<<NWG, NTHR, 0, stream>>>(xA, wqA, xB, wqB, ws);
  k_attn<MIMIC_ROUND != 0><<<NWG, NTHR, 0, stream>>>(kA, vA, kB, vB, ws);
  k_comb<<<NWG, NTHR, 0, stream>>>(woA, woB, ws);
  k_red<<<32, 256, 0, stream>>>(ws, (unsigned short*)d_out);
}
```

```cpp
#include <hip/hip_runtime.h>
#include <hip/hip_bf16.h>
#include <stdint.h>

#define GAS __attribute__((address_space(1)))
#define LAS __attribute__((address_space(3)))
typedef unsigned u32x4 __attribute__((ext_vector_type(4)));
typedef unsigned u32x2 __attribute__((ext_vector_type(2)));
typedef float f32x4 __attribute__((ext_vector_type(4)));
typedef float f32x2 __attribute__((ext_vector_type(2)));
typedef __bf16 bf16x8 __attribute__((ext_vector_type(8)));
typedef __bf16 bf16x2 __attribute__((ext_vector_type(2)));
typedef short s16x4 __attribute__((ext_vector_type(4)));

#define NS 18
#define INFL 4
#define PRE 3
#define PRETOUCH 0
#define QEARLY 2
#define QSLEEP 16
#define LDAUX 16
#define WSEND 200
#define STAUX 16
#define PSLEEP 0
#define BENCH 0
#define MAP 0
#define STAMPS 0

constexpr int SK = 65536, DH = 128, WIN = 4096;
constexpr int NWG = 256, NC = 8, NTHR = (NC + 1) * 64;
constexpr int SLOT = 8192, NSEQ = 4 + 136 + 4;
constexpr float LOG2E = 1.4426950408889634f;
constexpr float SCALE = 0.08838834764831845f;
constexpr unsigned SPIN_CAP = 1u << 20, LSPIN_CAP = 1u << 24;

constexpr unsigned WS_GEN = 0;
constexpr unsigned WS_QG = 4096;
constexpr unsigned WS_ML = WS_QG + 8 * 32 * 32 * 16;
constexpr unsigned WS_I1 = WS_ML + 256 * 128;
constexpr unsigned WS_I2 = WS_I1 + 8 * 32 * 32 * 256;
constexpr unsigned WS_END = WS_I2 + 64 * 128 * 128;
constexpr unsigned WS_STAMP = WS_END;

constexpr int L_RING = 0;
constexpr int L_FULL = NS * SLOT;
constexpr int L_FREE = L_FULL + 128;
constexpr int L_CNT = L_FREE + 128;
constexpr int L_RED = L_CNT + 64;
constexpr int L_Q = L_RED + 2048;
constexpr int L_H1 = L_Q + 2048;
constexpr int L_F = L_H1 + 8 * 36 * 4;
constexpr int L_END = L_F + 512 + 128;
constexpr int MREC = 132;
constexpr int L_MRG = 0;
constexpr int MRG_SLOTS = 5;
constexpr int L_Y = 5 * SLOT;
static_assert(L_END <= 160 * 1024, "LDS");
static_assert(2 * 8 * 4 * MREC * 4 <= MRG_SLOTS * SLOT, "merge area");
static_assert((140 % NS) >= 7 && (143 % NS) >= 7 && (140 % NS) <= (143 % NS), "wo slots clear of the merge / y areas");
static_assert((132 % NS) >= MRG_SLOTS && (139 % NS) >= MRG_SLOTS && (132 % NS) <= (139 % NS), "last round clear of the merge area");

__device__ __forceinline__ unsigned cvtpk(float lo, float hi) { f32x2 f = {lo, hi}; return __builtin_bit_cast(unsigned, __builtin_convertvector(f, bf16x2)); }
__device__ __forceinline__ float lo16(unsigned u) { return __builtin_bit_cast(float, u << 16); }
__device__ __forceinline__ float hi16(unsigned u) { return __builtin_bit_cast(float, u & 0xffff0000u); }
__device__ __forceinline__ float u2f(unsigned u) { return __builtin_bit_cast(float, u); }
__device__ __forceinline__ unsigned f2u(float f) { return __builtin_bit_cast(unsigned, f); }
template <int CTRL> __device__ __forceinline__ float dpp(float x) { return u2f(__builtin_amdgcn_mov_dpp(f2u(x), CTRL, 0xf, 0xf, true)); }
constexpr int XOR1 = 0xB1, XOR2 = 0x4E, HMIR = 0x141;
constexpr int ROR1 = 0x121, ROR2 = 0x122, ROR4 = 0x124, ROR8 = 0x128;
__device__ __forceinline__ float swap16_max(float x) { auto s = __builtin_amdgcn_permlane16_swap(f2u(x), f2u(x), false, false); return fmaxf(u2f(s[0]), u2f(s[1])); }
__device__ __forceinline__ float swap32_max(float x) { auto s = __builtin_amdgcn_permlane32_swap(f2u(x), f2u(x), false, false); return fmaxf(u2f(s[0]), u2f(s[1])); }
__device__ __forceinline__ float swap16_sum(float x) { auto s = __builtin_amdgcn_permlane16_swap(f2u(x), f2u(x), false, false); return u2f(s[0]) + u2f(s[1]); }
__device__ __forceinline__ float swap32_sum(float x) { auto s = __builtin_amdgcn_permlane32_swap(f2u(x), f2u(x), false, false); return u2f(s[0]) + u2f(s[1]); }
__device__ __forceinline__ float row_sum(float x) { x += dpp<ROR8>(x); x += dpp<ROR4>(x); x += dpp<ROR2>(x); x += dpp<ROR1>(x); return x; }
__device__ __forceinline__ float row_max(float x) { x = fmaxf(x, dpp<ROR8>(x)); x = fmaxf(x, dpp<ROR4>(x)); x = fmaxf(x, dpp<ROR2>(x)); x = fmaxf(x, dpp<ROR1>(x)); return x; }
#define LDS_BARRIER() asm volatile("s_waitcnt lgkmcnt(0)\n\ts_barrier" ::: "memory")
#define LDS_WAIT() asm volatile("s_waitcnt lgkmcnt(0)" ::: "memory")
#define MEMFENCE() asm volatile("" ::: "memory")
constexpr int AUX_SC1 = 16;

struct Args {
  const unsigned char *xA, *kA, *vA, *wqA, *woA, *xB, *kB, *vB, *wqB, *woB;
  unsigned short* out;
  unsigned char* ws;
};

struct St { float m, l; f32x4 a0, a1; };

__device__ __forceinline__ unsigned lds_ld(LAS unsigned char* L, int off) { return __builtin_amdgcn_readfirstlane(*(volatile LAS unsigned*)(L + off)); }
__device__ __forceinline__ void lds_st(LAS unsigned char* L, int off, unsigned v) { *(volatile LAS unsigned*)(L + off) = v; }
template <int SLEEP> __device__ __forceinline__ void lds_wait_ge(LAS unsigned char* L, int off, unsigned need) {
  for (unsigned spin = 0; lds_ld(L, off) < need && spin < LSPIN_CAP; ++spin) { if (SLEEP) __builtin_amdgcn_s_sleep(SLEEP); }
  MEMFENCE();
}
__device__ __forceinline__ void lds_arrive(LAS unsigned char* L, int off, int lane) {
  LDS_WAIT();
  if (lane == 0) __hip_atomic_fetch_add((LAS unsigned*)(L + off), 1u, __ATOMIC_RELAXED, __HIP_MEMORY_SCOPE_WORKGROUP);
}

__device__ __forceinline__ void dma1(unsigned dst, unsigned voff, const unsigned char* base) {
  unsigned keep;
  asm volatile("s_mov_b32 %0, m0\n\ts_mov_b32 m0, %1\n\ts_nop 0\n\tglobal_load_lds_dwordx4 %2, %3 nt\n\ts_mov_b32 m0, %0"
               : "=&s"(keep) : "s"(dst), "v"(voff), "s"(base) : "memory");
}
__device__ __forceinline__ void dma4(unsigned dst, unsigned v0, unsigned v1, unsigned v2, unsigned v3, const unsigned char* base) {
  unsigned keep;
  asm volatile("s_mov_b32 %0, m0\n\ts_mov_b32 m0, %1\n\ts_nop 0\n\t"
               "global_load_lds_dwordx4 %2, %6 nt\n\t"
               "global_load_lds_dwordx4 %3, %6 offset:1024 nt\n\t"
               "global_load_lds_dwordx4 %4, %6 offset:2048 nt\n\t"
               "global_load_lds_dwordx4 %5, %6 offset:3072 nt\n\t"
               "s_mov_b32 m0, %0"
               : "=&s"(keep) : "s"(dst), "v"(v0), "v"(v1), "v"(v2), "v"(v3), "s"(base) : "memory");
}
template <int N> __device__ __forceinline__ void vm_wait() { asm volatile("s_waitcnt vmcnt(%0)" :: "i"(N) : "memory"); }

__device__ __forceinline__ void tile_qk(St& st, const u32x4 (&K)[4], const u32x4 (&q)[4], float (&p)[4]) {
  f32x4 s = {0.f, 0.f, 0.f, 0.f};
#pragma unroll
  for (int i = 0; i < 4; ++i) s = __builtin_amdgcn_mfma_f32_16x16x32_bf16(__builtin_bit_cast(bf16x8, K[i]), __builtin_bit_cast(bf16x8, q[i]), s, 0, 0, 0);
  float t[4];
  {
    const unsigned r01 = cvtpk(s[0], s[1]), r23 = cvtpk(s[2], s[3]);
    const unsigned u01 = cvtpk(lo16(r01) * SCALE, hi16(r01) * SCALE), u23 = cvtpk(lo16(r23) * SCALE, hi16(r23) * SCALE);
    t[0] = lo16(u01) * LOG2E; t[1] = hi16(u01) * LOG2E; t[2] = lo16(u23) * LOG2E; t[3] = hi16(u23) * LOG2E;
  }
  float mx = fmaxf(fmaxf(t[0], t[1]), fmaxf(t[2], t[3]));
  mx = swap16_max(mx);
  mx = swap32_max(mx);
  const float mn = fmaxf(st.m, mx);
  const float alpha = __builtin_amdgcn_exp2f(st.m - mn);
  st.m = mn;
#pragma unroll
  for (int r = 0; r < 4; ++r) p[r] = __builtin_amdgcn_exp2f(t[r] - mn);
  st.l = st.l * alpha + ((p[0] + p[1]) + (p[2] + p[3]));
  unsigned al = __builtin_amdgcn_update_dpp(0u, f2u(alpha), 0x150, 0x1, 0xf, false);
  al = __builtin_amdgcn_update_dpp(al, f2u(alpha), 0x154, 0x2, 0xf, false);
  al = __builtin_amdgcn_update_dpp(al, f2u(alpha), 0x158, 0x4, 0xf, false);
  al = __builtin_amdgcn_update_dpp(al, f2u(alpha), 0x15c, 0x8, 0xf, false);
  st.a0 *= u2f(al); st.a1 *= u2f(al);
}
__device__ __forceinline__ void tile_pv(St& st, const u32x4 (&V)[4], const float (&p)[4], unsigned mask0, unsigned mask1) {
#pragma unroll
  for (int r = 0; r < 4; ++r) {
    const unsigned x = cvtpk(p[r], p[r]);
    const u32x2 a = {x & mask0, x & mask1};
    const u32x2 v0 = {V[r][0], V[r][1]}, v1 = {V[r][2], V[r][3]};
    st.a0 = __builtin_amdgcn_mfma_f32_16x16x16bf16_1k(__builtin_bit_cast(s16x4, a), __builtin_bit_cast(s16x4, v0), st.a0, 0, 0, 0);
    st.a1 = __builtin_amdgcn_mfma_f32_16x16x16bf16_1k(__builtin_bit_cast(s16x4, a), __builtin_bit_cast(s16x4, v1), st.a1, 0, 0, 0);
  }
}

#define STAMP(i) do {} while (0)
#define LSTAMP(i) do {} while (0)
#define WSTAMP(i) do {} while (0)

__global__ void __launch_bounds__(NTHR, 3) fused_decode(Args a) {
  __shared__ __attribute__((aligned(16))) unsigned char lds_raw[L_END];
  LAS unsigned char* lds = (LAS unsigned char*)lds_raw;
  const int b = blockIdx.x, h = b & 7, j = b >> 3;
  const int tid = threadIdx.x, lane = tid & 63, w = __builtin_amdgcn_readfirstlane(tid >> 6);
  const int hh = lane >> 4, i16 = lane & 15, jj = lane & 3;
  const int qcb = j >> 1, qrh = j & 1, qbr = qcb >> 3;

  if (w == NC) {
    const unsigned ring = (unsigned)(size_t)(lds + L_RING);
    int pub = 0, pslot = 0, slot = 0;
    bool parked = false; (void)parked;
    auto publish1 = [&]() { lds_st(lds, L_FULL + 4 * pslot, (unsigned)(pub + 1)); ++pub; pslot = pslot + 1 == NS ? 0 : pslot + 1; };
    auto pre = [&](int q) {
      if (q >= NS) {
        const unsigned need = (unsigned)(q - NS + 1);
        if (lds_ld(lds, L_FREE + 4 * slot) < need) {
          vm_wait<0>();
          while (pub < q) publish1();
          lds_wait_ge<1>(lds, L_FREE + 4 * slot, need);
        }
      }
    };
    auto post = [&](int q) {
      if (q + 1 - pub > INFL) { vm_wait<8 * INFL>(); publish1(); }
      slot = slot + 1 == NS ? 0 : slot + 1;
    };
    {
      const unsigned char* wq = (qbr ? a.wqB : a.wqA) + (size_t)(h * 512 + 64 * (qcb & 7)) * 2 + (size_t)qrh * 256 * 8192;
      const unsigned vo = (lane >> 3) * 8192 + (lane & 7) * 16;
#pragma unroll
      for (int q = 0; q < 4; ++q) {
#pragma unroll
        for (int m = 0; m < 8; ++m) dma1(ring + q * SLOT + m * 1024, vo, wq + (size_t)(q * 64 + m * 8) * 8192);
        if (q == 0) LDS_BARRIER();
        post(q);
      }
    }
    const unsigned k0 = i16 * 256 + 16 * hh + 3072, k1 = k0 + 64 - 1024, k2 = k0 + 128 - 2048, k3 = k0 + 192 - 3072;
    const unsigned v0 = hh * 1024 + 16 * i16 + 3072, v1 = v0 + 256 - 1024, v2 = v0 + 512 - 2048, v3 = v0 + 768 - 3072;
    const unsigned vl = lane * 16;
    {
      const unsigned char* kp = a.kB + ((size_t)h * SK + (SK - WIN) + j * 128) * 256 - 3072;
      const unsigned char* vp = a.vB + ((size_t)h * SK + (SK - WIN) + j * 128) * 256 - 3072;
      for (int q = 4; q < 140; ++q) {
        if (q == 12) { kp = a.kA + ((size_t)h * SK + j * 2048) * 256 - 3072; vp = a.vA + ((size_t)h * SK + j * 2048) * 256 - 3072; }
        pre(q);
        dma4(ring + slot * SLOT, k0, k1, k2, k3, kp);
        dma4(ring + slot * SLOT + 4096, v0, v1, v2, v3, vp);
        kp += 4096; vp += 4096;
        post(q);
      }
    }
    LSTAMP(0);
#pragma unroll
    for (int q = 140; q < 144; ++q) {
      const unsigned char* wo = (j >> 4 ? a.woB : a.woA) + (size_t)(h * 512 + 32 * (j & 15) + 8 * (q - 140)) * 1024;
      pre(q);
      dma4(ring + slot * SLOT, vl, vl, vl, vl, wo);
      dma4(ring + slot * SLOT + 4096, vl, vl, vl, vl, wo + 4096);
      post(q);
    }
    if (NSEQ - pub > 6) { vm_wait<48>(); publish1(); }
    if (NSEQ - pub > 5) { vm_wait<40>(); publish1(); }
    if (NSEQ - pub > 4) { vm_wait<32>(); publish1(); }
    if (NSEQ - pub > 3) { vm_wait<24>(); publish1(); }
    if (NSEQ - pub > 2) { vm_wait<16>(); publish1(); }
    if (NSEQ - pub > 1) { vm_wait<8>(); publish1(); }
    vm_wait<0>();
    while (pub < NSEQ) publish1();
    LSTAMP(1);
    return;
  }

  const __amdgpu_buffer_rsrc_t rs = __builtin_amdgcn_make_buffer_rsrc((void*)a.ws, 0, (int)WS_END, 0x00020000);
  const u32x2 gen = __builtin_amdgcn_raw_buffer_load_b64(rs, WS_GEN, 0, AUX_SC1);
  unsigned short xv[4];
  {
    const GAS unsigned char* xp = (const GAS unsigned char*)(qbr ? a.xB : a.xA) + 2 * (qrh * 256 + w * 32 + (lane >> 3));
#pragma unroll
    for (int i = 0; i < 4; ++i) xv[i] = *(const GAS unsigned short*)(xp + 16 * i);
  }
  if (tid < (L_RED - L_FULL) / 4) *(LAS unsigned*)(lds + L_FULL + 4 * tid) = 0u;
  LDS_BARRIER();

  lds_wait_ge<1>(lds, L_FULL + 4 * (w >> 1), (unsigned)((w >> 1) + 1));
  {
    u32x4 wv[4];
#pragma unroll
    for (int i = 0; i < 4; ++i) wv[i] = *(const LAS u32x4*)(lds + L_RING + (w >> 1) * SLOT + (w & 1) * 4096 + i * 1024 + lane * 16);
    float acc[8];
#pragma unroll
    for (int k = 0; k < 8; ++k) acc[k] = 0.f;
#pragma unroll
    for (int i = 0; i < 4; ++i) {
      const float x = lo16(xv[i]);
#pragma unroll
      for (int k = 0; k < 4; ++k) { acc[2 * k] = fmaf(x, lo16(wv[i][k]), acc[2 * k]); acc[2 * k + 1] = fmaf(x, hi16(wv[i][k]), acc[2 * k + 1]); }
    }
#pragma unroll
    for (int k = 0; k < 8; ++k) { float v = acc[k]; v += dpp<ROR8>(v); v = swap16_sum(v); v = swap32_sum(v); acc[k] = v; }
    if (lane < 8) {
      LAS f32x4* red = (LAS f32x4*)(lds + L_RED + w * 256 + lane * 32);
      red[0] = f32x4{acc[0], acc[1], acc[2], acc[3]}; red[1] = f32x4{acc[4], acc[5], acc[6], acc[7]};
    }
  }
  lds_arrive(lds, L_CNT + 0, lane);
  const unsigned tagA = __builtin_amdgcn_readfirstlane(gen[0]) + 1u, tagB = __builtin_amdgcn_readfirstlane(gen[1]) + 1u;
  if (w == 0) {
    lds_wait_ge<0>(lds, L_CNT + 0, NC);
    if (lane < 4) lds_st(lds, L_FREE + 4 * lane, (unsigned)(lane + 1));
    if (lane < 32) {
      f32x2 s = {0.f, 0.f};
#pragma unroll
      for (int ww = 0; ww < 8; ++ww) s += *(const LAS f32x2*)(lds + L_RED + ww * 256 + lane * 8);
      __builtin_amdgcn_raw_buffer_store_b128(u32x4{tagA, f2u(s[0]), f2u(s[1]), tagB}, rs, WS_QG + ((h * 32 + j) * 32 + lane) * 16, 0, AUX_SC1);
    }
  }
  STAMP(1);
  int slot = (4 + w) % NS;
  auto fetch = [&](int it, u32x4 (&K)[4], u32x4 (&V)[4]) {
    const unsigned seq1 = (unsigned)(4 + 8 * it + w + 1);
    lds_wait_ge<1>(lds, L_FULL + 4 * slot, seq1);
    const LAS unsigned char* sp = lds + L_RING + slot * SLOT + lane * 16;
#pragma unroll
    for (int r = 0; r < 4; ++r) K[r] = *(const LAS u32x4*)(sp + r * 1024);
#pragma unroll
    for (int r = 0; r < 4; ++r) V[r] = *(const LAS u32x4*)(sp + 4096 + r * 1024);
    LDS_WAIT();
    lds_st(lds, L_FREE + 4 * slot, seq1);
    asm volatile("" : "+v"(K[0]), "+v"(K[1]), "+v"(K[2]), "+v"(K[3]), "+v"(V[0]), "+v"(V[1]), "+v"(V[2]), "+v"(V[3]) :: "memory");
    slot += 8; if (slot >= NS) slot -= NS;
  };
  u32x4 K0[4], V0[4], K1[4], V1[4], K2[4], V2[4];
  auto full = [&](int it) { return lds_ld(lds, L_FULL + 4 * slot) >= (unsigned)(4 + 8 * it + w + 1); };
  {
    const unsigned off = WS_QG + ((h * 32 + 2 * (tid >> 5)) * 32 + (tid & 31)) * 16;
    u32x4 g0 = {0u, 0u, 0u, 0u}, g1 = {0u, 0u, 0u, 0u};
    bool ok = false, done = false;
    unsigned spin = 0;
    auto poll_until_full = [&](int it) {
      while (!done && !(it >= 0 && full(it))) {
        MEMFENCE();
        if (!ok) {
          g0 = __builtin_amdgcn_raw_buffer_load_b128(rs, off, 0, AUX_SC1);
          g1 = __builtin_amdgcn_raw_buffer_load_b128(rs, off + 512, 0, AUX_SC1);
          ok = g0[0] == tagA && g0[3] == tagB && g1[0] == tagA && g1[3] == tagB;
        }
        if (__all(ok) || ++spin >= (BENCH == 1 ? 1 : SPIN_CAP)) {
          *(LAS unsigned*)(lds + L_Q + tid * 4) = cvtpk(u2f(g0[1]) + u2f(g1[1]), u2f(g0[2]) + u2f(g1[2]));
          lds_arrive(lds, L_CNT + 4, lane);
          done = true;
        } else if (QSLEEP) __builtin_amdgcn_s_sleep(QSLEEP);
      }
    };
    if (PRE >= 1) fetch(0, K0, V0);
    if (PRE >= 2) { if (QEARLY == 1) poll_until_full(1); fetch(1, K1, V1); }
    if (PRE >= 3) { if (QEARLY < 2) poll_until_full(2); fetch(2, K2, V2); }
    poll_until_full(-1);
  }
  lds_wait_ge<0>(lds, L_CNT + 4, NC);
  u32x4 qA[4], qB[4];
#pragma unroll
  for (int s = 0; s < 4; ++s) {
    qA[s] = *(const LAS u32x4*)(lds + L_Q + ((i16 >> 2) * 128 + 32 * s + 8 * hh) * 2);
    qB[s] = *(const LAS u32x4*)(lds + L_Q + 1024 + ((i16 >> 2) * 128 + 32 * s + 8 * hh) * 2);
  }
  STAMP(2);

  const unsigned mask0 = jj == 0 ? 0x0000ffffu : jj == 1 ? 0xffff0000u : 0u;
  const unsigned mask1 = jj == 2 ? 0x0000ffffu : jj == 3 ? 0xffff0000u : 0u;
  St sA, sB;
  sA.m = -INFINITY; sA.l = 0.f; sA.a0 = f32x4{0, 0, 0, 0}; sA.a1 = f32x4{0, 0, 0, 0};
  sB = sA;
  auto compute = [&](St& st, const u32x4 (&K)[4], const u32x4 (&V)[4], const u32x4 (&q)[4]) {
    float p[4];
    tile_qk(st, K, q, p);
    tile_pv(st, V, p, mask0, mask1);
  };
  if (PRE < 1) fetch(0, K0, V0);
  compute(sB, K0, V0, qB);
  if (PRE < 2) fetch(1, K1, V1);
  compute(sA, K1, V1, qA);
  if (PRE < 3) fetch(2, K2, V2);
  compute(sA, K2, V2, qA);
  for (int it = 3; it < 16; ++it) {
    fetch(it, K0, V0); compute(sA, K0, V0, qA);
  }
  {
    const int s = lane < MRG_SLOTS ? lane : 0;
    const unsigned need = (unsigned)(s + NS * ((139 - s) / NS) + 1);
    for (unsigned spin = 0; spin < LSPIN_CAP; ++spin) { MEMFENCE(); if (__all(*(volatile LAS unsigned*)(lds + L_FREE + 4 * s) >= need)) break; }
    MEMFENCE();
  }
  fetch(16, K0, V0); compute(sA, K0, V0, qA);
  STAMP(3);
  if (w == 7) WSTAMP(11);

  {
    LAS float* mrg = (LAS float*)(lds + L_MRG);
    const float lA = swap32_sum(swap16_sum(sA.l)), lB = swap32_sum(swap16_sum(sB.l));
    LAS float* rA = mrg + (w * 4 + hh) * MREC + 4 + 8 * i16;
    LAS float* rB = rA + 8 * 4 * MREC;
    *(LAS f32x4*)(rA) = sA.a0; *(LAS f32x4*)(rA + 4) = sA.a1;
    *(LAS f32x4*)(rB) = sB.a0; *(LAS f32x4*)(rB + 4) = sB.a1;
    if (hh == 0 && jj == 0) {
      LAS float* mA = mrg + (w * 4 + (i16 >> 2)) * MREC;
      *(LAS f32x2*)mA = f32x2{sA.m, lA};
      *(LAS f32x2*)(mA + 8 * 4 * MREC) = f32x2{sB.m, lB};
    }
  }
  lds_arrive(lds, L_CNT + 8, lane);
  lds_wait_ge<0>(lds, L_CNT + 8, NC);
  {
    const int br = tid >> 8, g = (tid >> 6) & 3, dp = lane;
    const LAS float* mrg = (const LAS float*)(lds + L_MRG) + (br * 8 * 4 + g) * MREC;
    f32x2 ml[8], o2[8];
#pragma unroll
    for (int ww = 0; ww < 8; ++ww) { ml[ww] = *(const LAS f32x2*)(mrg + ww * 4 * MREC); o2[ww] = *(const LAS f32x2*)(mrg + ww * 4 * MREC + 4 + 2 * dp); }
    float M = ml[0][0];
#pragma unroll
    for (int ww = 1; ww < 8; ++ww) M = fmaxf(M, ml[ww][0]);
    float L = 0.f, o0 = 0.f, o1 = 0.f;
#pragma unroll
    for (int ww = 0; ww < 8; ++ww) { const float wt = __builtin_amdgcn_exp2f(ml[ww][0] - M); L = fmaf(wt, ml[ww][1], L); o0 = fmaf(wt, o2[ww][0], o0); o1 = fmaf(wt, o2[ww][1], o1); }
    const int jc = br * 16 + g * 4 + (dp >> 4), k = dp & 15;
    __builtin_amdgcn_raw_buffer_store_b128(u32x4{tagA, f2u(o0), f2u(o1), tagB}, rs, WS_I1 + (((h * 32 + jc) * 32 + j) * 16 + k) * 16, 0, STAUX);
    if (dp == 0) __builtin_amdgcn_raw_buffer_store_b128(u32x4{tagA, f2u(M), f2u(L), tagB}, rs, WS_ML + (((h * 2 + br) * 4 + g) * 32 + j) * 16, 0, STAUX);
  }
  STAMP(4);

  {
    const int p = 4 * w + hh;
    const unsigned offA = WS_I1 + ((h * 32 + j) * 32 * 16 + w * 64 + lane) * 16;
    const unsigned offM = WS_ML + (((h * 2 + (j >> 4)) * 4 + ((j >> 2) & 3)) * 32 + p) * 16;
    u32x4 av = {0u, 0u, 0u, 0u}, mv = {0u, 0u, 0u, 0u};
    bool ok = false;
    for (unsigned spin = 0; spin < SPIN_CAP; ++spin) {
      MEMFENCE();
      if (!ok) {
        av = __builtin_amdgcn_raw_buffer_load_b128(rs, offA, 0, LDAUX);
        mv = __builtin_amdgcn_raw_buffer_load_b128(rs, offM, 0, LDAUX);
        ok = av[0] == tagA && av[3] == tagB && mv[0] == tagA && mv[3] == tagB;
      }
      if (__all(ok)) break;
      if (PSLEEP) __builtin_amdgcn_s_sleep(PSLEEP);
    }
    STAMP(5);
    const float m = u2f(mv[1]);
    const float Mw = swap32_max(swap16_max(m));
    const float wt = __builtin_amdgcn_exp2f(m - Mw);
    const float Lw = swap32_sum(swap16_sum(wt * u2f(mv[2])));
    const float o0 = swap32_sum(swap16_sum(wt * u2f(av[1]))), o1 = swap32_sum(swap16_sum(wt * u2f(av[2])));
    LAS float* rec = (LAS float*)(lds + L_H1) + w * 36;
    if (lane < 16) *(LAS f32x2*)(rec + 4 + 2 * lane) = f32x2{o0, o1};
    if (lane == 0) *(LAS f32x2*)rec = f32x2{Mw, Lw};
  }
  LDS_BARRIER();
  {
    const int ww = lane & 7, r = (lane >> 3) & 3;
    const LAS float* rec = (const LAS float*)(lds + L_H1) + ww * 36;
    const f32x2 ml = *(const LAS f32x2*)rec;
    const float ov = rec[4 + 4 * w + r];
    float M = ml[0];
    M = fmaxf(M, dpp<XOR1>(M)); M = fmaxf(M, dpp<XOR2>(M)); M = fmaxf(M, dpp<HMIR>(M));
    const float wt = __builtin_amdgcn_exp2f(ml[0] - M);
    float L = wt * ml[1], o = wt * ov;
    L += dpp<XOR1>(L); o += dpp<XOR1>(o); L += dpp<XOR2>(L); o += dpp<XOR2>(o); L += dpp<HMIR>(L); o += dpp<HMIR>(o);
    const unsigned ob = cvtpk(o / L, 0.f) << 16;
    const float or0 = u2f(__builtin_amdgcn_readlane(ob, 0)), or1 = u2f(__builtin_amdgcn_readlane(ob, 8)), or2 = u2f(__builtin_amdgcn_readlane(ob, 16)), or3 = u2f(__builtin_amdgcn_readlane(ob, 24));
    const float orow[4] = {or0, or1, or2, or3};
    const int wseq = 140 + (w >> 1);
    lds_wait_ge<0>(lds, L_FULL + 4 * (wseq % NS), (unsigned)(wseq + 1));
    const LAS unsigned char* wp = lds + L_RING + (wseq % NS) * SLOT + (w & 1) * 4096 + lane * 16;
    float y[8];
#pragma unroll
    for (int k = 0; k < 8; ++k) y[k] = 0.f;
#pragma unroll
    for (int r4 = 0; r4 < 4; ++r4) {
      const u32x4 wv = *(const LAS u32x4*)(wp + r4 * 1024);
#pragma unroll
      for (int k = 0; k < 4; ++k) { y[2 * k] = fmaf(orow[r4], lo16(wv[k]), y[2 * k]); y[2 * k + 1] = fmaf(orow[r4], hi16(wv[k]), y[2 * k + 1]); }
    }
    LAS f32x4* yl = (LAS f32x4*)(lds + L_Y + w * 2048 + lane * 32);
    yl[0] = f32x4{y[0], y[1], y[2], y[3]}; yl[1] = f32x4{y[4], y[5], y[6], y[7]};
  }
  LDS_BARRIER();
  if (tid < 256) {
    const LAS unsigned char* yl = lds + L_Y + tid * 8;
    f32x2 s = *(const LAS f32x2*)yl;
#pragma unroll
    for (int k = 1; k < 8; ++k) s += *(const LAS f32x2*)(yl + k * 2048);
    const int c = (j >> 4) * 32 + (tid >> 3), k = tid & 7, pidx = h * 16 + (j & 15);
    __builtin_amdgcn_raw_buffer_store_b128(u32x4{tagA, f2u(s[0]), f2u(s[1]), tagB}, rs, WS_I2 + ((c * 128 + pidx) * 8 + k) * 16, 0, STAUX);
  }
  STAMP(6);

  if (j < 8) {
    const int c = h * 8 + j;
    const unsigned off = WS_I2 + (c * 128 + 16 * w) * 128 + lane * 16;
    u32x4 v[2];
    bool ok = false;
    for (unsigned spin = 0; spin < SPIN_CAP; ++spin) {
      MEMFENCE();
      if (!ok) {
#pragma unroll
        for (int i = 0; i < 2; ++i) v[i] = __builtin_amdgcn_raw_buffer_load_b128(rs, off + i * 1024, 0, LDAUX);
        ok = v[0][0] == tagA && v[0][3] == tagB && v[1][0] == tagA && v[1][3] == tagB;
      }
      if (__all(ok)) break;
      if (PSLEEP) __builtin_amdgcn_s_sleep(PSLEEP);
    }
    STAMP(7);
    float s0 = u2f(v[0][1]) + u2f(v[1][1]);
    float s1 = u2f(v[0][2]) + u2f(v[1][2]);
    s0 += dpp<ROR8>(s0); s1 += dpp<ROR8>(s1);
    s0 = swap32_sum(swap16_sum(s0)); s1 = swap32_sum(swap16_sum(s1));
    if (lane < 8) *(LAS f32x2*)(lds + L_F + w * 64 + lane * 8) = f32x2{s0, s1};
    LDS_BARRIER();
    if (tid < 8) {
      f32x2 s = *(const LAS f32x2*)(lds + L_F + tid * 8);
#pragma unroll
      for (int ww = 1; ww < 8; ++ww) s += *(const LAS f32x2*)(lds + L_F + ww * 64 + tid * 8);
      *(GAS unsigned*)((GAS unsigned char*)a.out + (c * 16 + 2 * tid) * 2) = cvtpk(s[0], s[1]);
      if (c == 0 && tid == 0) __builtin_amdgcn_raw_buffer_store_b64(u32x2{tagA, tagB}, rs, WS_GEN, 0, AUX_SC1);
    }
  }
}

extern "C" void kernel_launch(void* const* d_in, const int* in_sizes, int n_in, void* d_out, int out_size, void* d_ws, size_t ws_size, hipStream_t stream) {
  Args a;
  a.xA = (const unsigned char*)d_in[0]; a.kA = (const unsigned char*)d_in[1]; a.vA = (const unsigned char*)d_in[2]; a.wqA = (const unsigned char*)d_in[3]; a.woA = (const unsigned char*)d_in[4];
  a.xB = (const unsigned char*)d_in[5]; a.kB = (const unsigned char*)d_in[6]; a.vB = (const unsigned char*)d_in[7]; a.wqB = (const unsigned char*)d_in[8]; a.woB = (const unsigned char*)d_in[9];
  a.out = (unsigned short*)d_out;
  a.ws = (unsigned char*)d_ws + (WSEND ? ((ws_size - (size_t)WSEND * (1u << 20)) & ~(size_t)4095) : 0);
  hipLaunchKernelGGL(fused_decode, dim3(NWG), dim3(NTHR), 0, stream, a);
}
```

```cpp
#include <hip/hip_runtime.h>
#include <hip/hip_bf16.h>
#include <stdint.h>

#define GAS __attribute__((address_space(1)))
#define LAS __attribute__((address_space(3)))
typedef unsigned u32x4 __attribute__((ext_vector_type(4)));
typedef unsigned u32x2 __attribute__((ext_vector_type(2)));
typedef float f32x4 __attribute__((ext_vector_type(4)));
typedef float f32x2 __attribute__((ext_vector_type(2)));
typedef __bf16 bf16x8 __attribute__((ext_vector_type(8)));
typedef __bf16 bf16x2 __attribute__((ext_vector_type(2)));
typedef short s16x4 __attribute__((ext_vector_type(4)));

#define NS 18
#define WIN_LD 20
#define PRE 2
#define QSLEEP 16
#define PSLEEP 0
#define BENCH 0
#define STAMPS 0

constexpr int SK = 65536, WIN = 4096;
constexpr int NWG = 256, NC = 8, NTHR = (NC + 1) * 64;
constexpr int SLOT = 8192, NSEQ = 4 + 8 + 64 + 64 + 4;
constexpr int LAG = (WIN_LD + 7) / 8;
constexpr float LOG2E = 1.4426950408889634f;
constexpr float SCALE = 0.08838834764831845f;
constexpr unsigned SPIN_CAP = 1u << 20, LSPIN_CAP = 1u << 24;

constexpr unsigned WS_GEN = 0;
constexpr unsigned WS_QG = 4096;
constexpr unsigned WS_ML = WS_QG + 8 * 32 * 32 * 16;
constexpr unsigned WS_I1 = WS_ML + 256 * 128;
constexpr unsigned WS_I2 = WS_I1 + 8 * 32 * 32 * 256;
constexpr unsigned WS_END = WS_I2 + 64 * 128 * 128;
constexpr unsigned WS_STAMP = WS_END;

constexpr int L_RING = 0;
constexpr int L_FULL = NS * SLOT;
constexpr int L_FREE = L_FULL + 128;
constexpr int L_CNT = L_FREE + 128;
constexpr int L_RED = L_CNT + 64;
constexpr int L_Q = L_RED + 2048;
constexpr int L_T1 = L_Q + 2048;
constexpr int L_T2 = L_T1 + 128;
constexpr int L_TB = L_T2 + 128;
constexpr int L_FF = L_TB + 256;
constexpr int L_H1 = L_FF + 64;
constexpr int L_F = L_H1 + 8 * 32 * 4;
constexpr int L_END = L_F + 512 + 128;
constexpr int C_QRED = 0, C_QIN = 4, C_MAX = 8, C_SUM = 12, C_FF = 16, C_MRG = 20;
constexpr int MREC = 132;
constexpr int L_MRG = 0;
constexpr int MRG_SLOTS = 5;
constexpr int L_Y = 5 * SLOT;
static_assert(L_END <= 160 * 1024, "LDS");
static_assert(2 * 8 * 4 * MREC * 4 <= MRG_SLOTS * SLOT, "merge area");
static_assert((140 % NS) >= 7 && (143 % NS) >= 7 && (140 % NS) <= (143 % NS), "wo slots clear of the merge / y areas");
static_assert((132 % NS) >= MRG_SLOTS && (139 % NS) >= MRG_SLOTS && (132 % NS) <= (139 % NS), "last round clear of the merge area");
static_assert(WIN_LD >= 9 && WIN_LD <= 48, "window");

__device__ __forceinline__ unsigned cvtpk(float lo, float hi) { f32x2 f = {lo, hi}; return __builtin_bit_cast(unsigned, __builtin_convertvector(f, bf16x2)); }
__device__ __forceinline__ float lo16(unsigned u) { return __builtin_bit_cast(float, u << 16); }
__device__ __forceinline__ float hi16(unsigned u) { return __builtin_bit_cast(float, u & 0xffff0000u); }
__device__ __forceinline__ float u2f(unsigned u) { return __builtin_bit_cast(float, u); }
__device__ __forceinline__ unsigned f2u(float f) { return __builtin_bit_cast(unsigned, f); }
template <int CTRL> __device__ __forceinline__ float dpp(float x) { return u2f(__builtin_amdgcn_mov_dpp(f2u(x), CTRL, 0xf, 0xf, true)); }
constexpr int XOR1 = 0xB1, XOR2 = 0x4E, HMIR = 0x141;
constexpr int ROR1 = 0x121, ROR2 = 0x122, ROR4 = 0x124, ROR8 = 0x128;
__device__ __forceinline__ float swap16_max(float x) { auto s = __builtin_amdgcn_permlane16_swap(f2u(x), f2u(x), false, false); return fmaxf(u2f(s[0]), u2f(s[1])); }
__device__ __forceinline__ float swap32_max(float x) { auto s = __builtin_amdgcn_permlane32_swap(f2u(x), f2u(x), false, false); return fmaxf(u2f(s[0]), u2f(s[1])); }
__device__ __forceinline__ float swap16_sum(float x) { auto s = __builtin_amdgcn_permlane16_swap(f2u(x), f2u(x), false, false); return u2f(s[0]) + u2f(s[1]); }
__device__ __forceinline__ float swap32_sum(float x) { auto s = __builtin_amdgcn_permlane32_swap(f2u(x), f2u(x), false, false); return u2f(s[0]) + u2f(s[1]); }
__device__ __forceinline__ float row_sum(float x) { x += dpp<ROR8>(x); x += dpp<ROR4>(x); x += dpp<ROR2>(x); x += dpp<ROR1>(x); return x; }
__device__ __forceinline__ float row_max(float x) { x = fmaxf(x, dpp<ROR8>(x)); x = fmaxf(x, dpp<ROR4>(x)); x = fmaxf(x, dpp<ROR2>(x)); x = fmaxf(x, dpp<ROR1>(x)); return x; }
__device__ __forceinline__ float oct_sum(float x) { x += dpp<XOR1>(x); x += dpp<XOR2>(x); x += dpp<HMIR>(x); return x; }
__device__ __forceinline__ float oct_max(float x) { x = fmaxf(x, dpp<XOR1>(x)); x = fmaxf(x, dpp<XOR2>(x)); x = fmaxf(x, dpp<HMIR>(x)); return x; }
#define LDS_BARRIER() asm volatile("s_waitcnt lgkmcnt(0)\n\ts_barrier" ::: "memory")
#define LDS_WAIT() asm volatile("s_waitcnt lgkmcnt(0)" ::: "memory")
#define MEMFENCE() asm volatile("" ::: "memory")
constexpr int AUX_SC1 = 16;

struct Args {
  const unsigned char *xA, *kA, *vA, *wqA, *woA, *xB, *kB, *vB, *wqB, *woB;
  unsigned short* out;
  unsigned char* ws;
};

__device__ __forceinline__ unsigned lds_ld(LAS unsigned char* L, int off) { return __builtin_amdgcn_readfirstlane(*(volatile LAS unsigned*)(L + off)); }
__device__ __forceinline__ void lds_st(LAS unsigned char* L, int off, unsigned v) { *(volatile LAS unsigned*)(L + off) = v; }
template <int SLEEP> __device__ __forceinline__ void lds_wait_ge(LAS unsigned char* L, int off, unsigned need) {
  for (unsigned spin = 0; lds_ld(L, off) < need && spin < LSPIN_CAP; ++spin) { if (SLEEP) __builtin_amdgcn_s_sleep(SLEEP); }
  MEMFENCE();
}
__device__ __forceinline__ void lds_arrive(LAS unsigned char* L, int off, int lane) {
  LDS_WAIT();
  if (lane == 0) __hip_atomic_fetch_add((LAS unsigned*)(L + off), 1u, __ATOMIC_RELAXED, __HIP_MEMORY_SCOPE_WORKGROUP);
}

__device__ __forceinline__ void dma1(unsigned dst, unsigned voff, const unsigned char* base) {
  unsigned keep;
  asm volatile("s_mov_b32 %0, m0\n\ts_mov_b32 m0, %1\n\ts_nop 0\n\tglobal_load_lds_dwordx4 %2, %3 nt\n\ts_mov_b32 m0, %0\n\ts_waitcnt vmcnt(%4)"
               : "=&s"(keep) : "s"(dst), "v"(voff), "s"(base), "i"(WIN_LD) : "memory");
}
__device__ __forceinline__ void dma4(unsigned dst, unsigned v0, unsigned v1, unsigned v2, unsigned v3, const unsigned char* base) {
  unsigned keep;
  asm volatile("s_mov_b32 %0, m0\n\ts_mov_b32 m0, %1\n\ts_nop 0\n\t"
               "global_load_lds_dwordx4 %2, %6 nt\n\ts_waitcnt vmcnt(%7)\n\t"
               "global_load_lds_dwordx4 %3, %6 offset:1024 nt\n\ts_waitcnt vmcnt(%7)\n\t"
               "global_load_lds_dwordx4 %4, %6 offset:2048 nt\n\ts_waitcnt vmcnt(%7)\n\t"
               "global_load_lds_dwordx4 %5, %6 offset:3072 nt\n\ts_waitcnt vmcnt(%7)\n\t"
               "s_mov_b32 m0, %0"
               : "=&s"(keep) : "s"(dst), "v"(v0), "v"(v1), "v"(v2), "v"(v3), "s"(base), "i"(WIN_LD) : "memory");
}
template <int N> __device__ __forceinline__ void vm_wait() { asm volatile("s_waitcnt vmcnt(%0)" :: "i"(N) : "memory"); }

__device__ __forceinline__ u32x2 tile_scores(const u32x4* K, const u32x4 (&q)[4]) {
  f32x4 s = {0.f, 0.f, 0.f, 0.f};
#pragma unroll
  for (int i = 0; i < 4; ++i) s = __builtin_amdgcn_mfma_f32_16x16x32_bf16(__builtin_bit_cast(bf16x8, K[i]), __builtin_bit_cast(bf16x8, q[i]), s, 0, 0, 0);
  const unsigned r01 = cvtpk(s[0], s[1]), r23 = cvtpk(s[2], s[3]);
  return u32x2{cvtpk(lo16(r01) * SCALE, hi16(r01) * SCALE), cvtpk(lo16(r23) * SCALE, hi16(r23) * SCALE)};
}
__device__ __forceinline__ float max4(u32x2 t) { return fmaxf(fmaxf(lo16(t[0]), hi16(t[0])), fmaxf(lo16(t[1]), hi16(t[1]))); }
__device__ __forceinline__ u32x2 to_probs(u32x2 t, float m2, float& l) {
  const float p0 = __builtin_amdgcn_exp2f(fmaf(lo16(t[0]), LOG2E, -m2)), p1 = __builtin_amdgcn_exp2f(fmaf(hi16(t[0]), LOG2E, -m2));
  const float p2 = __builtin_amdgcn_exp2f(fmaf(lo16(t[1]), LOG2E, -m2)), p3 = __builtin_amdgcn_exp2f(fmaf(hi16(t[1]), LOG2E, -m2));
  l += (p0 + p1) + (p2 + p3);
  return u32x2{cvtpk(p0, p1), cvtpk(p2, p3)};
}
struct Sel { unsigned l0, l1, h0, h1; };
__device__ __forceinline__ void tile_pv(f32x4& a0, f32x4& a1, const u32x4* V, u32x2 p, const Sel& s) {
#pragma unroll
  for (int r = 0; r < 4; ++r) {
    const unsigned pk = p[r >> 1];
    const u32x2 a = {__builtin_amdgcn_perm(0u, pk, (r & 1) ? s.h0 : s.l0), __builtin_amdgcn_perm(0u, pk, (r & 1) ? s.h1 : s.l1)};
    const u32x2 v0 = {V[r][0], V[r][1]}, v1 = {V[r][2], V[r][3]};
    a0 = __builtin_amdgcn_mfma_f32_16x16x16bf16_1k(__builtin_bit_cast(s16x4, a), __builtin_bit_cast(s16x4, v0), a0, 0, 0, 0);
    a1 = __builtin_amdgcn_mfma_f32_16x16x16bf16_1k(__builtin_bit_cast(s16x4, a), __builtin_bit_cast(s16x4, v1), a1, 0, 0, 0);
  }
}

#define STAMP(i) do {} while (0)
#define WSTAMP(i) do {} while (0)
#define DUMP_STAMPS() do {} while (0)

__global__ void __launch_bounds__(NTHR, 3) fused_decode(Args a) {
  __shared__ __attribute__((aligned(16))) unsigned char lds_raw[L_END];
  LAS unsigned char* lds = (LAS unsigned char*)lds_raw;
  const int b = blockIdx.x, h = b & 7, j = b >> 3;
  const int tid = threadIdx.x, lane = tid & 63, w = __builtin_amdgcn_readfirstlane(tid >> 6);
  const int hh = lane >> 4, i16 = lane & 15, jj = lane & 3;
  const int qcb = j >> 1, qrh = j & 1, qbr = qcb >> 3;

  if (w == NC) {
    const unsigned ring = (unsigned)(size_t)(lds + L_RING);
    int pub = 0, pslot = 0, slot = 0;
    auto publish1 = [&]() { lds_st(lds, L_FULL + 4 * pslot, (unsigned)(pub + 1)); ++pub; pslot = pslot + 1 == NS ? 0 : pslot + 1; };
    auto pre = [&](int q) {
      if (q >= NS) {
        const unsigned need = (unsigned)(q - NS + 1);
        if (lds_ld(lds, L_FREE + 4 * slot) < need) {
          vm_wait<0>();
          while (pub < q) publish1();
          lds_wait_ge<1>(lds, L_FREE + 4 * slot, need);
        }
      }
    };
    auto post = [&](int q) {
      if (q - pub >= LAG) publish1();
      slot = slot + 1 == NS ? 0 : slot + 1;
    };
    {
      const unsigned char* wq = (qbr ? a.wqB : a.wqA) + (size_t)(h * 512 + 64 * (qcb & 7)) * 2 + (size_t)qrh * 256 * 8192;
      const unsigned vo = (lane >> 3) * 8192 + (lane & 7) * 16;
#pragma unroll
      for (int q = 0; q < 4; ++q) {
#pragma unroll
        for (int m = 0; m < 8; ++m) dma1(ring + q * SLOT + m * 1024, vo, wq + (size_t)(q * 64 + m * 8) * 8192);
        if (q == 0) LDS_BARRIER();
        post(q);
      }
    }
    const unsigned k0 = hh * 256 + ((i16 ^ (0 + hh)) * 16), k1 = hh * 256 + ((i16 ^ (4 + hh)) * 16), k2 = hh * 256 + ((i16 ^ (8 + hh)) * 16), k3 = hh * 256 + ((i16 ^ (12 + hh)) * 16);
    const unsigned vl = lane * 16;
    {
      const unsigned char* kp = a.kB + ((size_t)h * SK + (SK - WIN) + j * 128) * 256;
      const unsigned char* vp = a.vB + ((size_t)h * SK + (SK - WIN) + j * 128) * 256;
      for (int q = 4; q < 12; ++q) {
        dma4(ring + slot * SLOT, k0, k1, k2, k3, kp);
        dma4(ring + slot * SLOT + 4096, vl, vl, vl, vl, vp);
        kp += 4096; vp += 4096;
        post(q);
      }
      kp = a.kA + ((size_t)h * SK + j * 2048) * 256;
      for (int q = 12; q < 76; ++q) {
        pre(q);
        dma4(ring + slot * SLOT, k0, k1, k2, k3, kp);
        dma4(ring + slot * SLOT + 4096, k0, k1, k2, k3, kp + 4096);
        kp += 8192;
        post(q);
      }
      WSTAMP(12);
      vp = a.vA + ((size_t)h * SK + j * 2048) * 256;
      for (int q = 76; q < 140; ++q) {
        pre(q);
        dma4(ring + slot * SLOT, vl, vl, vl, vl, vp);
        dma4(ring + slot * SLOT + 4096, vl, vl, vl, vl, vp + 4096);
        vp += 8192;
        post(q);
      }
      WSTAMP(9);
      vp = (j >> 4 ? a.woB : a.woA) + (size_t)(h * 512 + 32 * (j & 15)) * 1024;
      for (int q = 140; q < 144; ++q) {
        pre(q);
        dma4(ring + slot * SLOT, vl, vl, vl, vl, vp);
        dma4(ring + slot * SLOT + 4096, vl, vl, vl, vl, vp + 4096);
        vp += 8192;
        post(q);
      }
    }
    if (NSEQ - pub > 6) { vm_wait<48>(); publish1(); }
    if (NSEQ - pub > 5) { vm_wait<40>(); publish1(); }
    if (NSEQ - pub > 4) { vm_wait<32>(); publish1(); }
    if (NSEQ - pub > 3) { vm_wait<24>(); publish1(); }
    if (NSEQ - pub > 2) { vm_wait<16>(); publish1(); }
    if (NSEQ - pub > 1) { vm_wait<8>(); publish1(); }
    vm_wait<0>();
    while (pub < NSEQ) publish1();
    WSTAMP(10);
    return;
  }

  const __amdgpu_buffer_rsrc_t rs = __builtin_amdgcn_make_buffer_rsrc((void*)a.ws, 0, (int)WS_END, 0x00020000);
  const u32x2 gen = __builtin_amdgcn_raw_buffer_load_b64(rs, WS_GEN, 0, AUX_SC1);
  unsigned short xv[4];
  {
    const GAS unsigned char* xp = (const GAS unsigned char*)(qbr ? a.xB : a.xA) + 2 * (qrh * 256 + w * 32 + (lane >> 3));
#pragma unroll
    for (int i = 0; i < 4; ++i) xv[i] = *(const GAS unsigned short*)(xp + 16 * i);
  }
  if (tid < (L_RED - L_FULL) / 4) *(LAS unsigned*)(lds + L_FULL + 4 * tid) = 0u;
  LDS_BARRIER();

  lds_wait_ge<1>(lds, L_FULL + 4 * (w >> 1), (unsigned)((w >> 1) + 1));
  {
    u32x4 wv[4];
#pragma unroll
    for (int i = 0; i < 4; ++i) wv[i] = *(const LAS u32x4*)(lds + L_RING + (w >> 1) * SLOT + (w & 1) * 4096 + i * 1024 + lane * 16);
    float acc[8];
#pragma unroll
    for (int k = 0; k < 8; ++k) acc[k] = 0.f;
#pragma unroll
    for (int i = 0; i < 4; ++i) {
      const float x = lo16(xv[i]);
#pragma unroll
      for (int k = 0; k < 4; ++k) { acc[2 * k] = fmaf(x, lo16(wv[i][k]), acc[2 * k]); acc[2 * k + 1] = fmaf(x, hi16(wv[i][k]), acc[2 * k + 1]); }
    }
#pragma unroll
    for (int k = 0; k < 8; ++k) { float v = acc[k]; v += dpp<ROR8>(v); v = swap16_sum(v); v = swap32_sum(v); acc[k] = v; }
    if (lane < 8) {
      LAS f32x4* red = (LAS f32x4*)(lds + L_RED + w * 256 + lane * 32);
      red[0] = f32x4{acc[0], acc[1], acc[2], acc[3]}; red[1] = f32x4{acc[4], acc[5], acc[6], acc[7]};
    }
  }
  lds_arrive(lds, L_CNT + C_QRED, lane);
  const unsigned tagA = __builtin_amdgcn_readfirstlane(gen[0]) + 1u, tagB = __builtin_amdgcn_readfirstlane(gen[1]) + 1u;
  if (w == 0) {
    lds_wait_ge<0>(lds, L_CNT + C_QRED, NC);
    if (lane < 4) lds_st(lds, L_FREE + 4 * lane, (unsigned)(lane + 1));
    if (lane < 32) {
      f32x2 s = {0.f, 0.f};
#pragma unroll
      for (int ww = 0; ww < 8; ++ww) s += *(const LAS f32x2*)(lds + L_RED + ww * 256 + lane * 8);
      __builtin_amdgcn_raw_buffer_store_b128(u32x4{tagA, f2u(s[0]), f2u(s[1]), tagB}, rs, WS_QG + ((h * 32 + j) * 32 + lane) * 16, 0, AUX_SC1);
    }
  }
  STAMP(1);
  int slot = (4 + w) % NS;
  const unsigned kr0 = i16 * 256 + (((0 + hh) ^ i16) * 16), kr1 = i16 * 256 + (((4 + hh) ^ i16) * 16), kr2 = i16 * 256 + (((8 + hh) ^ i16) * 16), kr3 = i16 * 256 + (((12 + hh) ^ i16) * 16);
  const unsigned vr = hh * 1024 + i16 * 16;
  auto fetch = [&](int it, int kind, u32x4 (&T)[8]) {
    const unsigned seq1 = (unsigned)(4 + 8 * it + w + 1);
    lds_wait_ge<1>(lds, L_FULL + 4 * slot, seq1);
    const LAS unsigned char* sp = lds + L_RING + slot * SLOT;
    if (kind != 2) { T[0] = *(const LAS u32x4*)(sp + kr0); T[1] = *(const LAS u32x4*)(sp + kr1); T[2] = *(const LAS u32x4*)(sp + kr2); T[3] = *(const LAS u32x4*)(sp + kr3); }
    else {
#pragma unroll
      for (int r = 0; r < 4; ++r) T[r] = *(const LAS u32x4*)(sp + vr + r * 256);
    }
    if (kind == 1) { T[4] = *(const LAS u32x4*)(sp + 4096 + kr0); T[5] = *(const LAS u32x4*)(sp + 4096 + kr1); T[6] = *(const LAS u32x4*)(sp + 4096 + kr2); T[7] = *(const LAS u32x4*)(sp + 4096 + kr3); }
    else {
#pragma unroll
      for (int r = 0; r < 4; ++r) T[4 + r] = *(const LAS u32x4*)(sp + 4096 + vr + r * 256);
    }
    LDS_WAIT();
    lds_st(lds, L_FREE + 4 * slot, seq1);
    asm volatile("" : "+v"(T[0]), "+v"(T[1]), "+v"(T[2]), "+v"(T[3]), "+v"(T[4]), "+v"(T[5]), "+v"(T[6]), "+v"(T[7]) :: "memory");
    slot += 8; if (slot >= NS) slot -= NS;
  };
  u32x4 X0[8], X1[8], X2[8];
  fetch(0, 0, X0);
  if (PRE >= 2) fetch(1, 1, X1);
  if (PRE >= 3) fetch(2, 1, X2);
  {
    const unsigned off = WS_QG + ((h * 32 + 2 * (tid >> 5)) * 32 + (tid & 31)) * 16;
    u32x4 g0 = {0u, 0u, 0u, 0u}, g1 = {0u, 0u, 0u, 0u};
    bool ok = false;
    for (unsigned spin = 0; spin < SPIN_CAP; ++spin) {
      MEMFENCE();
      if (!ok) {
        g0 = __builtin_amdgcn_raw_buffer_load_b128(rs, off, 0, AUX_SC1);
        g1 = __builtin_amdgcn_raw_buffer_load_b128(rs, off + 512, 0, AUX_SC1);
        ok = g0[0] == tagA && g0[3] == tagB && g1[0] == tagA && g1[3] == tagB;
      }
      if (__all(ok)) break;
      if (QSLEEP) __builtin_amdgcn_s_sleep(QSLEEP);
    }
    *(LAS unsigned*)(lds + L_Q + tid * 4) = cvtpk(u2f(g0[1]) + u2f(g1[1]), u2f(g0[2]) + u2f(g1[2]));
  }
  lds_arrive(lds, L_CNT + C_QIN, lane);
  lds_wait_ge<0>(lds, L_CNT + C_QIN, NC);
  u32x4 qA[4], qB[4];
#pragma unroll
  for (int s = 0; s < 4; ++s) {
    qA[s] = *(const LAS u32x4*)(lds + L_Q + ((i16 >> 2) * 128 + 32 * s + 8 * hh) * 2);
    qB[s] = *(const LAS u32x4*)(lds + L_Q + 1024 + ((i16 >> 2) * 128 + 32 * s + 8 * hh) * 2);
  }
  STAMP(2);

  Sel sel;
  sel.l0 = jj == 0 ? 0x0c0c0100u : jj == 1 ? 0x01000c0cu : 0x0c0c0c0cu;  sel.h0 = jj == 0 ? 0x0c0c0302u : jj == 1 ? 0x03020c0cu : 0x0c0c0c0cu;
  sel.l1 = jj == 2 ? 0x0c0c0100u : jj == 3 ? 0x01000c0cu : 0x0c0c0c0cu;  sel.h1 = jj == 2 ? 0x0c0c0302u : jj == 3 ? 0x03020c0cu : 0x0c0c0c0cu;

  f32x4 b0 = {0.f, 0.f, 0.f, 0.f}, b1 = {0.f, 0.f, 0.f, 0.f};
  {
    const u32x2 t = tile_scores(X0, qB);
    const float m2 = swap32_max(swap16_max(max4(t))) * LOG2E;
    float l = 0.f;
    const u32x2 p = to_probs(t, m2, l);
    l = swap32_sum(swap16_sum(l));
    tile_pv(b0, b1, X0 + 4, p, sel);
    if (hh == 0 && jj == 0) *(LAS f32x2*)(lds + L_TB + ((i16 >> 2) * 8 + w) * 8) = f32x2{m2, l};
  }

  u32x2 P[16];
  float mx = -INFINITY;
#pragma unroll
  for (int i = 0; i < 8; ++i) {
    if (i == 0) { if (PRE < 2) fetch(1, 1, X1); }
    else if (i == 1) { if (PRE < 3) fetch(2, 1, X2); }
    else fetch(i + 1, 1, X1);
    const u32x4* T = i == 1 ? X2 : X1;
    P[2 * i] = tile_scores(T, qA);
    P[2 * i + 1] = tile_scores(T + 4, qA);
    mx = fmaxf(mx, fmaxf(max4(P[2 * i]), max4(P[2 * i + 1])));
  }
  STAMP(13);
  mx = swap32_max(swap16_max(mx));
  if (hh == 0 && jj == 0) *(LAS float*)(lds + L_T1 + ((i16 >> 2) * 8 + w) * 4) = mx;
  lds_arrive(lds, L_CNT + C_MAX, lane);
  lds_wait_ge<0>(lds, L_CNT + C_MAX, NC);
  {
    const f32x4 m0 = *(const LAS f32x4*)(lds + L_T1 + (i16 >> 2) * 32), m1 = *(const LAS f32x4*)(lds + L_T1 + (i16 >> 2) * 32 + 16);
    const float m2 = fmaxf(fmaxf(fmaxf(m0[0], m0[1]), fmaxf(m0[2], m0[3])), fmaxf(fmaxf(m1[0], m1[1]), fmaxf(m1[2], m1[3]))) * LOG2E;
    float l = 0.f;
#pragma unroll
    for (int t = 0; t < 16; ++t) P[t] = to_probs(P[t], m2, l);
    l = swap32_sum(swap16_sum(l));
    if (hh == 0 && jj == 0) *(LAS float*)(lds + L_T2 + ((i16 >> 2) * 8 + w) * 4) = l;
  }
  lds_arrive(lds, L_CNT + C_SUM, lane);
  lds_wait_ge<0>(lds, L_CNT + C_SUM, NC);
  const int mbr = w >> 2, mg = w & 3;
  {
    const int ww = lane & 7;
    float M, L;
    if (mbr == 0) {
      M = oct_max(*(const LAS float*)(lds + L_T1 + (mg * 8 + ww) * 4)) * LOG2E;
      L = oct_sum(*(const LAS float*)(lds + L_T2 + (mg * 8 + ww) * 4));
    } else {
      const f32x2 ml = *(const LAS f32x2*)(lds + L_TB + (mg * 8 + ww) * 8);
      M = oct_max(ml[0]);
      L = oct_sum(ml[1] * __builtin_amdgcn_exp2f(ml[0] - M));
    }
    if (lane == 0) __builtin_amdgcn_raw_buffer_store_b128(u32x4{tagA, f2u(M), f2u(L), tagB}, rs, WS_ML + (((h * 2 + mbr) * 4 + mg) * 32 + j) * 16, 0, AUX_SC1);
  }
  STAMP(14);

  f32x4 a0 = {0.f, 0.f, 0.f, 0.f}, a1 = {0.f, 0.f, 0.f, 0.f};
  float fA = 0.f, fB = 0.f;
#pragma unroll
  for (int i = 0; i < 8; ++i) {
    if (i == 4) {
      const unsigned off = WS_ML + (((h * 2 + mbr) * 4 + mg) * 32 + (lane & 31)) * 16;
      u32x4 mv = {0u, 0u, 0u, 0u};
      bool ok = false;
      for (unsigned spin = 0; spin < SPIN_CAP; ++spin) {
        MEMFENCE();
        if (!ok) { mv = __builtin_amdgcn_raw_buffer_load_b128(rs, off, 0, AUX_SC1); ok = mv[0] == tagA && mv[3] == tagB; }
        if (__all(ok)) break;
        __builtin_amdgcn_s_sleep(16);
      }
      const float M = swap16_max(row_max(u2f(mv[1])));
      const float L = swap16_sum(row_sum(u2f(mv[2]) * __builtin_amdgcn_exp2f(u2f(mv[1]) - M)));
      if (lane == 0) *(LAS f32x2*)(lds + L_FF + w * 8) = f32x2{M, 1.0f / L};
      lds_arrive(lds, L_CNT + C_FF, lane);
    }
    if (i == 7) {
      lds_wait_ge<0>(lds, L_CNT + C_FF, NC);
      const f32x4 m0 = *(const LAS f32x4*)(lds + L_T1 + hh * 32), m1 = *(const LAS f32x4*)(lds + L_T1 + hh * 32 + 16);
      const float m2 = fmaxf(fmaxf(fmaxf(m0[0], m0[1]), fmaxf(m0[2], m0[3])), fmaxf(fmaxf(m1[0], m1[1]), fmaxf(m1[2], m1[3]))) * LOG2E;
      const f32x2 FA = *(const LAS f32x2*)(lds + L_FF + hh * 8), FB = *(const LAS f32x2*)(lds + L_FF + (4 + hh) * 8);
      const float mB = *(const LAS float*)(lds + L_TB + (hh * 8 + w) * 8);
      fA = __builtin_amdgcn_exp2f(m2 - FA[0]) * FA[1];
      fB = __builtin_amdgcn_exp2f(mB - FB[0]) * FB[1];
      const int s = lane < MRG_SLOTS ? lane : 0;
      const unsigned need = (unsigned)(s + NS * ((139 - s) / NS) + 1);
      for (unsigned spin = 0; spin < LSPIN_CAP; ++spin) { MEMFENCE(); if (__all(*(volatile LAS unsigned*)(lds + L_FREE + 4 * s) >= need)) break; }
      MEMFENCE();
      LAS float* rB = (LAS float*)(lds + L_MRG) + ((8 + w) * 4 + hh) * MREC + 8 * i16;
      *(LAS f32x4*)(rB) = b0 * fB; *(LAS f32x4*)(rB + 4) = b1 * fB;
    }
    fetch(9 + i, 2, X1);
    tile_pv(a0, a1, X1, P[2 * i], sel);
    tile_pv(a0, a1, X1 + 4, P[2 * i + 1], sel);
  }
  STAMP(3);
  if (w == 7) WSTAMP(11);

  {
    LAS float* rA = (LAS float*)(lds + L_MRG) + (w * 4 + hh) * MREC + 8 * i16;
    *(LAS f32x4*)(rA) = a0 * fA; *(LAS f32x4*)(rA + 4) = a1 * fA;
  }
  lds_arrive(lds, L_CNT + C_MRG, lane);
  lds_wait_ge<0>(lds, L_CNT + C_MRG, NC);
  {
    const int br = tid >> 8, g = (tid >> 6) & 3, dp = lane;
    const LAS float* mrg = (const LAS float*)(lds + L_MRG) + (br * 8 * 4 + g) * MREC + 2 * dp;
    f32x2 o2[8];
#pragma unroll
    for (int ww = 0; ww < 8; ++ww) o2[ww] = *(const LAS f32x2*)(mrg + ww * 4 * MREC);
    const f32x2 o = ((o2[0] + o2[1]) + (o2[2] + o2[3])) + ((o2[4] + o2[5]) + (o2[6] + o2[7]));
    const int jc = br * 16 + g * 4 + (dp >> 4), k = dp & 15;
    __builtin_amdgcn_raw_buffer_store_b128(u32x4{tagA, f2u(o[0]), f2u(o[1]), tagB}, rs, WS_I1 + (((h * 32 + jc) * 32 + j) * 16 + k) * 16, 0, AUX_SC1);
  }
  STAMP(4);

  {
    const unsigned offA = WS_I1 + ((h * 32 + j) * 32 * 16 + w * 64 + lane) * 16;
    u32x4 av = {0u, 0u, 0u, 0u};
    bool ok = false;
    for (unsigned spin = 0; spin < SPIN_CAP; ++spin) {
      MEMFENCE();
      if (!ok) { av = __builtin_amdgcn_raw_buffer_load_b128(rs, offA, 0, AUX_SC1); ok = av[0] == tagA && av[3] == tagB; }
      if (__all(ok)) break;
      if (PSLEEP) __builtin_amdgcn_s_sleep(PSLEEP);
    }
    STAMP(5);
    const float o0 = swap32_sum(swap16_sum(u2f(av[1]))), o1 = swap32_sum(swap16_sum(u2f(av[2])));
    if (lane < 16) *(LAS f32x2*)(lds + L_H1 + (w * 32 + 2 * lane) * 4) = f32x2{o0, o1};
  }
  LDS_BARRIER();
  {
    const float o = oct_sum(*(const LAS float*)(lds + L_H1 + ((lane & 7) * 32 + 4 * w + ((lane >> 3) & 3)) * 4));
    const unsigned ob = cvtpk(o, 0.f) << 16;
    const float orow[4] = {u2f(__builtin_amdgcn_readlane(ob, 0)), u2f(__builtin_amdgcn_readlane(ob, 8)), u2f(__builtin_amdgcn_readlane(ob, 16)), u2f(__builtin_amdgcn_readlane(ob, 24))};
    const int wseq = 140 + (w >> 1);
    lds_wait_ge<0>(lds, L_FULL + 4 * (wseq % NS), (unsigned)(wseq + 1));
    const LAS unsigned char* wp = lds + L_RING + (wseq % NS) * SLOT + (w & 1) * 4096 + lane * 16;
    float y[8];
#pragma unroll
    for (int k = 0; k < 8; ++k) y[k] = 0.f;
#pragma unroll
    for (int r4 = 0; r4 < 4; ++r4) {
      const u32x4 wv = *(const LAS u32x4*)(wp + r4 * 1024);
#pragma unroll
      for (int k = 0; k < 4; ++k) { y[2 * k] = fmaf(orow[r4], lo16(wv[k]), y[2 * k]); y[2 * k + 1] = fmaf(orow[r4], hi16(wv[k]), y[2 * k + 1]); }
    }
    LAS f32x4* yl = (LAS f32x4*)(lds + L_Y + w * 2048 + lane * 32);
    yl[0] = f32x4{y[0], y[1], y[2], y[3]}; yl[1] = f32x4{y[4], y[5], y[6], y[7]};
  }
  LDS_BARRIER();
  if (tid < 256) {
    const LAS unsigned char* yl = lds + L_Y + tid * 8;
    f32x2 s2[8];
#pragma unroll
    for (int k = 0; k < 8; ++k) s2[k] = *(const LAS f32x2*)(yl + k * 2048);
    const f32x2 s = ((s2[0] + s2[1]) + (s2[2] + s2[3])) + ((s2[4] + s2[5]) + (s2[6] + s2[7]));
    const int c = (j >> 4) * 32 + (tid >> 3), k = tid & 7, pidx = h * 16 + (j & 15);
    __builtin_amdgcn_raw_buffer_store_b128(u32x4{tagA, f2u(s[0]), f2u(s[1]), tagB}, rs, WS_I2 + ((c * 128 + pidx) * 8 + k) * 16, 0, AUX_SC1);
  }
  STAMP(6);

  if (j < 8) {
    const int c = h * 8 + j;
    const unsigned off = WS_I2 + (c * 128 + 16 * w) * 128 + lane * 16;
    u32x4 v[2];
    bool ok = false;
    for (unsigned spin = 0; spin < SPIN_CAP; ++spin) {
      MEMFENCE();
      if (!ok) {
#pragma unroll
        for (int i = 0; i < 2; ++i) v[i] = __builtin_amdgcn_raw_buffer_load_b128(rs, off + i * 1024, 0, AUX_SC1);
        ok = v[0][0] == tagA && v[0][3] == tagB && v[1][0] == tagA && v[1][3] == tagB;
      }
      if (__all(ok)) break;
      if (PSLEEP) __builtin_amdgcn_s_sleep(PSLEEP);
    }
    STAMP(7);
    float s0 = u2f(v[0][1]) + u2f(v[1][1]);
    float s1 = u2f(v[0][2]) + u2f(v[1][2]);
    s0 += dpp<ROR8>(s0); s1 += dpp<ROR8>(s1);
    s0 = swap32_sum(swap16_sum(s0)); s1 = swap32_sum(swap16_sum(s1));
    if (lane < 8) *(LAS f32x2*)(lds + L_F + w * 64 + lane * 8) = f32x2{s0, s1};
    LDS_BARRIER();
    if (tid < 8) {
      f32x2 s = *(const LAS f32x2*)(lds + L_F + tid * 8);
#pragma unroll
      for (int ww = 1; ww < 8; ++ww) s += *(const LAS f32x2*)(lds + L_F + ww * 64 + tid * 8);
      *(GAS unsigned*)((GAS unsigned char*)a.out + (c * 16 + 2 * tid) * 2) = cvtpk(s[0], s[1]);
      if (c == 0 && tid == 0) __builtin_amdgcn_raw_buffer_store_b64(u32x2{tagA, tagB}, rs, WS_GEN, 0, AUX_SC1);
    }
  }
  DUMP_STAMPS();
}

extern "C" void kernel_launch(void* const* d_in, const int* in_sizes, int n_in, void* d_out, int out_size, void* d_ws, size_t ws_size, hipStream_t stream) {
  Args a;
  a.xA = (const unsigned char*)d_in[0]; a.kA = (const unsigned char*)d_in[1]; a.vA = (const unsigned char*)d_in[2]; a.wqA = (const unsigned char*)d_in[3]; a.woA = (const unsigned char*)d_in[4];
  a.xB = (const unsigned char*)d_in[5]; a.kB = (const unsigned char*)d_in[6]; a.vB = (const unsigned char*)d_in[7]; a.wqB = (const unsigned char*)d_in[8]; a.woB = (const unsigned char*)d_in[9];
  a.out = (unsigned short*)d_out;
  a.ws = (unsigned char*)d_ws;
  hipLaunchKernelGGL(fused_decode, dim3(NWG), dim3(NTHR), 0, stream, a);
}
```

```cpp
#include <hip/hip_runtime.h>
#include <hip/hip_bf16.h>
#include <stdint.h>
#include <stdio.h>

#define GAS __attribute__((address_space(1)))
#define LAS __attribute__((address_space(3)))
typedef unsigned u32x4 __attribute__((ext_vector_type(4)));
typedef unsigned u32x2 __attribute__((ext_vector_type(2)));
typedef float f32x4 __attribute__((ext_vector_type(4)));
typedef float f32x2 __attribute__((ext_vector_type(2)));
typedef __bf16 bf16x8 __attribute__((ext_vector_type(8)));
typedef __bf16 bf16x2 __attribute__((ext_vector_type(2)));
typedef short s16x4 __attribute__((ext_vector_type(4)));

#define NS 18
#define WIN_LD 22
#define PRE 2
#define QSLEEP 16
#define PSLEEP 0
#define BENCH 0
#define STAMPS 1

constexpr int SK = 65536, WIN = 4096;
constexpr int NWG = 256, NC = 8, NTHR = (NC + 1) * 64;
constexpr int SLOT = 8192, NSEQ = 4 + 8 + 64 + 64 + 4;
constexpr int LAG = (WIN_LD + 7) / 8;
constexpr float LOG2E = 1.4426950408889634f;
constexpr float SCALE = 0.08838834764831845f;
constexpr unsigned SPIN_CAP = 1u << 20, LSPIN_CAP = 1u << 24;

constexpr unsigned WS_GEN = 0;
constexpr unsigned WS_QG = 4096;
constexpr unsigned WS_ML = WS_QG + 8 * 32 * 32 * 16;
constexpr unsigned WS_I1 = WS_ML + 256 * 128;
constexpr unsigned WS_I2 = WS_I1 + 8 * 32 * 32 * 256;
constexpr unsigned WS_END = WS_I2 + 64 * 128 * 128;
constexpr unsigned WS_STAMP = WS_END;

constexpr int L_RING = 0;
constexpr int L_FULL = NS * SLOT;
constexpr int L_FREE = L_FULL + 128;
constexpr int L_CNT = L_FREE + 128;
constexpr int L_RED = L_CNT + 64;
constexpr int L_Q = L_RED + 2048;
constexpr int L_T1 = L_Q + 2048;
constexpr int L_T2 = L_T1 + 128;
constexpr int L_TB = L_T2 + 128;
constexpr int L_FF = L_TB + 256;
constexpr int L_H1 = L_FF + 64;
constexpr int L_F = L_H1 + 8 * 32 * 4;
constexpr int L_END = L_F + 512 + 128;
constexpr int C_QRED = 0, C_QIN = 4, C_MAX = 8, C_SUM = 12, C_FF = 16, C_MRG = 20;
constexpr int MREC = 132;
constexpr int L_MRG = 0;
constexpr int MRG_SLOTS = 5;
constexpr int L_Y = 5 * SLOT;
static_assert(L_END <= 160 * 1024, "LDS");
static_assert(2 * 8 * 4 * MREC * 4 <= MRG_SLOTS * SLOT, "merge area");
static_assert((140 % NS) >= 7 && (143 % NS) >= 7 && (140 % NS) <= (143 % NS), "wo slots clear of the merge / y areas");
static_assert((132 % NS) >= MRG_SLOTS && (139 % NS) >= MRG_SLOTS && (132 % NS) <= (139 % NS), "last round clear of the merge area");
static_assert(WIN_LD >= 9 && WIN_LD <= 48, "window");

__device__ __forceinline__ unsigned cvtpk(float lo, float hi) { f32x2 f = {lo, hi}; return __builtin_bit_cast(unsigned, __builtin_convertvector(f, bf16x2)); }
__device__ __forceinline__ float lo16(unsigned u) { return __builtin_bit_cast(float, u << 16); }
__device__ __forceinline__ float hi16(unsigned u) { return __builtin_bit_cast(float, u & 0xffff0000u); }
__device__ __forceinline__ float u2f(unsigned u) { return __builtin_bit_cast(float, u); }
__device__ __forceinline__ unsigned f2u(float f) { return __builtin_bit_cast(unsigned, f); }
template <int CTRL> __device__ __forceinline__ float dpp(float x) { return u2f(__builtin_amdgcn_mov_dpp(f2u(x), CTRL, 0xf, 0xf, true)); }
constexpr int XOR1 = 0xB1, XOR2 = 0x4E, HMIR = 0x141;
constexpr int ROR1 = 0x121, ROR2 = 0x122, ROR4 = 0x124, ROR8 = 0x128;
__device__ __forceinline__ float swap16_max(float x) { auto s = __builtin_amdgcn_permlane16_swap(f2u(x), f2u(x), false, false); return fmaxf(u2f(s[0]), u2f(s[1])); }
__device__ __forceinline__ float swap32_max(float x) { auto s = __builtin_amdgcn_permlane32_swap(f2u(x), f2u(x), false, false); return fmaxf(u2f(s[0]), u2f(s[1])); }
__device__ __forceinline__ float swap16_sum(float x) { auto s = __builtin_amdgcn_permlane16_swap(f2u(x), f2u(x), false, false); return u2f(s[0]) + u2f(s[1]); }
__device__ __forceinline__ float swap32_sum(float x) { auto s = __builtin_amdgcn_permlane32_swap(f2u(x), f2u(x), false, false); return u2f(s[0]) + u2f(s[1]); }
__device__ __forceinline__ float row_sum(float x) { x += dpp<ROR8>(x); x += dpp<ROR4>(x); x += dpp<ROR2>(x); x += dpp<ROR1>(x); return x; }
__device__ __forceinline__ float row_max(float x) { x = fmaxf(x, dpp<ROR8>(x)); x = fmaxf(x, dpp<ROR4>(x)); x = fmaxf(x, dpp<ROR2>(x)); x = fmaxf(x, dpp<ROR1>(x)); return x; }
__device__ __forceinline__ float oct_sum(float x) { x += dpp<XOR1>(x); x += dpp<XOR2>(x); x += dpp<HMIR>(x); return x; }
__device__ __forceinline__ float oct_max(float x) { x = fmaxf(x, dpp<XOR1>(x)); x = fmaxf(x, dpp<XOR2>(x)); x = fmaxf(x, dpp<HMIR>(x)); return x; }
#define LDS_BARRIER() asm volatile("s_waitcnt lgkmcnt(0)\n\ts_barrier" ::: "memory")
#define LDS_WAIT() asm volatile("s_waitcnt lgkmcnt(0)" ::: "memory")
#define MEMFENCE() asm volatile("" ::: "memory")
constexpr int AUX_SC1 = 16;

struct Args {
  const unsigned char *xA, *kA, *vA, *wqA, *woA, *xB, *kB, *vB, *wqB, *woB;
  unsigned short* out;
  unsigned char* ws;
};

__device__ __forceinline__ unsigned lds_ld(LAS unsigned char* L, int off) { return __builtin_amdgcn_readfirstlane(*(volatile LAS unsigned*)(L + off)); }
__device__ __forceinline__ void lds_st(LAS unsigned char* L, int off, unsigned v) { *(volatile LAS unsigned*)(L + off) = v; }
template <int SLEEP> __device__ __forceinline__ void lds_wait_ge(LAS unsigned char* L, int off, unsigned need) {
  for (unsigned spin = 0; lds_ld(L, off) < need && spin < LSPIN_CAP; ++spin) { if (SLEEP) __builtin_amdgcn_s_sleep(SLEEP); }
  MEMFENCE();
}
__device__ __forceinline__ void lds_arrive(LAS unsigned char* L, int off, int lane) {
  LDS_WAIT();
  if (lane == 0) __hip_atomic_fetch_add((LAS unsigned*)(L + off), 1u, __ATOMIC_RELAXED, __HIP_MEMORY_SCOPE_WORKGROUP);
}

__device__ __forceinline__ void dma1(unsigned dst, unsigned voff, const unsigned char* base) {
  unsigned keep;
  asm volatile("s_mov_b32 %0, m0\n\ts_mov_b32 m0, %1\n\ts_nop 0\n\tglobal_load_lds_dwordx4 %2, %3 nt\n\ts_mov_b32 m0, %0\n\ts_waitcnt vmcnt(%4)"
               : "=&s"(keep) : "s"(dst), "v"(voff), "s"(base), "i"(WIN_LD) : "memory");
}
__device__ __forceinline__ void dma4(unsigned dst, unsigned v0, unsigned v1, unsigned v2, unsigned v3, const unsigned char* base) {
  unsigned keep;
  asm volatile("s_mov_b32 %0, m0\n\ts_mov_b32 m0, %1\n\ts_nop 0\n\t"
               "global_load_lds_dwordx4 %2, %6 nt\n\ts_waitcnt vmcnt(%7)\n\t"
               "global_load_lds_dwordx4 %3, %6 offset:1024 nt\n\ts_waitcnt vmcnt(%7)\n\t"
               "global_load_lds_dwordx4 %4, %6 offset:2048 nt\n\ts_waitcnt vmcnt(%7)\n\t"
               "global_load_lds_dwordx4 %5, %6 offset:3072 nt\n\ts_waitcnt vmcnt(%7)\n\t"
               "s_mov_b32 m0, %0"
               : "=&s"(keep) : "s"(dst), "v"(v0), "v"(v1), "v"(v2), "v"(v3), "s"(base), "i"(WIN_LD) : "memory");
}
template <int N> __device__ __forceinline__ void vm_wait() { asm volatile("s_waitcnt vmcnt(%0)" :: "i"(N) : "memory"); }

__device__ __forceinline__ u32x2 tile_scores(const u32x4* K, const u32x4 (&q)[4]) {
  f32x4 s = {0.f, 0.f, 0.f, 0.f};
#pragma unroll
  for (int i = 0; i < 4; ++i) s = __builtin_amdgcn_mfma_f32_16x16x32_bf16(__builtin_bit_cast(bf16x8, K[i]), __builtin_bit_cast(bf16x8, q[i]), s, 0, 0, 0);
  const unsigned r01 = cvtpk(s[0], s[1]), r23 = cvtpk(s[2], s[3]);
  return u32x2{cvtpk(lo16(r01) * SCALE, hi16(r01) * SCALE), cvtpk(lo16(r23) * SCALE, hi16(r23) * SCALE)};
}
__device__ __forceinline__ float max4(u32x2 t) { return fmaxf(fmaxf(lo16(t[0]), hi16(t[0])), fmaxf(lo16(t[1]), hi16(t[1]))); }
__device__ __forceinline__ u32x2 to_probs(u32x2 t, float m2, float& l) {
  const float p0 = __builtin_amdgcn_exp2f(fmaf(lo16(t[0]), LOG2E, -m2)), p1 = __builtin_amdgcn_exp2f(fmaf(hi16(t[0]), LOG2E, -m2));
  const float p2 = __builtin_amdgcn_exp2f(fmaf(lo16(t[1]), LOG2E, -m2)), p3 = __builtin_amdgcn_exp2f(fmaf(hi16(t[1]), LOG2E, -m2));
  l += (p0 + p1) + (p2 + p3);
  return u32x2{cvtpk(p0, p1), cvtpk(p2, p3)};
}
struct Sel { unsigned l0, l1, h0, h1; };
__device__ __forceinline__ void tile_pv(f32x4& a0, f32x4& a1, const u32x4* V, u32x2 p, const Sel& s) {
#pragma unroll
  for (int r = 0; r < 4; ++r) {
    const unsigned pk = p[r >> 1];
    const u32x2 a = {__builtin_amdgcn_perm(0u, pk, (r & 1) ? s.h0 : s.l0), __builtin_amdgcn_perm(0u, pk, (r & 1) ? s.h1 : s.l1)};
    const u32x2 v0 = {V[r][0], V[r][1]}, v1 = {V[r][2], V[r][3]};
    a0 = __builtin_amdgcn_mfma_f32_16x16x16bf16_1k(__builtin_bit_cast(s16x4, a), __builtin_bit_cast(s16x4, v0), a0, 0, 0, 0);
    a1 = __builtin_amdgcn_mfma_f32_16x16x16bf16_1k(__builtin_bit_cast(s16x4, a), __builtin_bit_cast(s16x4, v1), a1, 0, 0, 0);
  }
}

#define STAMP(i) do { if (tid == 0) { ((volatile LAS unsigned long long*)(lds + L_END - 128))[(i)] = __builtin_amdgcn_s_memrealtime(); } } while (0)
#define WSTAMP(i) do { if (lane == 0) { ((volatile LAS unsigned long long*)(lds + L_END - 128))[(i)] = __builtin_amdgcn_s_memrealtime(); } } while (0)
#define DUMP_STAMPS() do { if (tid == 0) { GAS unsigned long long* sp = (GAS unsigned long long*)(a.ws + WS_STAMP) + b * 16; \
    const unsigned long long t_end = __builtin_amdgcn_s_memrealtime(); \
    for (int i = 1; i < 16; ++i) sp[i] = ((volatile LAS unsigned long long*)(lds + L_END - 128))[i]; sp[0] = t_start; sp[8] = t_end; \
    if ((b % 37) == 0) { volatile LAS unsigned long long* q = (volatile LAS unsigned long long*)(lds + L_END - 128); \
      printf("WG %3d t0 %llu: qpub %d qfrag %d Kend(w0) %d MLpub %d ldKissued %d ldVissued %d landed %d Vend(w0) %d Vend(w7) %d pub1 %d hop1 %d pub2 %d end %d\n", b, t_start % 100000000ull, \
        (int)(q[1]-t_start), (int)(q[2]-t_start), (int)(q[13]-t_start), (int)(q[14]-t_start), (int)(q[12]-t_start), (int)(q[9]-t_start), (int)(q[10]-t_start), (int)(q[3]-t_start), (int)(q[11]-t_start), (int)(q[4]-t_start), (int)(q[5]-t_start), (int)(q[6]-t_start), (int)(t_end-t_start)); } } } while (0)

__global__ void __launch_bounds__(NTHR, 3) fused_decode(Args a) {
  __shared__ __attribute__((aligned(16))) unsigned char lds_raw[L_END];
  LAS unsigned char* lds = (LAS unsigned char*)lds_raw;
  const int b = blockIdx.x, h = b & 7, j = b >> 3;
  const int tid = threadIdx.x, lane = tid & 63, w = __builtin_amdgcn_readfirstlane(tid >> 6);
  const int hh = lane >> 4, i16 = lane & 15, jj = lane & 3;
  const unsigned long long t_start = __builtin_amdgcn_s_memrealtime();
  const int qcb = j >> 1, qrh = j & 1, qbr = qcb >> 3;

  if (w == NC) {
    const unsigned ring = (unsigned)(size_t)(lds + L_RING);
    int pub = 0, pslot = 0, slot = 0;
    auto publish1 = [&]() { lds_st(lds, L_FULL + 4 * pslot, (unsigned)(pub + 1)); ++pub; pslot = pslot + 1 == NS ? 0 : pslot + 1; };
    auto pre = [&](int q) {
      if (q >= NS) {
        const unsigned need = (unsigned)(q - NS + 1);
        if (lds_ld(lds, L_FREE + 4 * slot) < need) {
          vm_wait<0>();
          while (pub < q) publish1();
          lds_wait_ge<1>(lds, L_FREE + 4 * slot, need);
        }
      }
    };
    auto post = [&](int q) {
      if (q - pub >= LAG) publish1();
      slot = slot + 1 == NS ? 0 : slot + 1;
    };
    {
      const unsigned char* wq = (qbr ? a.wqB : a.wqA) + (size_t)(h * 512 + 64 * (qcb & 7)) * 2 + (size_t)qrh * 256 * 8192;
      const unsigned vo = (lane >> 3) * 8192 + (lane & 7) * 16;
#pragma unroll
      for (int q = 0; q < 4; ++q) {
#pragma unroll
        for (int m = 0; m < 8; ++m) dma1(ring + q * SLOT + m * 1024, vo, wq + (size_t)(q * 64 + m * 8) * 8192);
        if (q == 0) LDS_BARRIER();
        post(q);
      }
    }
    const unsigned k0 = hh * 256 + ((i16 ^ (0 + hh)) * 16), k1 = hh * 256 + ((i16 ^ (4 + hh)) * 16), k2 = hh * 256 + ((i16 ^ (8 + hh)) * 16), k3 = hh * 256 + ((i16 ^ (12 + hh)) * 16);
    const unsigned vl = lane * 16;
    {
      const unsigned char* kp = a.kB + ((size_t)h * SK + (SK - WIN) + j * 128) * 256;
      const unsigned char* vp = a.vB + ((size_t)h * SK + (SK - WIN) + j * 128) * 256;
      for (int q = 4; q < 12; ++q) {
        dma4(ring + slot * SLOT, k0, k1, k2, k3, kp);
        dma4(ring + slot * SLOT + 4096, vl, vl, vl, vl, vp);
        kp += 4096; vp += 4096;
        post(q);
      }
      kp = a.kA + ((size_t)h * SK + j * 2048) * 256;
      for (int q = 12; q < 76; ++q) {
        pre(q);
        dma4(ring + slot * SLOT, k0, k1, k2, k3, kp);
        dma4(ring + slot * SLOT + 4096, k0, k1, k2, k3, kp + 4096);
        kp += 8192;
        post(q);
      }
      WSTAMP(12);
      vp = a.vA + ((size_t)h * SK + j * 2048) * 256;
      for (int q = 76; q < 140; ++q) {
        pre(q);
        dma4(ring + slot * SLOT, vl, vl, vl, vl, vp);
        dma4(ring + slot * SLOT + 4096, vl, vl, vl, vl, vp + 4096);
        vp += 8192;
        post(q);
      }
      WSTAMP(9);
      vp = (j >> 4 ? a.woB : a.woA) + (size_t)(h * 512 + 32 * (j & 15)) * 1024;
      for (int q = 140; q < 144; ++q) {
        pre(q);
        dma4(ring + slot * SLOT, vl, vl, vl, vl, vp);
        dma4(ring + slot * SLOT + 4096, vl, vl, vl, vl, vp + 4096);
        vp += 8192;
        post(q);
      }
    }
    if (NSEQ - pub > 6) { vm_wait<48>(); publish1(); }
    if (NSEQ - pub > 5) { vm_wait<40>(); publish1(); }
    if (NSEQ - pub > 4) { vm_wait<32>(); publish1(); }
    if (NSEQ - pub > 3) { vm_wait<24>(); publish1(); }
    if (NSEQ - pub > 2) { vm_wait<16>(); publish1(); }
    if (NSEQ - pub > 1) { vm_wait<8>(); publish1(); }
    vm_wait<0>();
    while (pub < NSEQ) publish1();
    WSTAMP(10);
    return;
  }

  const __amdgpu_buffer_rsrc_t rs = __builtin_amdgcn_make_buffer_rsrc((void*)a.ws, 0, (int)WS_END, 0x00020000);
  const u32x2 gen = __builtin_amdgcn_raw_buffer_load_b64(rs, WS_GEN, 0, AUX_SC1);
  unsigned short xv[4];
  {
    const GAS unsigned char* xp = (const GAS unsigned char*)(qbr ? a.xB : a.xA) + 2 * (qrh * 256 + w * 32 + (lane >> 3));
#pragma unroll
    for (int i = 0; i < 4; ++i) xv[i] = *(const GAS unsigned short*)(xp + 16 * i);
  }
  if (tid < (L_RED - L_FULL) / 4) *(LAS unsigned*)(lds + L_FULL + 4 * tid) = 0u;
  if (tid >= 128 && tid < 160) *(LAS unsigned*)(lds + L_END - 128 + 4 * (tid - 128)) = 0u;
  LDS_BARRIER();

  lds_wait_ge<1>(lds, L_FULL + 4 * (w >> 1), (unsigned)((w >> 1) + 1));
  {
    u32x4 wv[4];
#pragma unroll
    for (int i = 0; i < 4; ++i) wv[i] = *(const LAS u32x4*)(lds + L_RING + (w >> 1) * SLOT + (w & 1) * 4096 + i * 1024 + lane * 16);
    float acc[8];
#pragma unroll
    for (int k = 0; k < 8; ++k) acc[k] = 0.f;
#pragma unroll
    for (int i = 0; i < 4; ++i) {
      const float x = lo16(xv[i]);
#pragma unroll
      for (int k = 0; k < 4; ++k) { acc[2 * k] = fmaf(x, lo16(wv[i][k]), acc[2 * k]); acc[2 * k + 1] = fmaf(x, hi16(wv[i][k]), acc[2 * k + 1]); }
    }
#pragma unroll
    for (int k = 0; k < 8; ++k) { float v = acc[k]; v += dpp<ROR8>(v); v = swap16_sum(v); v = swap32_sum(v); acc[k] = v; }
    if (lane < 8) {
      LAS f32x4* red = (LAS f32x4*)(lds + L_RED + w * 256 + lane * 32);
      red[0] = f32x4{acc[0], acc[1], acc[2], acc[3]}; red[1] = f32x4{acc[4], acc[5], acc[6], acc[7]};
    }
  }
  lds_arrive(lds, L_CNT + C_QRED, lane);
  const unsigned tagA = __builtin_amdgcn_readfirstlane(gen[0]) + 1u, tagB = __builtin_amdgcn_readfirstlane(gen[1]) + 1u;
  if (w == 0) {
    lds_wait_ge<0>(lds, L_CNT + C_QRED, NC);
    if (lane < 4) lds_st(lds, L_FREE + 4 * lane, (unsigned)(lane + 1));
    if (lane < 32) {
      f32x2 s = {0.f, 0.f};
#pragma unroll
      for (int ww = 0; ww < 8; ++ww) s += *(const LAS f32x2*)(lds + L_RED + ww * 256 + lane * 8);
      __builtin_amdgcn_raw_buffer_store_b128(u32x4{tagA, f2u(s[0]), f2u(s[1]), tagB}, rs, WS_QG + ((h * 32 + j) * 32 + lane) * 16, 0, AUX_SC1);
    }
  }
  STAMP(1);
  int slot = (4 + w) % NS;
  const unsigned kr0 = i16 * 256 + (((0 + hh) ^ i16) * 16), kr1 = i16 * 256 + (((4 + hh) ^ i16) * 16), kr2 = i16 * 256 + (((8 + hh) ^ i16) * 16), kr3 = i16 * 256 + (((12 + hh) ^ i16) * 16);
  const unsigned vr = hh * 1024 + i16 * 16;
  auto fetch = [&](int it, int kind, u32x4 (&T)[8]) {
    const unsigned seq1 = (unsigned)(4 + 8 * it + w + 1);
    lds_wait_ge<1>(lds, L_FULL + 4 * slot, seq1);
    const LAS unsigned char* sp = lds + L_RING + slot * SLOT;
    if (kind != 2) { T[0] = *(const LAS u32x4*)(sp + kr0); T[1] = *(const LAS u32x4*)(sp + kr1); T[2] = *(const LAS u32x4*)(sp + kr2); T[3] = *(const LAS u32x4*)(sp + kr3); }
    else {
#pragma unroll
      for (int r = 0; r < 4; ++r) T[r] = *(const LAS u32x4*)(sp + vr + r * 256);
    }
    if (kind == 1) { T[4] = *(const LAS u32x4*)(sp + 4096 + kr0); T[5] = *(const LAS u32x4*)(sp + 4096 + kr1); T[6] = *(const LAS u32x4*)(sp + 4096 + kr2); T[7] = *(const LAS u32x4*)(sp + 4096 + kr3); }
    else {
#pragma unroll
      for (int r = 0; r < 4; ++r) T[4 + r] = *(const LAS u32x4*)(sp + 4096 + vr + r * 256);
    }
    LDS_WAIT();
    lds_st(lds, L_FREE + 4 * slot, seq1);
    asm volatile("" : "+v"(T[0]), "+v"(T[1]), "+v"(T[2]), "+v"(T[3]), "+v"(T[4]), "+v"(T[5]), "+v"(T[6]), "+v"(T[7]) :: "memory");
    slot += 8; if (slot >= NS) slot -= NS;
  };
  u32x4 X0[8], X1[8], X2[8];
  fetch(0, 0, X0);
  if (PRE >= 2) fetch(1, 1, X1);
  if (PRE >= 3) fetch(2, 1, X2);
  {
    const unsigned off = WS_QG + ((h * 32 + 2 * (tid >> 5)) * 32 + (tid & 31)) * 16;
    u32x4 g0 = {0u, 0u, 0u, 0u}, g1 = {0u, 0u, 0u, 0u};
    bool ok = false;
    for (unsigned spin = 0; spin < SPIN_CAP; ++spin) {
      MEMFENCE();
      if (!ok) {
        g0 = __builtin_amdgcn_raw_buffer_load_b128(rs, off, 0, AUX_SC1);
        g1 = __builtin_amdgcn_raw_buffer_load_b128(rs, off + 512, 0, AUX_SC1);
        ok = g0[0] == tagA && g0[3] == tagB && g1[0] == tagA && g1[3] == tagB;
      }
      if (__all(ok)) break;
      if (QSLEEP) __builtin_amdgcn_s_sleep(QSLEEP);
    }
    *(LAS unsigned*)(lds + L_Q + tid * 4) = cvtpk(u2f(g0[1]) + u2f(g1[1]), u2f(g0[2]) + u2f(g1[2]));
  }
  lds_arrive(lds, L_CNT + C_QIN, lane);
  lds_wait_ge<0>(lds, L_CNT + C_QIN, NC);
  u32x4 qA[4], qB[4];
#pragma unroll
  for (int s = 0; s < 4; ++s) {
    qA[s] = *(const LAS u32x4*)(lds + L_Q + ((i16 >> 2) * 128 + 32 * s + 8 * hh) * 2);
    qB[s] = *(const LAS u32x4*)(lds + L_Q + 1024 + ((i16 >> 2) * 128 + 32 * s + 8 * hh) * 2);
  }
  STAMP(2);

  Sel sel;
  sel.l0 = jj == 0 ? 0x0c0c0100u : jj == 1 ? 0x01000c0cu : 0x0c0c0c0cu;  sel.h0 = jj == 0 ? 0x0c0c0302u : jj == 1 ? 0x03020c0cu : 0x0c0c0c0cu;
  sel.l1 = jj == 2 ? 0x0c0c0100u : jj == 3 ? 0x01000c0cu : 0x0c0c0c0cu;  sel.h1 = jj == 2 ? 0x0c0c0302u : jj == 3 ? 0x03020c0cu : 0x0c0c0c0cu;

  f32x4 b0 = {0.f, 0.f, 0.f, 0.f}, b1 = {0.f, 0.f, 0.f, 0.f};
  {
    const u32x2 t = tile_scores(X0, qB);
    const float m2 = swap32_max(swap16_max(max4(t))) * LOG2E;
    float l = 0.f;
    const u32x2 p = to_probs(t, m2, l);
    l = swap32_sum(swap16_sum(l));
    tile_pv(b0, b1, X0 + 4, p, sel);
    if (hh == 0 && jj == 0) *(LAS f32x2*)(lds + L_TB + ((i16 >> 2) * 8 + w) * 8) = f32x2{m2, l};
  }

  u32x2 P[16];
  float mx = -INFINITY;
#pragma unroll
  for (int i = 0; i < 8; ++i) {
    if (i == 0) { if (PRE < 2) fetch(1, 1, X1); }
    else if (i == 1) { if (PRE < 3) fetch(2, 1, X2); }
    else fetch(i + 1, 1, X1);
    const u32x4* T = i == 1 ? X2 : X1;
    P[2 * i] = tile_scores(T, qA);
    P[2 * i + 1] = tile_scores(T + 4, qA);
    mx = fmaxf(mx, fmaxf(max4(P[2 * i]), max4(P[2 * i + 1])));
  }
  STAMP(13);
  mx = swap32_max(swap16_max(mx));
  if (hh == 0 && jj == 0) *(LAS float*)(lds + L_T1 + ((i16 >> 2) * 8 + w) * 4) = mx;
  lds_arrive(lds, L_CNT + C_MAX, lane);
  lds_wait_ge<0>(lds, L_CNT + C_MAX, NC);
  {
    const f32x4 m0 = *(const LAS f32x4*)(lds + L_T1 + (i16 >> 2) * 32), m1 = *(const LAS f32x4*)(lds + L_T1 + (i16 >> 2) * 32 + 16);
    const float m2 = fmaxf(fmaxf(fmaxf(m0[0], m0[1]), fmaxf(m0[2], m0[3])), fmaxf(fmaxf(m1[0], m1[1]), fmaxf(m1[2], m1[3]))) * LOG2E;
    float l = 0.f;
#pragma unroll
    for (int t = 0; t < 16; ++t) P[t] = to_probs(P[t], m2, l);
    l = swap32_sum(swap16_sum(l));
    if (hh == 0 && jj == 0) *(LAS float*)(lds + L_T2 + ((i16 >> 2) * 8 + w) * 4) = l;
  }
  lds_arrive(lds, L_CNT + C_SUM, lane);
  lds_wait_ge<0>(lds, L_CNT + C_SUM, NC);
  const int mbr = w >> 2, mg = w & 3;
  {
    const int ww = lane & 7;
    float M, L;
    if (mbr == 0) {
      M = oct_max(*(const LAS float*)(lds + L_T1 + (mg * 8 + ww) * 4)) * LOG2E;
      L = oct_sum(*(const LAS float*)(lds + L_T2 + (mg * 8 + ww) * 4));
    } else {
      const f32x2 ml = *(const LAS f32x2*)(lds + L_TB + (mg * 8 + ww) * 8);
      M = oct_max(ml[0]);
      L = oct_sum(ml[1] * __builtin_amdgcn_exp2f(ml[0] - M));
    }
    if (lane == 0) __builtin_amdgcn_raw_buffer_store_b128(u32x4{tagA, f2u(M), f2u(L), tagB}, rs, WS_ML + (((h * 2 + mbr) * 4 + mg) * 32 + j) * 16, 0, AUX_SC1);
  }
  STAMP(14);

  f32x4 a0 = {0.f, 0.f, 0.f, 0.f}, a1 = {0.f, 0.f, 0.f, 0.f};
  float fA = 0.f, fB = 0.f;
#pragma unroll
  for (int i = 0; i < 8; ++i) {
    if (i == 4) {
      const unsigned off = WS_ML + (((h * 2 + mbr) * 4 + mg) * 32 + (lane & 31)) * 16;
      u32x4 mv = {0u, 0u, 0u, 0u};
      bool ok = false;
      for (unsigned spin = 0; spin < SPIN_CAP; ++spin) {
        MEMFENCE();
        if (!ok) { mv = __builtin_amdgcn_raw_buffer_load_b128(rs, off, 0, AUX_SC1); ok = mv[0] == tagA && mv[3] == tagB; }
        if (__all(ok)) break;
        __builtin_amdgcn_s_sleep(16);
      }
      const float M = swap16_max(row_max(u2f(mv[1])));
      const float L = swap16_sum(row_sum(u2f(mv[2]) * __builtin_amdgcn_exp2f(u2f(mv[1]) - M)));
      if (lane == 0) *(LAS f32x2*)(lds + L_FF + w * 8) = f32x2{M, 1.0f / L};
      lds_arrive(lds, L_CNT + C_FF, lane);
    }
    if (i == 7) {
      lds_wait_ge<0>(lds, L_CNT + C_FF, NC);
      const f32x4 m0 = *(const LAS f32x4*)(lds + L_T1 + hh * 32), m1 = *(const LAS f32x4*)(lds + L_T1 + hh * 32 + 16);
      const float m2 = fmaxf(fmaxf(fmaxf(m0[0], m0[1]), fmaxf(m0[2], m0[3])), fmaxf(fmaxf(m1[0], m1[1]), fmaxf(m1[2], m1[3]))) * LOG2E;
      const f32x2 FA = *(const LAS f32x2*)(lds + L_FF + hh * 8), FB = *(const LAS f32x2*)(lds + L_FF + (4 + hh) * 8);
      const float mB = *(const LAS float*)(lds + L_TB + (hh * 8 + w) * 8);
      fA = __builtin_amdgcn_exp2f(m2 - FA[0]) * FA[1];
      fB = __builtin_amdgcn_exp2f(mB - FB[0]) * FB[1];
      const int s = lane < MRG_SLOTS ? lane : 0;
      const unsigned need = (unsigned)(s + NS * ((139 - s) / NS) + 1);
      for (unsigned spin = 0; spin < LSPIN_CAP; ++spin) { MEMFENCE(); if (__all(*(volatile LAS unsigned*)(lds + L_FREE + 4 * s) >= need)) break; }
      MEMFENCE();
      LAS float* rB = (LAS float*)(lds + L_MRG) + ((8 + w) * 4 + hh) * MREC + 8 * i16;
      *(LAS f32x4*)(rB) = b0 * fB; *(LAS f32x4*)(rB + 4) = b1 * fB;
    }
    fetch(9 + i, 2, X1);
    tile_pv(a0, a1, X1, P[2 * i], sel);
    tile_pv(a0, a1, X1 + 4, P[2 * i + 1], sel);
  }
  STAMP(3);
  if (w == 7) WSTAMP(11);

  {
    LAS float* rA = (LAS float*)(lds + L_MRG) + (w * 4 + hh) * MREC + 8 * i16;
    *(LAS f32x4*)(rA) = a0 * fA; *(LAS f32x4*)(rA + 4) = a1 * fA;
  }
  lds_arrive(lds, L_CNT + C_MRG, lane);
  lds_wait_ge<0>(lds, L_CNT + C_MRG, NC);
  {
    const int br = tid >> 8, g = (tid >> 6) & 3, dp = lane;
    const LAS float* mrg = (const LAS float*)(lds + L_MRG) + (br * 8 * 4 + g) * MREC + 2 * dp;
    f32x2 o2[8];
#pragma unroll
    for (int ww = 0; ww < 8; ++ww) o2[ww] = *(const LAS f32x2*)(mrg + ww * 4 * MREC);
    const f32x2 o = ((o2[0] + o2[1]) + (o2[2] + o2[3])) + ((o2[4] + o2[5]) + (o2[6] + o2[7]));
    const int jc = br * 16 + g * 4 + (dp >> 4), k = dp & 15;
    __builtin_amdgcn_raw_buffer_store_b128(u32x4{tagA, f2u(o[0]), f2u(o[1]), tagB}, rs, WS_I1 + (((h * 32 + jc) * 32 + j) * 16 + k) * 16, 0, AUX_SC1);
  }
  STAMP(4);

  {
    const unsigned offA = WS_I1 + ((h * 32 + j) * 32 * 16 + w * 64 + lane) * 16;
    u32x4 av = {0u, 0u, 0u, 0u};
    bool ok = false;
    for (unsigned spin = 0; spin < SPIN_CAP; ++spin) {
      MEMFENCE();
      if (!ok) { av = __builtin_amdgcn_raw_buffer_load_b128(rs, offA, 0, AUX_SC1); ok = av[0] == tagA && av[3] == tagB; }
      if (__all(ok)) break;
      if (PSLEEP) __builtin_amdgcn_s_sleep(PSLEEP);
    }
    STAMP(5);
    const float o0 = swap32_sum(swap16_sum(u2f(av[1]))), o1 = swap32_sum(swap16_sum(u2f(av[2])));
    if (lane < 16) *(LAS f32x2*)(lds + L_H1 + (w * 32 + 2 * lane) * 4) = f32x2{o0, o1};
  }
  LDS_BARRIER();
  {
    const float o = oct_sum(*(const LAS float*)(lds + L_H1 + ((lane & 7) * 32 + 4 * w + ((lane >> 3) & 3)) * 4));
    const unsigned ob = cvtpk(o, 0.f) << 16;
    const float orow[4] = {u2f(__builtin_amdgcn_readlane(ob, 0)), u2f(__builtin_amdgcn_readlane(ob, 8)), u2f(__builtin_amdgcn_readlane(ob, 16)), u2f(__builtin_amdgcn_readlane(ob, 24))};
    const int wseq = 140 + (w >> 1);
    lds_wait_ge<0>(lds, L_FULL + 4 * (wseq % NS), (unsigned)(wseq + 1));
    const LAS unsigned char* wp = lds + L_RING + (wseq % NS) * SLOT + (w & 1) * 4096 + lane * 16;
    float y[8];
#pragma unroll
    for (int k = 0; k < 8; ++k) y[k] = 0.f;
#pragma unroll
    for (int r4 = 0; r4 < 4; ++r4) {
      const u32x4 wv = *(const LAS u32x4*)(wp + r4 * 1024);
#pragma unroll
      for (int k = 0; k < 4; ++k) { y[2 * k] = fmaf(orow[r4], lo16(wv[k]), y[2 * k]); y[2 * k + 1] = fmaf(orow[r4], hi16(wv[k]), y[2 * k + 1]); }
    }
    LAS f32x4* yl = (LAS f32x4*)(lds + L_Y + w * 2048 + lane * 32);
    yl[0] = f32x4{y[0], y[1], y[2], y[3]}; yl[1] = f32x4{y[4], y[5], y[6], y[7]};
  }
  LDS_BARRIER();
  if (tid < 256) {
    const LAS unsigned char* yl = lds + L_Y + tid * 8;
    f32x2 s2[8];
#pragma unroll
    for (int k = 0; k < 8; ++k) s2[k] = *(const LAS f32x2*)(yl + k * 2048);
    const f32x2 s = ((s2[0] + s2[1]) + (s2[2] + s2[3])) + ((s2[4] + s2[5]) + (s2[6] + s2[7]));
    const int c = (j >> 4) * 32 + (tid >> 3), k = tid & 7, pidx = h * 16 + (j & 15);
    __builtin_amdgcn_raw_buffer_store_b128(u32x4{tagA, f2u(s[0]), f2u(s[1]), tagB}, rs, WS_I2 + ((c * 128 + pidx) * 8 + k) * 16, 0, AUX_SC1);
  }
  STAMP(6);

  if (j < 8) {
    const int c = h * 8 + j;
    const unsigned off = WS_I2 + (c * 128 + 16 * w) * 128 + lane * 16;
    u32x4 v[2];
    bool ok = false;
    for (unsigned spin = 0; spin < SPIN_CAP; ++spin) {
      MEMFENCE();
      if (!ok) {
#pragma unroll
        for (int i = 0; i < 2; ++i) v[i] = __builtin_amdgcn_raw_buffer_load_b128(rs, off + i * 1024, 0, AUX_SC1);
        ok = v[0][0] == tagA && v[0][3] == tagB && v[1][0] == tagA && v[1][3] == tagB;
      }
      if (__all(ok)) break;
      if (PSLEEP) __builtin_amdgcn_s_sleep(PSLEEP);
    }
    STAMP(7);
    float s0 = u2f(v[0][1]) + u2f(v[1][1]);
    float s1 = u2f(v[0][2]) + u2f(v[1][2]);
    s0 += dpp<ROR8>(s0); s1 += dpp<ROR8>(s1);
    s0 = swap32_sum(swap16_sum(s0)); s1 = swap32_sum(swap16_sum(s1));
    if (lane < 8) *(LAS f32x2*)(lds + L_F + w * 64 + lane * 8) = f32x2{s0, s1};
    LDS_BARRIER();
    if (tid < 8) {
      f32x2 s = *(const LAS f32x2*)(lds + L_F + tid * 8);
#pragma unroll
      for (int ww = 1; ww < 8; ++ww) s += *(const LAS f32x2*)(lds + L_F + ww * 64 + tid * 8);
      *(GAS unsigned*)((GAS unsigned char*)a.out + (c * 16 + 2 * tid) * 2) = cvtpk(s[0], s[1]);
      if (c == 0 && tid == 0) __builtin_amdgcn_raw_buffer_store_b64(u32x2{tagA, tagB}, rs, WS_GEN, 0, AUX_SC1);
    }
  }
  DUMP_STAMPS();
}

extern "C" void kernel_launch(void* const* d_in, const int* in_sizes, int n_in, void* d_out, int out_size, void* d_ws, size_t ws_size, hipStream_t stream) {
  Args a;
  a.xA = (const unsigned char*)d_in[0]; a.kA = (const unsigned char*)d_in[1]; a.vA = (const unsigned char*)d_in[2]; a.wqA = (const unsigned char*)d_in[3]; a.woA = (const unsigned char*)d_in[4];
  a.xB = (const unsigned char*)d_in[5]; a.kB = (const unsigned char*)d_in[6]; a.vB = (const unsigned char*)d_in[7]; a.wqB = (const unsigned char*)d_in[8]; a.woB = (const unsigned char*)d_in[9];
  a.out = (unsigned short*)d_out;
  a.ws = (unsigned char*)d_ws;
  fprintf(stderr, "ptrs: x %p kA %p vA %p wqA %p woA %p kB %p vB %p wqB %p woB %p out %p ws %p\n", d_in[0], d_in[1], d_in[2], d_in[3], d_in[4], d_in[6], d_in[7], d_in[8], d_in[9], d_out, d_ws);
  hipLaunchKernelGGL(fused_decode, dim3(NWG), dim3(NTHR), 0, stream, a);
}
```

```cpp
#include <hip/hip_runtime.h>
#include <hip/hip_bf16.h>
#include <stdint.h>

#define GAS __attribute__((address_space(1)))
#define LAS __attribute__((address_space(3)))
typedef unsigned u32x4 __attribute__((ext_vector_type(4)));
typedef unsigned u32x2 __attribute__((ext_vector_type(2)));
typedef float f32x4 __attribute__((ext_vector_type(4)));
typedef float f32x2 __attribute__((ext_vector_type(2)));
typedef __bf16 bf16x8 __attribute__((ext_vector_type(8)));
typedef __bf16 bf16x2 __attribute__((ext_vector_type(2)));
typedef short s16x4 __attribute__((ext_vector_type(4)));

#define AMPI -1
#define NS 18
#define WIN_LD 22
#define PRE 2
#define QSLEEP 16
#define PSLEEP 0
#define BENCH 0
#define STAMPS 1

constexpr int SK = 65536, WIN = 4096;
constexpr int NWG = 256, NC = 8, NTHR = (NC + 1) * 64;
constexpr int SLOT = 8192, NSEQ = 4 + 8 + 64 + 64 + 4;
constexpr int LAG = (WIN_LD + 7) / 8;
constexpr float LOG2E = 1.4426950408889634f;
constexpr float SCALE = 0.08838834764831845f;
constexpr unsigned SPIN_CAP = 1u << 20, LSPIN_CAP = 1u << 24;

constexpr unsigned WS_GEN = 0;
constexpr unsigned WS_QG = 4096;
constexpr unsigned WS_ML = WS_QG + 8 * 32 * 32 * 16;
constexpr unsigned WS_I1 = WS_ML + 256 * 128;
constexpr unsigned WS_I2 = WS_I1 + 8 * 32 * 32 * 256;
constexpr unsigned WS_END = WS_I2 + 64 * 128 * 128;
constexpr unsigned WS_STAMP = WS_END;

constexpr int L_RING = 0;
constexpr int L_FULL = NS * SLOT;
constexpr int L_FREE = L_FULL + 128;
constexpr int L_CNT = L_FREE + 128;
constexpr int L_RED = L_CNT + 64;
constexpr int L_Q = L_RED + 2048;
constexpr int L_T1 = L_Q + 2048;
constexpr int L_T2 = L_T1 + 128;
constexpr int L_TB = L_T2 + 128;
constexpr int L_FF = L_TB + 256;
constexpr int L_H1 = L_FF + 64;
constexpr int L_F = L_H1 + 8 * 32 * 4;
constexpr int L_END = L_F + 512 + 128;
constexpr int C_QRED = 0, C_QIN = 4, C_MAX = 8, C_SUM = 12, C_FF = 16, C_MRG = 20;
constexpr int MREC = 132;
constexpr int L_MRG = 0;
constexpr int MRG_SLOTS = 5;
constexpr int L_Y = 5 * SLOT;
static_assert(L_END <= 160 * 1024, "LDS");
static_assert(2 * 8 * 4 * MREC * 4 <= MRG_SLOTS * SLOT, "merge area");
static_assert((140 % NS) >= 7 && (143 % NS) >= 7 && (140 % NS) <= (143 % NS), "wo slots clear of the merge / y areas");
static_assert((132 % NS) >= MRG_SLOTS && (139 % NS) >= MRG_SLOTS && (132 % NS) <= (139 % NS), "last round clear of the merge area");
static_assert(WIN_LD >= 9 && WIN_LD <= 48, "window");

__device__ __forceinline__ unsigned cvtpk(float lo, float hi) { f32x2 f = {lo, hi}; return __builtin_bit_cast(unsigned, __builtin_convertvector(f, bf16x2)); }
__device__ __forceinline__ float lo16(unsigned u) { return __builtin_bit_cast(float, u << 16); }
__device__ __forceinline__ float hi16(unsigned u) { return __builtin_bit_cast(float, u & 0xffff0000u); }
__device__ __forceinline__ float u2f(unsigned u) { return __builtin_bit_cast(float, u); }
__device__ __forceinline__ unsigned f2u(float f) { return __builtin_bit_cast(unsigned, f); }
template <int CTRL> __device__ __forceinline__ float dpp(float x) { return u2f(__builtin_amdgcn_mov_dpp(f2u(x), CTRL, 0xf, 0xf, true)); }
constexpr int XOR1 = 0xB1, XOR2 = 0x4E, HMIR = 0x141;
constexpr int ROR1 = 0x121, ROR2 = 0x122, ROR4 = 0x124, ROR8 = 0x128;
__device__ __forceinline__ float swap16_max(float x) { auto s = __builtin_amdgcn_permlane16_swap(f2u(x), f2u(x), false, false); return fmaxf(u2f(s[0]), u2f(s[1])); }
__device__ __forceinline__ float swap32_max(float x) { auto s = __builtin_amdgcn_permlane32_swap(f2u(x), f2u(x), false, false); return fmaxf(u2f(s[0]), u2f(s[1])); }
__device__ __forceinline__ float swap16_sum(float x) { auto s = __builtin_amdgcn_permlane16_swap(f2u(x), f2u(x), false, false); return u2f(s[0]) + u2f(s[1]); }
__device__ __forceinline__ float swap32_sum(float x) { auto s = __builtin_amdgcn_permlane32_swap(f2u(x), f2u(x), false, false); return u2f(s[0]) + u2f(s[1]); }
__device__ __forceinline__ float row_sum(float x) { x += dpp<ROR8>(x); x += dpp<ROR4>(x); x += dpp<ROR2>(x); x += dpp<ROR1>(x); return x; }
__device__ __forceinline__ float row_max(float x) { x = fmaxf(x, dpp<ROR8>(x)); x = fmaxf(x, dpp<ROR4>(x)); x = fmaxf(x, dpp<ROR2>(x)); x = fmaxf(x, dpp<ROR1>(x)); return x; }
__device__ __forceinline__ float oct_sum(float x) { x += dpp<XOR1>(x); x += dpp<XOR2>(x); x += dpp<HMIR>(x); return x; }
__device__ __forceinline__ float oct_max(float x) { x = fmaxf(x, dpp<XOR1>(x)); x = fmaxf(x, dpp<XOR2>(x)); x = fmaxf(x, dpp<HMIR>(x)); return x; }
#define LDS_BARRIER() asm volatile("s_waitcnt lgkmcnt(0)\n\ts_barrier" ::: "memory")
#define LDS_WAIT() asm volatile("s_waitcnt lgkmcnt(0)" ::: "memory")
#define MEMFENCE() asm volatile("" ::: "memory")
constexpr int AUX_SC1 = 16;

struct Args {
  const unsigned char *xA, *kA, *vA, *wqA, *woA, *xB, *kB, *vB, *wqB, *woB;
  unsigned short* out;
  unsigned char* ws;
};

__device__ __forceinline__ unsigned lds_ld(LAS unsigned char* L, int off) { return __builtin_amdgcn_readfirstlane(*(volatile LAS unsigned*)(L + off)); }
__device__ __forceinline__ void lds_st(LAS unsigned char* L, int off, unsigned v) { *(volatile LAS unsigned*)(L + off) = v; }
template <int SLEEP> __device__ __forceinline__ void lds_wait_ge(LAS unsigned char* L, int off, unsigned need) {
  for (unsigned spin = 0; lds_ld(L, off) < need && spin < LSPIN_CAP; ++spin) { if (SLEEP) __builtin_amdgcn_s_sleep(SLEEP); }
  MEMFENCE();
}
__device__ __forceinline__ void lds_arrive(LAS unsigned char* L, int off, int lane) {
  LDS_WAIT();
  if (lane == 0) __hip_atomic_fetch_add((LAS unsigned*)(L + off), 1u, __ATOMIC_RELAXED, __HIP_MEMORY_SCOPE_WORKGROUP);
}

__device__ __forceinline__ void dma1(unsigned dst, unsigned voff, const unsigned char* base) {
  unsigned keep;
  asm volatile("s_mov_b32 %0, m0\n\ts_mov_b32 m0, %1\n\ts_nop 0\n\tglobal_load_lds_dwordx4 %2, %3 nt\n\ts_mov_b32 m0, %0\n\ts_waitcnt vmcnt(%4)"
               : "=&s"(keep) : "s"(dst), "v"(voff), "s"(base), "i"(WIN_LD) : "memory");
}
__device__ __forceinline__ void dma4(unsigned dst, unsigned v0, unsigned v1, unsigned v2, unsigned v3, const unsigned char* base) {
  unsigned keep;
  asm volatile("s_mov_b32 %0, m0\n\ts_mov_b32 m0, %1\n\ts_nop 0\n\t"
               "global_load_lds_dwordx4 %2, %6 nt\n\ts_waitcnt vmcnt(%7)\n\t"
               "global_load_lds_dwordx4 %3, %6 offset:1024 nt\n\ts_waitcnt vmcnt(%7)\n\t"
               "global_load_lds_dwordx4 %4, %6 offset:2048 nt\n\ts_waitcnt vmcnt(%7)\n\t"
               "global_load_lds_dwordx4 %5, %6 offset:3072 nt\n\ts_waitcnt vmcnt(%7)\n\t"
               "s_mov_b32 m0, %0"
               : "=&s"(keep) : "s"(dst), "v"(v0), "v"(v1), "v"(v2), "v"(v3), "s"(base), "i"(WIN_LD) : "memory");
}
template <int N> __device__ __forceinline__ void vm_wait() { asm volatile("s_waitcnt vmcnt(%0)" :: "i"(N) : "memory"); }

__device__ __forceinline__ u32x2 tile_scores(const u32x4* K, const u32x4 (&q)[4]) {
  f32x4 s = {0.f, 0.f, 0.f, 0.f};
#pragma unroll
  for (int i = 0; i < 4; ++i) s = __builtin_amdgcn_mfma_f32_16x16x32_bf16(__builtin_bit_cast(bf16x8, K[i]), __builtin_bit_cast(bf16x8, q[i]), s, 0, 0, 0);
  const unsigned r01 = cvtpk(s[0], s[1]), r23 = cvtpk(s[2], s[3]);
  return u32x2{cvtpk(lo16(r01) * SCALE, hi16(r01) * SCALE), cvtpk(lo16(r23) * SCALE, hi16(r23) * SCALE)};
}
__device__ __forceinline__ float max4(u32x2 t) { return fmaxf(fmaxf(lo16(t[0]), hi16(t[0])), fmaxf(lo16(t[1]), hi16(t[1]))); }
__device__ __forceinline__ u32x2 to_probs(u32x2 t, float m2, float& l) {
  const float p0 = __builtin_amdgcn_exp2f(fmaf(lo16(t[0]), LOG2E, -m2)), p1 = __builtin_amdgcn_exp2f(fmaf(hi16(t[0]), LOG2E, -m2));
  const float p2 = __builtin_amdgcn_exp2f(fmaf(lo16(t[1]), LOG2E, -m2)), p3 = __builtin_amdgcn_exp2f(fmaf(hi16(t[1]), LOG2E, -m2));
  l += (p0 + p1) + (p2 + p3);
  return u32x2{cvtpk(p0, p1), cvtpk(p2, p3)};
}
struct Sel { unsigned l0, l1, h0, h1; };
__device__ __forceinline__ void tile_pv(f32x4& a0, f32x4& a1, const u32x4* V, u32x2 p, const Sel& s) {
#pragma unroll
  for (int r = 0; r < 4; ++r) {
    const unsigned pk = p[r >> 1];
    const u32x2 a = {__builtin_amdgcn_perm(0u, pk, (r & 1) ? s.h0 : s.l0), __builtin_amdgcn_perm(0u, pk, (r & 1) ? s.h1 : s.l1)};
    const u32x2 v0 = {V[r][0], V[r][1]}, v1 = {V[r][2], V[r][3]};
    a0 = __builtin_amdgcn_mfma_f32_16x16x16bf16_1k(__builtin_bit_cast(s16x4, a), __builtin_bit_cast(s16x4, v0), a0, 0, 0, 0);
    a1 = __builtin_amdgcn_mfma_f32_16x16x16bf16_1k(__builtin_bit_cast(s16x4, a), __builtin_bit_cast(s16x4, v1), a1, 0, 0, 0);
  }
}

#define STAMP(i) do { if (tid == 0) { ((volatile LAS unsigned long long*)(lds + L_END - 128))[(i)] = __builtin_amdgcn_s_memrealtime(); } } while (0)
#define WSTAMP(i) do { if (lane == 0) { ((volatile LAS unsigned long long*)(lds + L_END - 128))[(i)] = __builtin_amdgcn_s_memrealtime(); } } while (0)
#define DUMP_STAMPS() do { if (AMPI >= 0 && b == 0 && tid == 0) { const unsigned long long t_end = __builtin_amdgcn_s_memrealtime(); \
    const unsigned long long d = (AMPI == 8 ? t_end : ((volatile LAS unsigned long long*)(lds + L_END - 128))[AMPI >= 0 ? AMPI : 0]) - t_start; \
    for (unsigned sp_ = 0; sp_ < 10000000u && __builtin_amdgcn_s_memrealtime() < t_end + 4 * d; ++sp_) __builtin_amdgcn_s_sleep(1); } } while (0)

__global__ void __launch_bounds__(NTHR, 3) fused_decode(Args a) {
  __shared__ __attribute__((aligned(16))) unsigned char lds_raw[L_END];
  LAS unsigned char* lds = (LAS unsigned char*)lds_raw;
  const int b = blockIdx.x, h = b & 7, j = b >> 3;
  const int tid = threadIdx.x, lane = tid & 63, w = __builtin_amdgcn_readfirstlane(tid >> 6);
  const int hh = lane >> 4, i16 = lane & 15, jj = lane & 3;
  const unsigned long long t_start = __builtin_amdgcn_s_memrealtime();
  const int qcb = j >> 1, qrh = j & 1, qbr = qcb >> 3;

  if (w == NC) {
    const unsigned ring = (unsigned)(size_t)(lds + L_RING);
    int pub = 0, pslot = 0, slot = 0;
    auto publish1 = [&]() { lds_st(lds, L_FULL + 4 * pslot, (unsigned)(pub + 1)); ++pub; pslot = pslot + 1 == NS ? 0 : pslot + 1; };
    auto pre = [&](int q) {
      if (q >= NS) {
        const unsigned need = (unsigned)(q - NS + 1);
        if (lds_ld(lds, L_FREE + 4 * slot) < need) {
          vm_wait<0>();
          while (pub < q) publish1();
          lds_wait_ge<1>(lds, L_FREE + 4 * slot, need);
        }
      }
    };
    auto post = [&](int q) {
      if (q - pub >= LAG) publish1();
      slot = slot + 1 == NS ? 0 : slot + 1;
    };
    {
      const unsigned char* wq = (qbr ? a.wqB : a.wqA) + (size_t)(h * 512 + 64 * (qcb & 7)) * 2 + (size_t)qrh * 256 * 8192;
      const unsigned vo = (lane >> 3) * 8192 + (lane & 7) * 16;
#pragma unroll
      for (int q = 0; q < 4; ++q) {
#pragma unroll
        for (int m = 0; m < 8; ++m) dma1(ring + q * SLOT + m * 1024, vo, wq + (size_t)(q * 64 + m * 8) * 8192);
        if (q == 0) LDS_BARRIER();
        post(q);
      }
    }
    const unsigned k0 = hh * 256 + ((i16 ^ (0 + hh)) * 16), k1 = hh * 256 + ((i16 ^ (4 + hh)) * 16), k2 = hh * 256 + ((i16 ^ (8 + hh)) * 16), k3 = hh * 256 + ((i16 ^ (12 + hh)) * 16);
    const unsigned vl = lane * 16;
    {
      const unsigned char* kp = a.kB + ((size_t)h * SK + (SK - WIN) + j * 128) * 256;
      const unsigned char* vp = a.vB + ((size_t)h * SK + (SK - WIN) + j * 128) * 256;
      for (int q = 4; q < 12; ++q) {
        dma4(ring + slot * SLOT, k0, k1, k2, k3, kp);
        dma4(ring + slot * SLOT + 4096, vl, vl, vl, vl, vp);
        kp += 4096; vp += 4096;
        post(q);
      }
      kp = a.kA + ((size_t)h * SK + j * 2048) * 256;
      for (int q = 12; q < 76; ++q) {
        pre(q);
        dma4(ring + slot * SLOT, k0, k1, k2, k3, kp);
        dma4(ring + slot * SLOT + 4096, k0, k1, k2, k3, kp + 4096);
        kp += 8192;
        post(q);
      }
      WSTAMP(12);
      vp = a.vA + ((size_t)h * SK + j * 2048) * 256;
      for (int q = 76; q < 140; ++q) {
        pre(q);
        dma4(ring + slot * SLOT, vl, vl, vl, vl, vp);
        dma4(ring + slot * SLOT + 4096, vl, vl, vl, vl, vp + 4096);
        vp += 8192;
        post(q);
      }
      WSTAMP(9);
      vp = (j >> 4 ? a.woB : a.woA) + (size_t)(h * 512 + 32 * (j & 15)) * 1024;
      for (int q = 140; q < 144; ++q) {
        pre(q);
        dma4(ring + slot * SLOT, vl, vl, vl, vl, vp);
        dma4(ring + slot * SLOT + 4096, vl, vl, vl, vl, vp + 4096);
        vp += 8192;
        post(q);
      }
    }
    if (NSEQ - pub > 6) { vm_wait<48>(); publish1(); }
    if (NSEQ - pub > 5) { vm_wait<40>(); publish1(); }
    if (NSEQ - pub > 4) { vm_wait<32>(); publish1(); }
    if (NSEQ - pub > 3) { vm_wait<24>(); publish1(); }
    if (NSEQ - pub > 2) { vm_wait<16>(); publish1(); }
    if (NSEQ - pub > 1) { vm_wait<8>(); publish1(); }
    vm_wait<0>();
    while (pub < NSEQ) publish1();
    WSTAMP(10);
    return;
  }

  const __amdgpu_buffer_rsrc_t rs = __builtin_amdgcn_make_buffer_rsrc((void*)a.ws, 0, (int)WS_END, 0x00020000);
  const u32x2 gen = __builtin_amdgcn_raw_buffer_load_b64(rs, WS_GEN, 0, AUX_SC1);
  unsigned short xv[4];
  {
    const GAS unsigned char* xp = (const GAS unsigned char*)(qbr ? a.xB : a.xA) + 2 * (qrh * 256 + w * 32 + (lane >> 3));
#pragma unroll
    for (int i = 0; i < 4; ++i) xv[i] = *(const GAS unsigned short*)(xp + 16 * i);
  }
  if (tid < (L_RED - L_FULL) / 4) *(LAS unsigned*)(lds + L_FULL + 4 * tid) = 0u;
  if (tid >= 128 && tid < 160) *(LAS unsigned*)(lds + L_END - 128 + 4 * (tid - 128)) = 0u;
  LDS_BARRIER();

  lds_wait_ge<1>(lds, L_FULL + 4 * (w >> 1), (unsigned)((w >> 1) + 1));
  {
    u32x4 wv[4];
#pragma unroll
    for (int i = 0; i < 4; ++i) wv[i] = *(const LAS u32x4*)(lds + L_RING + (w >> 1) * SLOT + (w & 1) * 4096 + i * 1024 + lane * 16);
    float acc[8];
#pragma unroll
    for (int k = 0; k < 8; ++k) acc[k] = 0.f;
#pragma unroll
    for (int i = 0; i < 4; ++i) {
      const float x = lo16(xv[i]);
#pragma unroll
      for (int k = 0; k < 4; ++k) { acc[2 * k] = fmaf(x, lo16(wv[i][k]), acc[2 * k]); acc[2 * k + 1] = fmaf(x, hi16(wv[i][k]), acc[2 * k + 1]); }
    }
#pragma unroll
    for (int k = 0; k < 8; ++k) { float v = acc[k]; v += dpp<ROR8>(v); v = swap16_sum(v); v = swap32_sum(v); acc[k] = v; }
    if (lane < 8) {
      LAS f32x4* red = (LAS f32x4*)(lds + L_RED + w * 256 + lane * 32);
      red[0] = f32x4{acc[0], acc[1], acc[2], acc[3]}; red[1] = f32x4{acc[4], acc[5], acc[6], acc[7]};
    }
  }
  lds_arrive(lds, L_CNT + C_QRED, lane);
  const unsigned tagA = __builtin_amdgcn_readfirstlane(gen[0]) + 1u, tagB = __builtin_amdgcn_readfirstlane(gen[1]) + 1u;
  if (w == 0) {
    lds_wait_ge<0>(lds, L_CNT + C_QRED, NC);
    if (lane < 4) lds_st(lds, L_FREE + 4 * lane, (unsigned)(lane + 1));
    if (lane < 32) {
      f32x2 s = {0.f, 0.f};
#pragma unroll
      for (int ww = 0; ww < 8; ++ww) s += *(const LAS f32x2*)(lds + L_RED + ww * 256 + lane * 8);
      __builtin_amdgcn_raw_buffer_store_b128(u32x4{tagA, f2u(s[0]), f2u(s[1]), tagB}, rs, WS_QG + ((h * 32 + j) * 32 + lane) * 16, 0, AUX_SC1);
    }
  }
  STAMP(1);
  int slot = (4 + w) % NS;
  const unsigned kr0 = i16 * 256 + (((0 + hh) ^ i16) * 16), kr1 = i16 * 256 + (((4 + hh) ^ i16) * 16), kr2 = i16 * 256 + (((8 + hh) ^ i16) * 16), kr3 = i16 * 256 + (((12 + hh) ^ i16) * 16);
  const unsigned vr = hh * 1024 + i16 * 16;
  auto fetch = [&](int it, int kind, u32x4 (&T)[8]) {
    const unsigned seq1 = (unsigned)(4 + 8 * it + w + 1);
    lds_wait_ge<1>(lds, L_FULL + 4 * slot, seq1);
    const LAS unsigned char* sp = lds + L_RING + slot * SLOT;
    if (kind != 2) { T[0] = *(const LAS u32x4*)(sp + kr0); T[1] = *(const LAS u32x4*)(sp + kr1); T[2] = *(const LAS u32x4*)(sp + kr2); T[3] = *(const LAS u32x4*)(sp + kr3); }
    else {
#pragma unroll
      for (int r = 0; r < 4; ++r) T[r] = *(const LAS u32x4*)(sp + vr + r * 256);
    }
    if (kind == 1) { T[4] = *(const LAS u32x4*)(sp + 4096 + kr0); T[5] = *(const LAS u32x4*)(sp + 4096 + kr1); T[6] = *(const LAS u32x4*)(sp + 4096 + kr2); T[7] = *(const LAS u32x4*)(sp + 4096 + kr3); }
    else {
#pragma unroll
      for (int r = 0; r < 4; ++r) T[4 + r] = *(const LAS u32x4*)(sp + 4096 + vr + r * 256);
    }
    LDS_WAIT();
    lds_st(lds, L_FREE + 4 * slot, seq1);
    asm volatile("" : "+v"(T[0]), "+v"(T[1]), "+v"(T[2]), "+v"(T[3]), "+v"(T[4]), "+v"(T[5]), "+v"(T[6]), "+v"(T[7]) :: "memory");
    slot += 8; if (slot >= NS) slot -= NS;
  };
  u32x4 X0[8], X1[8], X2[8];
  fetch(0, 0, X0);
  if (PRE >= 2) fetch(1, 1, X1);
  if (PRE >= 3) fetch(2, 1, X2);
  {
    const unsigned off = WS_QG + ((h * 32 + 2 * (tid >> 5)) * 32 + (tid & 31)) * 16;
    u32x4 g0 = {0u, 0u, 0u, 0u}, g1 = {0u, 0u, 0u, 0u};
    bool ok = false;
    for (unsigned spin = 0; spin < SPIN_CAP; ++spin) {
      MEMFENCE();
      if (!ok) {
        g0 = __builtin_amdgcn_raw_buffer_load_b128(rs, off, 0, AUX_SC1);
        g1 = __builtin_amdgcn_raw_buffer_load_b128(rs, off + 512, 0, AUX_SC1);
        ok = g0[0] == tagA && g0[3] == tagB && g1[0] == tagA && g1[3] == tagB;
      }
      if (__all(ok)) break;
      if (QSLEEP) __builtin_amdgcn_s_sleep(QSLEEP);
    }
    *(LAS unsigned*)(lds + L_Q + tid * 4) = cvtpk(u2f(g0[1]) + u2f(g1[1]), u2f(g0[2]) + u2f(g1[2]));
  }
  lds_arrive(lds, L_CNT + C_QIN, lane);
  lds_wait_ge<0>(lds, L_CNT + C_QIN, NC);
  u32x4 qA[4], qB[4];
#pragma unroll
  for (int s = 0; s < 4; ++s) {
    qA[s] = *(const LAS u32x4*)(lds + L_Q + ((i16 >> 2) * 128 + 32 * s + 8 * hh) * 2);
    qB[s] = *(const LAS u32x4*)(lds + L_Q + 1024 + ((i16 >> 2) * 128 + 32 * s + 8 * hh) * 2);
  }
  STAMP(2);

  Sel sel;
  sel.l0 = jj == 0 ? 0x0c0c0100u : jj == 1 ? 0x01000c0cu : 0x0c0c0c0cu;  sel.h0 = jj == 0 ? 0x0c0c0302u : jj == 1 ? 0x03020c0cu : 0x0c0c0c0cu;
  sel.l1 = jj == 2 ? 0x0c0c0100u : jj == 3 ? 0x01000c0cu : 0x0c0c0c0cu;  sel.h1 = jj == 2 ? 0x0c0c0302u : jj == 3 ? 0x03020c0cu : 0x0c0c0c0cu;

  f32x4 b0 = {0.f, 0.f, 0.f, 0.f}, b1 = {0.f, 0.f, 0.f, 0.f};
  {
    const u32x2 t = tile_scores(X0, qB);
    const float m2 = swap32_max(swap16_max(max4(t))) * LOG2E;
    float l = 0.f;
    const u32x2 p = to_probs(t, m2, l);
    l = swap32_sum(swap16_sum(l));
    tile_pv(b0, b1, X0 + 4, p, sel);
    if (hh == 0 && jj == 0) *(LAS f32x2*)(lds + L_TB + ((i16 >> 2) * 8 + w) * 8) = f32x2{m2, l};
  }

  u32x2 P[16];
  float mx = -INFINITY;
#pragma unroll
  for (int i = 0; i < 8; ++i) {
    if (i == 0) { if (PRE < 2) fetch(1, 1, X1); }
    else if (i == 1) { if (PRE < 3) fetch(2, 1, X2); }
    else fetch(i + 1, 1, X1);
    const u32x4* T = i == 1 ? X2 : X1;
    P[2 * i] = tile_scores(T, qA);
    P[2 * i + 1] = tile_scores(T + 4, qA);
    mx = fmaxf(mx, fmaxf(max4(P[2 * i]), max4(P[2 * i + 1])));
  }
  STAMP(13);
  mx = swap32_max(swap16_max(mx));
  if (hh == 0 && jj == 0) *(LAS float*)(lds + L_T1 + ((i16 >> 2) * 8 + w) * 4) = mx;
  lds_arrive(lds, L_CNT + C_MAX, lane);
  lds_wait_ge<0>(lds, L_CNT + C_MAX, NC);
  {
    const f32x4 m0 = *(const LAS f32x4*)(lds + L_T1 + (i16 >> 2) * 32), m1 = *(const LAS f32x4*)(lds + L_T1 + (i16 >> 2) * 32 + 16);
    const float m2 = fmaxf(fmaxf(fmaxf(m0[0], m0[1]), fmaxf(m0[2], m0[3])), fmaxf(fmaxf(m1[0], m1[1]), fmaxf(m1[2], m1[3]))) * LOG2E;
    float l = 0.f;
#pragma unroll
    for (int t = 0; t < 16; ++t) P[t] = to_probs(P[t], m2, l);
    l = swap32_sum(swap16_sum(l));
    if (hh == 0 && jj == 0) *(LAS float*)(lds + L_T2 + ((i16 >> 2) * 8 + w) * 4) = l;
  }
  lds_arrive(lds, L_CNT + C_SUM, lane);
  lds_wait_ge<0>(lds, L_CNT + C_SUM, NC);
  const int mbr = w >> 2, mg = w & 3;
  {
    const int ww = lane & 7;
    float M, L;
    if (mbr == 0) {
      M = oct_max(*(const LAS float*)(lds + L_T1 + (mg * 8 + ww) * 4)) * LOG2E;
      L = oct_sum(*(const LAS float*)(lds + L_T2 + (mg * 8 + ww) * 4));
    } else {
      const f32x2 ml = *(const LAS f32x2*)(lds + L_TB + (mg * 8 + ww) * 8);
      M = oct_max(ml[0]);
      L = oct_sum(ml[1] * __builtin_amdgcn_exp2f(ml[0] - M));
    }
    if (lane == 0) __builtin_amdgcn_raw_buffer_store_b128(u32x4{tagA, f2u(M), f2u(L), tagB}, rs, WS_ML + (((h * 2 + mbr) * 4 + mg) * 32 + j) * 16, 0, AUX_SC1);
  }
  STAMP(14);

  f32x4 a0 = {0.f, 0.f, 0.f, 0.f}, a1 = {0.f, 0.f, 0.f, 0.f};
  float fA = 0.f, fB = 0.f;
#pragma unroll
  for (int i = 0; i < 8; ++i) {
    if (i == 4) {
      const unsigned off = WS_ML + (((h * 2 + mbr) * 4 + mg) * 32 + (lane & 31)) * 16;
      u32x4 mv = {0u, 0u, 0u, 0u};
      bool ok = false;
      for (unsigned spin = 0; spin < SPIN_CAP; ++spin) {
        MEMFENCE();
        if (!ok) { mv = __builtin_amdgcn_raw_buffer_load_b128(rs, off, 0, AUX_SC1); ok = mv[0] == tagA && mv[3] == tagB; }
        if (__all(ok)) break;
        __builtin_amdgcn_s_sleep(16);
      }
      const float M = swap16_max(row_max(u2f(mv[1])));
      const float L = swap16_sum(row_sum(u2f(mv[2]) * __builtin_amdgcn_exp2f(u2f(mv[1]) - M)));
      if (lane == 0) *(LAS f32x2*)(lds + L_FF + w * 8) = f32x2{M, 1.0f / L};
      lds_arrive(lds, L_CNT + C_FF, lane);
    }
    if (i == 7) {
      lds_wait_ge<0>(lds, L_CNT + C_FF, NC);
      const f32x4 m0 = *(const LAS f32x4*)(lds + L_T1 + hh * 32), m1 = *(const LAS f32x4*)(lds + L_T1 + hh * 32 + 16);
      const float m2 = fmaxf(fmaxf(fmaxf(m0[0], m0[1]), fmaxf(m0[2], m0[3])), fmaxf(fmaxf(m1[0], m1[1]), fmaxf(m1[2], m1[3]))) * LOG2E;
      const f32x2 FA = *(const LAS f32x2*)(lds + L_FF + hh * 8), FB = *(const LAS f32x2*)(lds + L_FF + (4 + hh) * 8);
      const float mB = *(const LAS float*)(lds + L_TB + (hh * 8 + w) * 8);
      fA = __builtin_amdgcn_exp2f(m2 - FA[0]) * FA[1];
      fB = __builtin_amdgcn_exp2f(mB - FB[0]) * FB[1];
      const int s = lane < MRG_SLOTS ? lane : 0;
      const unsigned need = (unsigned)(s + NS * ((139 - s) / NS) + 1);
      for (unsigned spin = 0; spin < LSPIN_CAP; ++spin) { MEMFENCE(); if (__all(*(volatile LAS unsigned*)(lds + L_FREE + 4 * s) >= need)) break; }
      MEMFENCE();
      LAS float* rB = (LAS float*)(lds + L_MRG) + ((8 + w) * 4 + hh) * MREC + 8 * i16;
      *(LAS f32x4*)(rB) = b0 * fB; *(LAS f32x4*)(rB + 4) = b1 * fB;
    }
    fetch(9 + i, 2, X1);
    tile_pv(a0, a1, X1, P[2 * i], sel);
    tile_pv(a0, a1, X1 + 4, P[2 * i + 1], sel);
  }
  STAMP(3);
  if (w == 7) WSTAMP(11);

  {
    LAS float* rA = (LAS float*)(lds + L_MRG) + (w * 4 + hh) * MREC + 8 * i16;
    *(LAS f32x4*)(rA) = a0 * fA; *(LAS f32x4*)(rA + 4) = a1 * fA;
  }
  lds_arrive(lds, L_CNT + C_MRG, lane);
  lds_wait_ge<0>(lds, L_CNT + C_MRG, NC);
  {
    const int br = tid >> 8, g = (tid >> 6) & 3, dp = lane;
    const LAS float* mrg = (const LAS float*)(lds + L_MRG) + (br * 8 * 4 + g) * MREC + 2 * dp;
    f32x2 o2[8];
#pragma unroll
    for (int ww = 0; ww < 8; ++ww) o2[ww] = *(const LAS f32x2*)(mrg + ww * 4 * MREC);
    const f32x2 o = ((o2[0] + o2[1]) + (o2[2] + o2[3])) + ((o2[4] + o2[5]) + (o2[6] + o2[7]));
    const int jc = br * 16 + g * 4 + (dp >> 4), k = dp & 15;
    __builtin_amdgcn_raw_buffer_store_b128(u32x4{tagA, f2u(o[0]), f2u(o[1]), tagB}, rs, WS_I1 + (((h * 32 + jc) * 32 + j) * 16 + k) * 16, 0, AUX_SC1);
  }
  STAMP(4);

  {
    const unsigned offA = WS_I1 + ((h * 32 + j) * 32 * 16 + w * 64 + lane) * 16;
    u32x4 av = {0u, 0u, 0u, 0u};
    bool ok = false;
    for (unsigned spin = 0; spin < SPIN_CAP; ++spin) {
      MEMFENCE();
      if (!ok) { av = __builtin_amdgcn_raw_buffer_load_b128(rs, offA, 0, AUX_SC1); ok = av[0] == tagA && av[3] == tagB; }
      if (__all(ok)) break;
      if (PSLEEP) __builtin_amdgcn_s_sleep(PSLEEP);
    }
    STAMP(5);
    const float o0 = swap32_sum(swap16_sum(u2f(av[1]))), o1 = swap32_sum(swap16_sum(u2f(av[2])));
    if (lane < 16) *(LAS f32x2*)(lds + L_H1 + (w * 32 + 2 * lane) * 4) = f32x2{o0, o1};
  }
  LDS_BARRIER();
  {
    const float o = oct_sum(*(const LAS float*)(lds + L_H1 + ((lane & 7) * 32 + 4 * w + ((lane >> 3) & 3)) * 4));
    const unsigned ob = cvtpk(o, 0.f) << 16;
    const float orow[4] = {u2f(__builtin_amdgcn_readlane(ob, 0)), u2f(__builtin_amdgcn_readlane(ob, 8)), u2f(__builtin_amdgcn_readlane(ob, 16)), u2f(__builtin_amdgcn_readlane(ob, 24))};
    const int wseq = 140 + (w >> 1);
    lds_wait_ge<0>(lds, L_FULL + 4 * (wseq % NS), (unsigned)(wseq + 1));
    const LAS unsigned char* wp = lds + L_RING + (wseq % NS) * SLOT + (w & 1) * 4096 + lane * 16;
    float y[8];
#pragma unroll
    for (int k = 0; k < 8; ++k) y[k] = 0.f;
#pragma unroll
    for (int r4 = 0; r4 < 4; ++r4) {
      const u32x4 wv = *(const LAS u32x4*)(wp + r4 * 1024);
#pragma unroll
      for (int k = 0; k < 4; ++k) { y[2 * k] = fmaf(orow[r4], lo16(wv[k]), y[2 * k]); y[2 * k + 1] = fmaf(orow[r4], hi16(wv[k]), y[2 * k + 1]); }
    }
    LAS f32x4* yl = (LAS f32x4*)(lds + L_Y + w * 2048 + lane * 32);
    yl[0] = f32x4{y[0], y[1], y[2], y[3]}; yl[1] = f32x4{y[4], y[5], y[6], y[7]};
  }
  LDS_BARRIER();
  if (tid < 256) {
    const LAS unsigned char* yl = lds + L_Y + tid * 8;
    f32x2 s2[8];
#pragma unroll
    for (int k = 0; k < 8; ++k) s2[k] = *(const LAS f32x2*)(yl + k * 2048);
    const f32x2 s = ((s2[0] + s2[1]) + (s2[2] + s2[3])) + ((s2[4] + s2[5]) + (s2[6] + s2[7]));
    const int c = (j >> 4) * 32 + (tid >> 3), k = tid & 7, pidx = h * 16 + (j & 15);
    __builtin_amdgcn_raw_buffer_store_b128(u32x4{tagA, f2u(s[0]), f2u(s[1]), tagB}, rs, WS_I2 + ((c * 128 + pidx) * 8 + k) * 16, 0, AUX_SC1);
  }
  STAMP(6);

  if (j < 8) {
    const int c = h * 8 + j;
    const unsigned off = WS_I2 + (c * 128 + 16 * w) * 128 + lane * 16;
    u32x4 v[2];
    bool ok = false;
    for (unsigned spin = 0; spin < SPIN_CAP; ++spin) {
      MEMFENCE();
      if (!ok) {
#pragma unroll
        for (int i = 0; i < 2; ++i) v[i] = __builtin_amdgcn_raw_buffer_load_b128(rs, off + i * 1024, 0, AUX_SC1);
        ok = v[0][0] == tagA && v[0][3] == tagB && v[1][0] == tagA && v[1][3] == tagB;
      }
      if (__all(ok)) break;
      if (PSLEEP) __builtin_amdgcn_s_sleep(PSLEEP);
    }
    STAMP(7);
    float s0 = u2f(v[0][1]) + u2f(v[1][1]);
    float s1 = u2f(v[0][2]) + u2f(v[1][2]);
    s0 += dpp<ROR8>(s0); s1 += dpp<ROR8>(s1);
    s0 = swap32_sum(swap16_sum(s0)); s1 = swap32_sum(swap16_sum(s1));
    if (lane < 8) *(LAS f32x2*)(lds + L_F + w * 64 + lane * 8) = f32x2{s0, s1};
    LDS_BARRIER();
    if (tid < 8) {
      f32x2 s = *(const LAS f32x2*)(lds + L_F + tid * 8);
#pragma unroll
      for (int ww = 1; ww < 8; ++ww) s += *(const LAS f32x2*)(lds + L_F + ww * 64 + tid * 8);
      *(GAS unsigned*)((GAS unsigned char*)a.out + (c * 16 + 2 * tid) * 2) = cvtpk(s[0], s[1]);
      if (c == 0 && tid == 0) __builtin_amdgcn_raw_buffer_store_b64(u32x2{tagA, tagB}, rs, WS_GEN, 0, AUX_SC1);
    }
  }
  DUMP_STAMPS();
}

extern "C" void kernel_launch(void* const* d_in, const int* in_sizes, int n_in, void* d_out, int out_size, void* d_ws, size_t ws_size, hipStream_t stream) {
  Args a;
  a.xA = (const unsigned char*)d_in[0]; a.kA = (const unsigned char*)d_in[1]; a.vA = (const unsigned char*)d_in[2]; a.wqA = (const unsigned char*)d_in[3]; a.woA = (const unsigned char*)d_in[4];
  a.xB = (const unsigned char*)d_in[5]; a.kB = (const unsigned char*)d_in[6]; a.vB = (const unsigned char*)d_in[7]; a.wqB = (const unsigned char*)d_in[8]; a.woB = (const unsigned char*)d_in[9];
  a.out = (unsigned short*)d_out;
  a.ws = (unsigned char*)d_ws;
  hipLaunchKernelGGL(fused_decode, dim3(NWG), dim3(NTHR), 0, stream, a);
}
```

```cpp
#include <hip/hip_runtime.h>
#include <hip/hip_bf16.h>
#include <stdint.h>

#define GAS __attribute__((address_space(1)))
#define LAS __attribute__((address_space(3)))
typedef unsigned u32x4 __attribute__((ext_vector_type(4)));
typedef unsigned u32x2 __attribute__((ext_vector_type(2)));
typedef float f32x4 __attribute__((ext_vector_type(4)));
typedef float f32x2 __attribute__((ext_vector_type(2)));
typedef __bf16 bf16x8 __attribute__((ext_vector_type(8)));
typedef __bf16 bf16x2 __attribute__((ext_vector_type(2)));
typedef short s16x4 __attribute__((ext_vector_type(4)));

#define AMPK 0
#define NS 18
#define WIN_LD 22
#define PRE 2
#define QSLEEP 16
#define PSLEEP 0
#define BENCH 0
#define STAMPS 1

constexpr int SK = 65536, WIN = 4096;
constexpr int NWG = 256, NC = 8, NTHR = (NC + 1) * 64;
constexpr int SLOT = 8192, NSEQ = 4 + 8 + 64 + 64 + 4;
constexpr int LAG = (WIN_LD + 7) / 8;
constexpr float LOG2E = 1.4426950408889634f;
constexpr float SCALE = 0.08838834764831845f;
constexpr unsigned SPIN_CAP = 1u << 20, LSPIN_CAP = 1u << 24;

constexpr unsigned WS_GEN = 0;
constexpr unsigned WS_QG = 4096;
constexpr unsigned WS_ML = WS_QG + 8 * 32 * 32 * 16;
constexpr unsigned WS_I1 = WS_ML + 256 * 128;
constexpr unsigned WS_I2 = WS_I1 + 8 * 32 * 32 * 256;
constexpr unsigned WS_END = WS_I2 + 64 * 128 * 128;
constexpr unsigned WS_STAMP = WS_END;

constexpr int L_RING = 0;
constexpr int L_FULL = NS * SLOT;
constexpr int L_FREE = L_FULL + 128;
constexpr int L_CNT = L_FREE + 128;
constexpr int L_RED = L_CNT + 64;
constexpr int L_Q = L_RED + 2048;
constexpr int L_T1 = L_Q + 2048;
constexpr int L_T2 = L_T1 + 128;
constexpr int L_TB = L_T2 + 128;
constexpr int L_FF = L_TB + 256;
constexpr int L_H1 = L_FF + 64;
constexpr int L_F = L_H1 + 8 * 32 * 4;
constexpr int L_END = L_F + 512 + 128;
constexpr int C_QRED = 0, C_QIN = 4, C_MAX = 8, C_SUM = 12, C_FF = 16, C_MRG = 20;
constexpr int MREC = 132;
constexpr int L_MRG = 0;
constexpr int MRG_SLOTS = 5;
constexpr int L_Y = 5 * SLOT;
static_assert(L_END <= 160 * 1024, "LDS");
static_assert(2 * 8 * 4 * MREC * 4 <= MRG_SLOTS * SLOT, "merge area");
static_assert((140 % NS) >= 7 && (143 % NS) >= 7 && (140 % NS) <= (143 % NS), "wo slots clear of the merge / y areas");
static_assert((132 % NS) >= MRG_SLOTS && (139 % NS) >= MRG_SLOTS && (132 % NS) <= (139 % NS), "last round clear of the merge area");
static_assert(WIN_LD >= 9 && WIN_LD <= 48, "window");

__device__ __forceinline__ unsigned cvtpk(float lo, float hi) { f32x2 f = {lo, hi}; return __builtin_bit_cast(unsigned, __builtin_convertvector(f, bf16x2)); }
__device__ __forceinline__ float lo16(unsigned u) { return __builtin_bit_cast(float, u << 16); }
__device__ __forceinline__ float hi16(unsigned u) { return __builtin_bit_cast(float, u & 0xffff0000u); }
__device__ __forceinline__ float u2f(unsigned u) { return __builtin_bit_cast(float, u); }
__device__ __forceinline__ unsigned f2u(float f) { return __builtin_bit_cast(unsigned, f); }
template <int CTRL> __device__ __forceinline__ float dpp(float x) { return u2f(__builtin_amdgcn_mov_dpp(f2u(x), CTRL, 0xf, 0xf, true)); }
constexpr int XOR1 = 0xB1, XOR2 = 0x4E, HMIR = 0x141;
constexpr int ROR1 = 0x121, ROR2 = 0x122, ROR4 = 0x124, ROR8 = 0x128;
__device__ __forceinline__ float swap16_max(float x) { auto s = __builtin_amdgcn_permlane16_swap(f2u(x), f2u(x), false, false); return fmaxf(u2f(s[0]), u2f(s[1])); }
__device__ __forceinline__ float swap32_max(float x) { auto s = __builtin_amdgcn_permlane32_swap(f2u(x), f2u(x), false, false); return fmaxf(u2f(s[0]), u2f(s[1])); }
__device__ __forceinline__ float swap16_sum(float x) { auto s = __builtin_amdgcn_permlane16_swap(f2u(x), f2u(x), false, false); return u2f(s[0]) + u2f(s[1]); }
__device__ __forceinline__ float swap32_sum(float x) { auto s = __builtin_amdgcn_permlane32_swap(f2u(x), f2u(x), false, false); return u2f(s[0]) + u2f(s[1]); }
__device__ __forceinline__ float row_sum(float x) { x += dpp<ROR8>(x); x += dpp<ROR4>(x); x += dpp<ROR2>(x); x += dpp<ROR1>(x); return x; }
__device__ __forceinline__ float row_max(float x) { x = fmaxf(x, dpp<ROR8>(x)); x = fmaxf(x, dpp<ROR4>(x)); x = fmaxf(x, dpp<ROR2>(x)); x = fmaxf(x, dpp<ROR1>(x)); return x; }
__device__ __forceinline__ float oct_sum(float x) { x += dpp<XOR1>(x); x += dpp<XOR2>(x); x += dpp<HMIR>(x); return x; }
__device__ __forceinline__ float oct_max(float x) { x = fmaxf(x, dpp<XOR1>(x)); x = fmaxf(x, dpp<XOR2>(x)); x = fmaxf(x, dpp<HMIR>(x)); return x; }
#define LDS_BARRIER() asm volatile("s_waitcnt lgkmcnt(0)\n\ts_barrier" ::: "memory")
#define LDS_WAIT() asm volatile("s_waitcnt lgkmcnt(0)" ::: "memory")
#define MEMFENCE() asm volatile("" ::: "memory")
constexpr int AUX_SC1 = 16;

struct Args {
  const unsigned char *xA, *kA, *vA, *wqA, *woA, *xB, *kB, *vB, *wqB, *woB;
  unsigned short* out;
  unsigned char* ws;
};

__device__ __forceinline__ unsigned lds_ld(LAS unsigned char* L, int off) { return __builtin_amdgcn_readfirstlane(*(volatile LAS unsigned*)(L + off)); }
__device__ __forceinline__ void lds_st(LAS unsigned char* L, int off, unsigned v) { *(volatile LAS unsigned*)(L + off) = v; }
template <int SLEEP> __device__ __forceinline__ void lds_wait_ge(LAS unsigned char* L, int off, unsigned need) {
  for (unsigned spin = 0; lds_ld(L, off) < need && spin < LSPIN_CAP; ++spin) { if (SLEEP) __builtin_amdgcn_s_sleep(SLEEP); }
  MEMFENCE();
}
__device__ __forceinline__ void lds_arrive(LAS unsigned char* L, int off, int lane) {
  LDS_WAIT();
  if (lane == 0) __hip_atomic_fetch_add((LAS unsigned*)(L + off), 1u, __ATOMIC_RELAXED, __HIP_MEMORY_SCOPE_WORKGROUP);
}

__device__ __forceinline__ void dma1(unsigned dst, unsigned voff, const unsigned char* base) {
  unsigned keep;
  asm volatile("s_mov_b32 %0, m0\n\ts_mov_b32 m0, %1\n\ts_nop 0\n\tglobal_load_lds_dwordx4 %2, %3 nt\n\ts_mov_b32 m0, %0\n\ts_waitcnt vmcnt(%4)"
               : "=&s"(keep) : "s"(dst), "v"(voff), "s"(base), "i"(WIN_LD) : "memory");
}
__device__ __forceinline__ void dma4(unsigned dst, unsigned v0, unsigned v1, unsigned v2, unsigned v3, const unsigned char* base) {
  unsigned keep;
  asm volatile("s_mov_b32 %0, m0\n\ts_mov_b32 m0, %1\n\ts_nop 0\n\t"
               "global_load_lds_dwordx4 %2, %6 nt\n\ts_waitcnt vmcnt(%7)\n\t"
               "global_load_lds_dwordx4 %3, %6 offset:1024 nt\n\ts_waitcnt vmcnt(%7)\n\t"
               "global_load_lds_dwordx4 %4, %6 offset:2048 nt\n\ts_waitcnt vmcnt(%7)\n\t"
               "global_load_lds_dwordx4 %5, %6 offset:3072 nt\n\ts_waitcnt vmcnt(%7)\n\t"
               "s_mov_b32 m0, %0"
               : "=&s"(keep) : "s"(dst), "v"(v0), "v"(v1), "v"(v2), "v"(v3), "s"(base), "i"(WIN_LD) : "memory");
}
template <int N> __device__ __forceinline__ void vm_wait() { asm volatile("s_waitcnt vmcnt(%0)" :: "i"(N) : "memory"); }

__device__ __forceinline__ u32x2 tile_scores(const u32x4* K, const u32x4 (&q)[4]) {
  f32x4 s = {0.f, 0.f, 0.f, 0.f};
#pragma unroll
  for (int i = 0; i < 4; ++i) s = __builtin_amdgcn_mfma_f32_16x16x32_bf16(__builtin_bit_cast(bf16x8, K[i]), __builtin_bit_cast(bf16x8, q[i]), s, 0, 0, 0);
  const unsigned r01 = cvtpk(s[0], s[1]), r23 = cvtpk(s[2], s[3]);
  return u32x2{cvtpk(lo16(r01) * SCALE, hi16(r01) * SCALE), cvtpk(lo16(r23) * SCALE, hi16(r23) * SCALE)};
}
__device__ __forceinline__ float max4(u32x2 t) { return fmaxf(fmaxf(lo16(t[0]), hi16(t[0])), fmaxf(lo16(t[1]), hi16(t[1]))); }
__device__ __forceinline__ u32x2 to_probs(u32x2 t, float m2, float& l) {
  const float p0 = __builtin_amdgcn_exp2f(fmaf(lo16(t[0]), LOG2E, -m2)), p1 = __builtin_amdgcn_exp2f(fmaf(hi16(t[0]), LOG2E, -m2));
  const float p2 = __builtin_amdgcn_exp2f(fmaf(lo16(t[1]), LOG2E, -m2)), p3 = __builtin_amdgcn_exp2f(fmaf(hi16(t[1]), LOG2E, -m2));
  l += (p0 + p1) + (p2 + p3);
  return u32x2{cvtpk(p0, p1), cvtpk(p2, p3)};
}
struct Sel { unsigned l0, l1, h0, h1; };
__device__ __forceinline__ void tile_pv(f32x4& a0, f32x4& a1, const u32x4* V, u32x2 p, const Sel& s) {
#pragma unroll
  for (int r = 0; r < 4; ++r) {
    const unsigned pk = p[r >> 1];
    const u32x2 a = {__builtin_amdgcn_perm(0u, pk, (r & 1) ? s.h0 : s.l0), __builtin_amdgcn_perm(0u, pk, (r & 1) ? s.h1 : s.l1)};
    const u32x2 v0 = {V[r][0], V[r][1]}, v1 = {V[r][2], V[r][3]};
    a0 = __builtin_amdgcn_mfma_f32_16x16x16bf16_1k(__builtin_bit_cast(s16x4, a), __builtin_bit_cast(s16x4, v0), a0, 0, 0, 0);
    a1 = __builtin_amdgcn_mfma_f32_16x16x16bf16_1k(__builtin_bit_cast(s16x4, a), __builtin_bit_cast(s16x4, v1), a1, 0, 0, 0);
  }
}

#define STAMP(i) do { if (tid == 0) { ((volatile LAS unsigned long long*)(lds + L_END - 128))[(i)] = __builtin_amdgcn_s_memrealtime(); } } while (0)
#define WSTAMP(i) do { if (lane == 0) { ((volatile LAS unsigned long long*)(lds + L_END - 128))[(i)] = __builtin_amdgcn_s_memrealtime(); } } while (0)
#define DUMP_STAMPS() do { if (b == 0) { const unsigned long long t_end = __builtin_amdgcn_s_memrealtime(); \
    volatile LAS unsigned long long* q_ = (volatile LAS unsigned long long*)(lds + L_END - 128); \
    if (tid == 0) { q_[0] = ~0ull; q_[1] = 0ull; } \
    LDS_BARRIER(); \
    if (tid < 256) { const unsigned long long v = __hip_atomic_load((GAS unsigned long long*)(a.ws + WS_STAMP) + tid, __ATOMIC_RELAXED, __HIP_MEMORY_SCOPE_AGENT); \
      __hip_atomic_fetch_min((LAS unsigned long long*)(lds + L_END - 128), v, __ATOMIC_RELAXED, __HIP_MEMORY_SCOPE_WORKGROUP); \
      __hip_atomic_fetch_max((LAS unsigned long long*)(lds + L_END - 128) + 1, v, __ATOMIC_RELAXED, __HIP_MEMORY_SCOPE_WORKGROUP); } \
    LDS_BARRIER(); \
    if (tid == 0) { const unsigned long long mn = q_[0], mxs = q_[1]; \
      unsigned long long d = AMPK == 0 ? 0ull : AMPK == 1 ? 20ull * (mxs - mn) : AMPK == 2 ? 20ull * (t_start - mn) : 4ull * (t_end - mn); \
      if (d > 100000ull) d = 100000ull; \
      const unsigned long long t1 = __builtin_amdgcn_s_memrealtime(); \
      for (unsigned sp_ = 0; sp_ < 10000000u && __builtin_amdgcn_s_memrealtime() < t1 + d; ++sp_) __builtin_amdgcn_s_sleep(1); } } } while (0)

__global__ void __launch_bounds__(NTHR, 3) fused_decode(Args a) {
  __shared__ __attribute__((aligned(16))) unsigned char lds_raw[L_END];
  LAS unsigned char* lds = (LAS unsigned char*)lds_raw;
  const int b = blockIdx.x, h = b & 7, j = b >> 3;
  const int tid = threadIdx.x, lane = tid & 63, w = __builtin_amdgcn_readfirstlane(tid >> 6);
  const int hh = lane >> 4, i16 = lane & 15, jj = lane & 3;
  const unsigned long long t_start = __builtin_amdgcn_s_memrealtime();
  const int qcb = j >> 1, qrh = j & 1, qbr = qcb >> 3;

  if (w == NC) {
    const unsigned ring = (unsigned)(size_t)(lds + L_RING);
    int pub = 0, pslot = 0, slot = 0;
    auto publish1 = [&]() { lds_st(lds, L_FULL + 4 * pslot, (unsigned)(pub + 1)); ++pub; pslot = pslot + 1 == NS ? 0 : pslot + 1; };
    auto pre = [&](int q) {
      if (q >= NS) {
        const unsigned need = (unsigned)(q - NS + 1);
        if (lds_ld(lds, L_FREE + 4 * slot) < need) {
          vm_wait<0>();
          while (pub < q) publish1();
          lds_wait_ge<1>(lds, L_FREE + 4 * slot, need);
        }
      }
    };
    auto post = [&](int q) {
      if (q - pub >= LAG) publish1();
      slot = slot + 1 == NS ? 0 : slot + 1;
    };
    {
      const unsigned char* wq = (qbr ? a.wqB : a.wqA) + (size_t)(h * 512 + 64 * (qcb & 7)) * 2 + (size_t)qrh * 256 * 8192;
      const unsigned vo = (lane >> 3) * 8192 + (lane & 7) * 16;
#pragma unroll
      for (int q = 0; q < 4; ++q) {
#pragma unroll
        for (int m = 0; m < 8; ++m) dma1(ring + q * SLOT + m * 1024, vo, wq + (size_t)(q * 64 + m * 8) * 8192);
        if (q == 0) LDS_BARRIER();
        post(q);
      }
    }
    const unsigned k0 = hh * 256 + ((i16 ^ (0 + hh)) * 16), k1 = hh * 256 + ((i16 ^ (4 + hh)) * 16), k2 = hh * 256 + ((i16 ^ (8 + hh)) * 16), k3 = hh * 256 + ((i16 ^ (12 + hh)) * 16);
    const unsigned vl = lane * 16;
    {
      const unsigned char* kp = a.kB + ((size_t)h * SK + (SK - WIN) + j * 128) * 256;
      const unsigned char* vp = a.vB + ((size_t)h * SK + (SK - WIN) + j * 128) * 256;
      for (int q = 4; q < 12; ++q) {
        dma4(ring + slot * SLOT, k0, k1, k2, k3, kp);
        dma4(ring + slot * SLOT + 4096, vl, vl, vl, vl, vp);
        kp += 4096; vp += 4096;
        post(q);
      }
      kp = a.kA + ((size_t)h * SK + j * 2048) * 256;
      for (int q = 12; q < 76; ++q) {
        pre(q);
        dma4(ring + slot * SLOT, k0, k1, k2, k3, kp);
        dma4(ring + slot * SLOT + 4096, k0, k1, k2, k3, kp + 4096);
        kp += 8192;
        post(q);
      }
      WSTAMP(12);
      vp = a.vA + ((size_t)h * SK + j * 2048) * 256;
      for (int q = 76; q < 140; ++q) {
        pre(q);
        dma4(ring + slot * SLOT, vl, vl, vl, vl, vp);
        dma4(ring + slot * SLOT + 4096, vl, vl, vl, vl, vp + 4096);
        vp += 8192;
        post(q);
      }
      WSTAMP(9);
      vp = (j >> 4 ? a.woB : a.woA) + (size_t)(h * 512 + 32 * (j & 15)) * 1024;
      for (int q = 140; q < 144; ++q) {
        pre(q);
        dma4(ring + slot * SLOT, vl, vl, vl, vl, vp);
        dma4(ring + slot * SLOT + 4096, vl, vl, vl, vl, vp + 4096);
        vp += 8192;
        post(q);
      }
    }
    if (NSEQ - pub > 6) { vm_wait<48>(); publish1(); }
    if (NSEQ - pub > 5) { vm_wait<40>(); publish1(); }
    if (NSEQ - pub > 4) { vm_wait<32>(); publish1(); }
    if (NSEQ - pub > 3) { vm_wait<24>(); publish1(); }
    if (NSEQ - pub > 2) { vm_wait<16>(); publish1(); }
    if (NSEQ - pub > 1) { vm_wait<8>(); publish1(); }
    vm_wait<0>();
    while (pub < NSEQ) publish1();
    WSTAMP(10);
    return;
  }

  const __amdgpu_buffer_rsrc_t rs = __builtin_amdgcn_make_buffer_rsrc((void*)a.ws, 0, (int)WS_END, 0x00020000);
  const u32x2 gen = __builtin_amdgcn_raw_buffer_load_b64(rs, WS_GEN, 0, AUX_SC1);
  unsigned short xv[4];
  {
    const GAS unsigned char* xp = (const GAS unsigned char*)(qbr ? a.xB : a.xA) + 2 * (qrh * 256 + w * 32 + (lane >> 3));
#pragma unroll
    for (int i = 0; i < 4; ++i) xv[i] = *(const GAS unsigned short*)(xp + 16 * i);
  }
  if (tid == 0) __hip_atomic_store((GAS unsigned long long*)(a.ws + WS_STAMP) + b, t_start, __ATOMIC_RELAXED, __HIP_MEMORY_SCOPE_AGENT);
  if (tid < (L_RED - L_FULL) / 4) *(LAS unsigned*)(lds + L_FULL + 4 * tid) = 0u;
  if (tid >= 128 && tid < 160) *(LAS unsigned*)(lds + L_END - 128 + 4 * (tid - 128)) = 0u;
  LDS_BARRIER();

  lds_wait_ge<1>(lds, L_FULL + 4 * (w >> 1), (unsigned)((w >> 1) + 1));
  {
    u32x4 wv[4];
#pragma unroll
    for (int i = 0; i < 4; ++i) wv[i] = *(const LAS u32x4*)(lds + L_RING + (w >> 1) * SLOT + (w & 1) * 4096 + i * 1024 + lane * 16);
    float acc[8];
#pragma unroll
    for (int k = 0; k < 8; ++k) acc[k] = 0.f;
#pragma unroll
    for (int i = 0; i < 4; ++i) {
      const float x = lo16(xv[i]);
#pragma unroll
      for (int k = 0; k < 4; ++k) { acc[2 * k] = fmaf(x, lo16(wv[i][k]), acc[2 * k]); acc[2 * k + 1] = fmaf(x, hi16(wv[i][k]), acc[2 * k + 1]); }
    }
#pragma unroll
    for (int k = 0; k < 8; ++k) { float v = acc[k]; v += dpp<ROR8>(v); v = swap16_sum(v); v = swap32_sum(v); acc[k] = v; }
    if (lane < 8) {
      LAS f32x4* red = (LAS f32x4*)(lds + L_RED + w * 256 + lane * 32);
      red[0] = f32x4{acc[0], acc[1], acc[2], acc[3]}; red[1] = f32x4{acc[4], acc[5], acc[6], acc[7]};
    }
  }
  lds_arrive(lds, L_CNT + C_QRED, lane);
  const unsigned tagA = __builtin_amdgcn_readfirstlane(gen[0]) + 1u, tagB = __builtin_amdgcn_readfirstlane(gen[1]) + 1u;
  if (w == 0) {
    lds_wait_ge<0>(lds, L_CNT + C_QRED, NC);
    if (lane < 4) lds_st(lds, L_FREE + 4 * lane, (unsigned)(lane + 1));
    if (lane < 32) {
      f32x2 s = {0.f, 0.f};
#pragma unroll
      for (int ww = 0; ww < 8; ++ww) s += *(const LAS f32x2*)(lds + L_RED + ww * 256 + lane * 8);
      __builtin_amdgcn_raw_buffer_store_b128(u32x4{tagA, f2u(s[0]), f2u(s[1]), tagB}, rs, WS_QG + ((h * 32 + j) * 32 + lane) * 16, 0, AUX_SC1);
    }
  }
  STAMP(1);
  int slot = (4 + w) % NS;
  const unsigned kr0 = i16 * 256 + (((0 + hh) ^ i16) * 16), kr1 = i16 * 256 + (((4 + hh) ^ i16) * 16), kr2 = i16 * 256 + (((8 + hh) ^ i16) * 16), kr3 = i16 * 256 + (((12 + hh) ^ i16) * 16);
  const unsigned vr = hh * 1024 + i16 * 16;
  auto fetch = [&](int it, int kind, u32x4 (&T)[8]) {
    const unsigned seq1 = (unsigned)(4 + 8 * it + w + 1);
    lds_wait_ge<1>(lds, L_FULL + 4 * slot, seq1);
    const LAS unsigned char* sp = lds + L_RING + slot * SLOT;
    if (kind != 2) { T[0] = *(const LAS u32x4*)(sp + kr0); T[1] = *(const LAS u32x4*)(sp + kr1); T[2] = *(const LAS u32x4*)(sp + kr2); T[3] = *(const LAS u32x4*)(sp + kr3); }
    else {
#pragma unroll
      for (int r = 0; r < 4; ++r) T[r] = *(const LAS u32x4*)(sp + vr + r * 256);
    }
    if (kind == 1) { T[4] = *(const LAS u32x4*)(sp + 4096 + kr0); T[5] = *(const LAS u32x4*)(sp + 4096 + kr1); T[6] = *(const LAS u32x4*)(sp + 4096 + kr2); T[7] = *(const LAS u32x4*)(sp + 4096 + kr3); }
    else {
#pragma unroll
      for (int r = 0; r < 4; ++r) T[4 + r] = *(const LAS u32x4*)(sp + 4096 + vr + r * 256);
    }
    LDS_WAIT();
    lds_st(lds, L_FREE + 4 * slot, seq1);
    asm volatile("" : "+v"(T[0]), "+v"(T[1]), "+v"(T[2]), "+v"(T[3]), "+v"(T[4]), "+v"(T[5]), "+v"(T[6]), "+v"(T[7]) :: "memory");
    slot += 8; if (slot >= NS) slot -= NS;
  };
  u32x4 X0[8], X1[8], X2[8];
  fetch(0, 0, X0);
  if (PRE >= 2) fetch(1, 1, X1);
  if (PRE >= 3) fetch(2, 1, X2);
  {
    const unsigned off = WS_QG + ((h * 32 + 2 * (tid >> 5)) * 32 + (tid & 31)) * 16;
    u32x4 g0 = {0u, 0u, 0u, 0u}, g1 = {0u, 0u, 0u, 0u};
    bool ok = false;
    for (unsigned spin = 0; spin < SPIN_CAP; ++spin) {
      MEMFENCE();
      if (!ok) {
        g0 = __builtin_amdgcn_raw_buffer_load_b128(rs, off, 0, AUX_SC1);
        g1 = __builtin_amdgcn_raw_buffer_load_b128(rs, off + 512, 0, AUX_SC1);
        ok = g0[0] == tagA && g0[3] == tagB && g1[0] == tagA && g1[3] == tagB;
      }
      if (__all(ok)) break;
      if (QSLEEP) __builtin_amdgcn_s_sleep(QSLEEP);
    }
    *(LAS unsigned*)(lds + L_Q + tid * 4) = cvtpk(u2f(g0[1]) + u2f(g1[1]), u2f(g0[2]) + u2f(g1[2]));
  }
  lds_arrive(lds, L_CNT + C_QIN, lane);
  lds_wait_ge<0>(lds, L_CNT + C_QIN, NC);
  u32x4 qA[4], qB[4];
#pragma unroll
  for (int s = 0; s < 4; ++s) {
    qA[s] = *(const LAS u32x4*)(lds + L_Q + ((i16 >> 2) * 128 + 32 * s + 8 * hh) * 2);
    qB[s] = *(const LAS u32x4*)(lds + L_Q + 1024 + ((i16 >> 2) * 128 + 32 * s + 8 * hh) * 2);
  }
  STAMP(2);

  Sel sel;
  sel.l0 = jj == 0 ? 0x0c0c0100u : jj == 1 ? 0x01000c0cu : 0x0c0c0c0cu;  sel.h0 = jj == 0 ? 0x0c0c0302u : jj == 1 ? 0x03020c0cu : 0x0c0c0c0cu;
  sel.l1 = jj == 2 ? 0x0c0c0100u : jj == 3 ? 0x01000c0cu : 0x0c0c0c0cu;  sel.h1 = jj == 2 ? 0x0c0c0302u : jj == 3 ? 0x03020c0cu : 0x0c0c0c0cu;

  f32x4 b0 = {0.f, 0.f, 0.f, 0.f}, b1 = {0.f, 0.f, 0.f, 0.f};
  {
    const u32x2 t = tile_scores(X0, qB);
    const float m2 = swap32_max(swap16_max(max4(t))) * LOG2E;
    float l = 0.f;
    const u32x2 p = to_probs(t, m2, l);
    l = swap32_sum(swap16_sum(l));
    tile_pv(b0, b1, X0 + 4, p, sel);
    if (hh == 0 && jj == 0) *(LAS f32x2*)(lds + L_TB + ((i16 >> 2) * 8 + w) * 8) = f32x2{m2, l};
  }

  u32x2 P[16];
  float mx = -INFINITY;
#pragma unroll
  for (int i = 0; i < 8; ++i) {
    if (i == 0) { if (PRE < 2) fetch(1, 1, X1); }
    else if (i == 1) { if (PRE < 3) fetch(2, 1, X2); }
    else fetch(i + 1, 1, X1);
    const u32x4* T = i == 1 ? X2 : X1;
    P[2 * i] = tile_scores(T, qA);
    P[2 * i + 1] = tile_scores(T + 4, qA);
    mx = fmaxf(mx, fmaxf(max4(P[2 * i]), max4(P[2 * i + 1])));
  }
  STAMP(13);
  mx = swap32_max(swap16_max(mx));
  if (hh == 0 && jj == 0) *(LAS float*)(lds + L_T1 + ((i16 >> 2) * 8 + w) * 4) = mx;
  lds_arrive(lds, L_CNT + C_MAX, lane);
  lds_wait_ge<0>(lds, L_CNT + C_MAX, NC);
  {
    const f32x4 m0 = *(const LAS f32x4*)(lds + L_T1 + (i16 >> 2) * 32), m1 = *(const LAS f32x4*)(lds + L_T1 + (i16 >> 2) * 32 + 16);
    const float m2 = fmaxf(fmaxf(fmaxf(m0[0], m0[1]), fmaxf(m0[2], m0[3])), fmaxf(fmaxf(m1[0], m1[1]), fmaxf(m1[2], m1[3]))) * LOG2E;
    float l = 0.f;
#pragma unroll
    for (int t = 0; t < 16; ++t) P[t] = to_probs(P[t], m2, l);
    l = swap32_sum(swap16_sum(l));
    if (hh == 0 && jj == 0) *(LAS float*)(lds + L_T2 + ((i16 >> 2) * 8 + w) * 4) = l;
  }
  lds_arrive(lds, L_CNT + C_SUM, lane);
  lds_wait_ge<0>(lds, L_CNT + C_SUM, NC);
  const int mbr = w >> 2, mg = w & 3;
  {
    const int ww = lane & 7;
    float M, L;
    if (mbr == 0) {
      M = oct_max(*(const LAS float*)(lds + L_T1 + (mg * 8 + ww) * 4)) * LOG2E;
      L = oct_sum(*(const LAS float*)(lds + L_T2 + (mg * 8 + ww) * 4));
    } else {
      const f32x2 ml = *(const LAS f32x2*)(lds + L_TB + (mg * 8 + ww) * 8);
      M = oct_max(ml[0]);
      L = oct_sum(ml[1] * __builtin_amdgcn_exp2f(ml[0] - M));
    }
    if (lane == 0) __builtin_amdgcn_raw_buffer_store_b128(u32x4{tagA, f2u(M), f2u(L), tagB}, rs, WS_ML + (((h * 2 + mbr) * 4 + mg) * 32 + j) * 16, 0, AUX_SC1);
  }
  STAMP(14);

  f32x4 a0 = {0.f, 0.f, 0.f, 0.f}, a1 = {0.f, 0.f, 0.f, 0.f};
  float fA = 0.f, fB = 0.f;
#pragma unroll
  for (int i = 0; i < 8; ++i) {
    if (i == 4) {
      const unsigned off = WS_ML + (((h * 2 + mbr) * 4 + mg) * 32 + (lane & 31)) * 16;
      u32x4 mv = {0u, 0u, 0u, 0u};
      bool ok = false;
      for (unsigned spin = 0; spin < SPIN_CAP; ++spin) {
        MEMFENCE();
        if (!ok) { mv = __builtin_amdgcn_raw_buffer_load_b128(rs, off, 0, AUX_SC1); ok = mv[0] == tagA && mv[3] == tagB; }
        if (__all(ok)) break;
        __builtin_amdgcn_s_sleep(16);
      }
      const float M = swap16_max(row_max(u2f(mv[1])));
      const float L = swap16_sum(row_sum(u2f(mv[2]) * __builtin_amdgcn_exp2f(u2f(mv[1]) - M)));
      if (lane == 0) *(LAS f32x2*)(lds + L_FF + w * 8) = f32x2{M, 1.0f / L};
      lds_arrive(lds, L_CNT + C_FF, lane);
    }
    if (i == 7) {
      lds_wait_ge<0>(lds, L_CNT + C_FF, NC);
      const f32x4 m0 = *(const LAS f32x4*)(lds + L_T1 + hh * 32), m1 = *(const LAS f32x4*)(lds + L_T1 + hh * 32 + 16);
      const float m2 = fmaxf(fmaxf(fmaxf(m0[0], m0[1]), fmaxf(m0[2], m0[3])), fmaxf(fmaxf(m1[0], m1[1]), fmaxf(m1[2], m1[3]))) * LOG2E;
      const f32x2 FA = *(const LAS f32x2*)(lds + L_FF + hh * 8), FB = *(const LAS f32x2*)(lds + L_FF + (4 + hh) * 8);
      const float mB = *(const LAS float*)(lds + L_TB + (hh * 8 + w) * 8);
      fA = __builtin_amdgcn_exp2f(m2 - FA[0]) * FA[1];
      fB = __builtin_amdgcn_exp2f(mB - FB[0]) * FB[1];
      const int s = lane < MRG_SLOTS ? lane : 0;
      const unsigned need = (unsigned)(s + NS * ((139 - s) / NS) + 1);
      for (unsigned spin = 0; spin < LSPIN_CAP; ++spin) { MEMFENCE(); if (__all(*(volatile LAS unsigned*)(lds + L_FREE + 4 * s) >= need)) break; }
      MEMFENCE();
      LAS float* rB = (LAS float*)(lds + L_MRG) + ((8 + w) * 4 + hh) * MREC + 8 * i16;
      *(LAS f32x4*)(rB) = b0 * fB; *(LAS f32x4*)(rB + 4) = b1 * fB;
    }
    fetch(9 + i, 2, X1);
    tile_pv(a0, a1, X1, P[2 * i], sel);
    tile_pv(a0, a1, X1 + 4, P[2 * i + 1], sel);
  }
  STAMP(3);
  if (w == 7) WSTAMP(11);

  {
    LAS float* rA = (LAS float*)(lds + L_MRG) + (w * 4 + hh) * MREC + 8 * i16;
    *(LAS f32x4*)(rA) = a0 * fA; *(LAS f32x4*)(rA + 4) = a1 * fA;
  }
  lds_arrive(lds, L_CNT + C_MRG, lane);
  lds_wait_ge<0>(lds, L_CNT + C_MRG, NC);
  {
    const int br = tid >> 8, g = (tid >> 6) & 3, dp = lane;
    const LAS float* mrg = (const LAS float*)(lds + L_MRG) + (br * 8 * 4 + g) * MREC + 2 * dp;
    f32x2 o2[8];
#pragma unroll
    for (int ww = 0; ww < 8; ++ww) o2[ww] = *(const LAS f32x2*)(mrg + ww * 4 * MREC);
    const f32x2 o = ((o2[0] + o2[1]) + (o2[2] + o2[3])) + ((o2[4] + o2[5]) + (o2[6] + o2[7]));
    const int jc = br * 16 + g * 4 + (dp >> 4), k = dp & 15;
    __builtin_amdgcn_raw_buffer_store_b128(u32x4{tagA, f2u(o[0]), f2u(o[1]), tagB}, rs, WS_I1 + (((h * 32 + jc) * 32 + j) * 16 + k) * 16, 0, AUX_SC1);
  }
  STAMP(4);

  {
    const unsigned offA = WS_I1 + ((h * 32 + j) * 32 * 16 + w * 64 + lane) * 16;
    u32x4 av = {0u, 0u, 0u, 0u};
    bool ok = false;
    for (unsigned spin = 0; spin < SPIN_CAP; ++spin) {
      MEMFENCE();
      if (!ok) { av = __builtin_amdgcn_raw_buffer_load_b128(rs, offA, 0, AUX_SC1); ok = av[0] == tagA && av[3] == tagB; }
      if (__all(ok)) break;
      if (PSLEEP) __builtin_amdgcn_s_sleep(PSLEEP);
    }
    STAMP(5);
    const float o0 = swap32_sum(swap16_sum(u2f(av[1]))), o1 = swap32_sum(swap16_sum(u2f(av[2])));
    if (lane < 16) *(LAS f32x2*)(lds + L_H1 + (w * 32 + 2 * lane) * 4) = f32x2{o0, o1};
  }
  LDS_BARRIER();
  {
    const float o = oct_sum(*(const LAS float*)(lds + L_H1 + ((lane & 7) * 32 + 4 * w + ((lane >> 3) & 3)) * 4));
    const unsigned ob = cvtpk(o, 0.f) << 16;
    const float orow[4] = {u2f(__builtin_amdgcn_readlane(ob, 0)), u2f(__builtin_amdgcn_readlane(ob, 8)), u2f(__builtin_amdgcn_readlane(ob, 16)), u2f(__builtin_amdgcn_readlane(ob, 24))};
    const int wseq = 140 + (w >> 1);
    lds_wait_ge<0>(lds, L_FULL + 4 * (wseq % NS), (unsigned)(wseq + 1));
    const LAS unsigned char* wp = lds + L_RING + (wseq % NS) * SLOT + (w & 1) * 4096 + lane * 16;
    float y[8];
#pragma unroll
    for (int k = 0; k < 8; ++k) y[k] = 0.f;
#pragma unroll
    for (int r4 = 0; r4 < 4; ++r4) {
      const u32x4 wv = *(const LAS u32x4*)(wp + r4 * 1024);
#pragma unroll
      for (int k = 0; k < 4; ++k) { y[2 * k] = fmaf(orow[r4], lo16(wv[k]), y[2 * k]); y[2 * k + 1] = fmaf(orow[r4], hi16(wv[k]), y[2 * k + 1]); }
    }
    LAS f32x4* yl = (LAS f32x4*)(lds + L_Y + w * 2048 + lane * 32);
    yl[0] = f32x4{y[0], y[1], y[2], y[3]}; yl[1] = f32x4{y[4], y[5], y[6], y[7]};
  }
  LDS_BARRIER();
  if (tid < 256) {
    const LAS unsigned char* yl = lds + L_Y + tid * 8;
    f32x2 s2[8];
#pragma unroll
    for (int k = 0; k < 8; ++k) s2[k] = *(const LAS f32x2*)(yl + k * 2048);
    const f32x2 s = ((s2[0] + s2[1]) + (s2[2] + s2[3])) + ((s2[4] + s2[5]) + (s2[6] + s2[7]));
    const int c = (j >> 4) * 32 + (tid >> 3), k = tid & 7, pidx = h * 16 + (j & 15);
    __builtin_amdgcn_raw_buffer_store_b128(u32x4{tagA, f2u(s[0]), f2u(s[1]), tagB}, rs, WS_I2 + ((c * 128 + pidx) * 8 + k) * 16, 0, AUX_SC1);
  }
  STAMP(6);

  if (j < 8) {
    const int c = h * 8 + j;
    const unsigned off = WS_I2 + (c * 128 + 16 * w) * 128 + lane * 16;
    u32x4 v[2];
    bool ok = false;
    for (unsigned spin = 0; spin < SPIN_CAP; ++spin) {
      MEMFENCE();
      if (!ok) {
#pragma unroll
        for (int i = 0; i < 2; ++i) v[i] = __builtin_amdgcn_raw_buffer_load_b128(rs, off + i * 1024, 0, AUX_SC1);
        ok = v[0][0] == tagA && v[0][3] == tagB && v[1][0] == tagA && v[1][3] == tagB;
      }
      if (__all(ok)) break;
      if (PSLEEP) __builtin_amdgcn_s_sleep(PSLEEP);
    }
    STAMP(7);
    float s0 = u2f(v[0][1]) + u2f(v[1][1]);
    float s1 = u2f(v[0][2]) + u2f(v[1][2]);
    s0 += dpp<ROR8>(s0); s1 += dpp<ROR8>(s1);
    s0 = swap32_sum(swap16_sum(s0)); s1 = swap32_sum(swap16_sum(s1));
    if (lane < 8) *(LAS f32x2*)(lds + L_F + w * 64 + lane * 8) = f32x2{s0, s1};
    LDS_BARRIER();
    if (tid < 8) {
      f32x2 s = *(const LAS f32x2*)(lds + L_F + tid * 8);
#pragma unroll
      for (int ww = 1; ww < 8; ++ww) s += *(const LAS f32x2*)(lds + L_F + ww * 64 + tid * 8);
      *(GAS unsigned*)((GAS unsigned char*)a.out + (c * 16 + 2 * tid) * 2) = cvtpk(s[0], s[1]);
      if (c == 0 && tid == 0) __builtin_amdgcn_raw_buffer_store_b64(u32x2{tagA, tagB}, rs, WS_GEN, 0, AUX_SC1);
    }
  }
  DUMP_STAMPS();
}

extern "C" void kernel_launch(void* const* d_in, const int* in_sizes, int n_in, void* d_out, int out_size, void* d_ws, size_t ws_size, hipStream_t stream) {
  Args a;
  a.xA = (const unsigned char*)d_in[0]; a.kA = (const unsigned char*)d_in[1]; a.vA = (const unsigned char*)d_in[2]; a.wqA = (const unsigned char*)d_in[3]; a.woA = (const unsigned char*)d_in[4];
  a.xB = (const unsigned char*)d_in[5]; a.kB = (const unsigned char*)d_in[6]; a.vB = (const unsigned char*)d_in[7]; a.wqB = (const unsigned char*)d_in[8]; a.woB = (const unsigned char*)d_in[9];
  a.out = (unsigned short*)d_out;
  a.ws = (unsigned char*)d_ws;
  hipLaunchKernelGGL(fused_decode, dim3(NWG), dim3(NTHR), 0, stream, a);
}
```

```cpp
#include <hip/hip_runtime.h>
#include <hip/hip_bf16.h>
#include <stdint.h>

#define GAS __attribute__((address_space(1)))
#define LAS __attribute__((address_space(3)))
typedef unsigned u32x4 __attribute__((ext_vector_type(4)));
typedef unsigned u32x2 __attribute__((ext_vector_type(2)));
typedef float f32x4 __attribute__((ext_vector_type(4)));
typedef float f32x2 __attribute__((ext_vector_type(2)));
typedef __bf16 bf16x8 __attribute__((ext_vector_type(8)));
typedef __bf16 bf16x2 __attribute__((ext_vector_type(2)));
typedef short s16x4 __attribute__((ext_vector_type(4)));

#define NS 18
#define WIN_LD 22
#define PRE 2
#define QSLEEP 16
#define PSLEEP 0
#define BENCH 0
#define STAMPS 0

constexpr int SK = 65536, WIN = 4096;
constexpr int NWG = 256, NC = 8, NTHR = (NC + 1) * 64;
constexpr int SLOT = 8192, NSEQ = 4 + 8 + 64 + 64 + 4;
constexpr int LAG = (WIN_LD + 7) / 8;
constexpr float LOG2E = 1.4426950408889634f;
constexpr float SCALE = 0.08838834764831845f;
constexpr unsigned SPIN_CAP = 1u << 20, LSPIN_CAP = 1u << 24;

constexpr unsigned WS_GEN = 0;
constexpr unsigned WS_QG = 4096;
constexpr unsigned WS_ML = WS_QG + 8 * 32 * 32 * 16;
constexpr unsigned WS_I1 = WS_ML + 256 * 128;
constexpr unsigned WS_I2 = WS_I1 + 8 * 32 * 32 * 256;
constexpr unsigned WS_END = WS_I2 + 64 * 128 * 128;
constexpr unsigned WS_STAMP = WS_END;

constexpr int L_RING = 0;
constexpr int L_FULL = NS * SLOT;
constexpr int L_FREE = L_FULL + 128;
constexpr int L_CNT = L_FREE + 128;
constexpr int L_RED = L_CNT + 64;
constexpr int L_Q = L_RED + 2048;
constexpr int L_T1 = L_Q + 2048;
constexpr int L_T2 = L_T1 + 128;
constexpr int L_TB = L_T2 + 128;
constexpr int L_FF = L_TB + 256;
constexpr int L_H1 = L_FF + 64;
constexpr int L_F = L_H1 + 8 * 32 * 4;
constexpr int L_END = L_F + 512 + 128;
constexpr int C_QRED = 0, C_QIN = 4, C_MAX = 8, C_SUM = 12, C_FF = 16, C_MRG = 20;
constexpr int MREC = 132;
constexpr int L_MRG = 0;
constexpr int MRG_SLOTS = 5;
constexpr int L_Y = 5 * SLOT;
static_assert(L_END <= 160 * 1024, "LDS");
static_assert(2 * 8 * 4 * MREC * 4 <= MRG_SLOTS * SLOT, "merge area");
static_assert((140 % NS) >= 7 && (143 % NS) >= 7 && (140 % NS) <= (143 % NS), "wo slots clear of the merge / y areas");
static_assert((132 % NS) >= MRG_SLOTS && (139 % NS) >= MRG_SLOTS && (132 % NS) <= (139 % NS), "last round clear of the merge area");
static_assert(WIN_LD >= 9 && WIN_LD <= 48, "window");

__device__ __forceinline__ unsigned cvtpk(float lo, float hi) { f32x2 f = {lo, hi}; return __builtin_bit_cast(unsigned, __builtin_convertvector(f, bf16x2)); }
__device__ __forceinline__ float lo16(unsigned u) { return __builtin_bit_cast(float, u << 16); }
__device__ __forceinline__ float hi16(unsigned u) { return __builtin_bit_cast(float, u & 0xffff0000u); }
__device__ __forceinline__ float u2f(unsigned u) { return __builtin_bit_cast(float, u); }
__device__ __forceinline__ unsigned f2u(float f) { return __builtin_bit_cast(unsigned, f); }
template <int CTRL> __device__ __forceinline__ float dpp(float x) { return u2f(__builtin_amdgcn_mov_dpp(f2u(x), CTRL, 0xf, 0xf, true)); }
constexpr int XOR1 = 0xB1, XOR2 = 0x4E, HMIR = 0x141;
constexpr int ROR1 = 0x121, ROR2 = 0x122, ROR4 = 0x124, ROR8 = 0x128;
__device__ __forceinline__ float swap16_max(float x) { auto s = __builtin_amdgcn_permlane16_swap(f2u(x), f2u(x), false, false); return fmaxf(u2f(s[0]), u2f(s[1])); }
__device__ __forceinline__ float swap32_max(float x) { auto s = __builtin_amdgcn_permlane32_swap(f2u(x), f2u(x), false, false); return fmaxf(u2f(s[0]), u2f(s[1])); }
__device__ __forceinline__ float swap16_sum(float x) { auto s = __builtin_amdgcn_permlane16_swap(f2u(x), f2u(x), false, false); return u2f(s[0]) + u2f(s[1]); }
__device__ __forceinline__ float swap32_sum(float x) { auto s = __builtin_amdgcn_permlane32_swap(f2u(x), f2u(x), false, false); return u2f(s[0]) + u2f(s[1]); }
__device__ __forceinline__ float row_sum(float x) { x += dpp<ROR8>(x); x += dpp<ROR4>(x); x += dpp<ROR2>(x); x += dpp<ROR1>(x); return x; }
__device__ __forceinline__ float row_max(float x) { x = fmaxf(x, dpp<ROR8>(x)); x = fmaxf(x, dpp<ROR4>(x)); x = fmaxf(x, dpp<ROR2>(x)); x = fmaxf(x, dpp<ROR1>(x)); return x; }
__device__ __forceinline__ float oct_sum(float x) { x += dpp<XOR1>(x); x += dpp<XOR2>(x); x += dpp<HMIR>(x); return x; }
__device__ __forceinline__ float oct_max(float x) { x = fmaxf(x, dpp<XOR1>(x)); x = fmaxf(x, dpp<XOR2>(x)); x = fmaxf(x, dpp<HMIR>(x)); return x; }
#define LDS_BARRIER() asm volatile("s_waitcnt lgkmcnt(0)\n\ts_barrier" ::: "memory")
#define LDS_WAIT() asm volatile("s_waitcnt lgkmcnt(0)" ::: "memory")
#define MEMFENCE() asm volatile("" ::: "memory")
constexpr int AUX_SC1 = 16;

struct Args {
  const unsigned char *xA, *kA, *vA, *wqA, *woA, *xB, *kB, *vB, *wqB, *woB;
  unsigned short* out;
  unsigned char* ws;
};

__device__ __forceinline__ unsigned lds_ld(LAS unsigned char* L, int off) { return __builtin_amdgcn_readfirstlane(*(volatile LAS unsigned*)(L + off)); }
__device__ __forceinline__ void lds_st(LAS unsigned char* L, int off, unsigned v) { *(volatile LAS unsigned*)(L + off) = v; }
template <int SLEEP> __device__ __forceinline__ void lds_wait_ge(LAS unsigned char* L, int off, unsigned need) {
  for (unsigned spin = 0; lds_ld(L, off) < need && spin < LSPIN_CAP; ++spin) { if (SLEEP) __builtin_amdgcn_s_sleep(SLEEP); }
  MEMFENCE();
}
__device__ __forceinline__ void lds_arrive(LAS unsigned char* L, int off, int lane) {
  LDS_WAIT();
  if (lane == 0) __hip_atomic_fetch_add((LAS unsigned*)(L + off), 1u, __ATOMIC_RELAXED, __HIP_MEMORY_SCOPE_WORKGROUP);
}

__device__ __forceinline__ void dma1(unsigned dst, unsigned voff, const unsigned char* base) {
  unsigned keep;
  asm volatile("s_mov_b32 %0, m0\n\ts_mov_b32 m0, %1\n\ts_nop 0\n\tglobal_load_lds_dwordx4 %2, %3 nt\n\ts_mov_b32 m0, %0\n\ts_waitcnt vmcnt(%4)"
               : "=&s"(keep) : "s"(dst), "v"(voff), "s"(base), "i"(WIN_LD) : "memory");
}
__device__ __forceinline__ void dma4(unsigned dst, unsigned v0, unsigned v1, unsigned v2, unsigned v3, const unsigned char* base) {
  unsigned keep;
  asm volatile("s_mov_b32 %0, m0\n\ts_mov_b32 m0, %1\n\ts_nop 0\n\t"
               "global_load_lds_dwordx4 %2, %6 nt\n\ts_waitcnt vmcnt(%7)\n\t"
               "global_load_lds_dwordx4 %3, %6 offset:1024 nt\n\ts_waitcnt vmcnt(%7)\n\t"
               "global_load_lds_dwordx4 %4, %6 offset:2048 nt\n\ts_waitcnt vmcnt(%7)\n\t"
               "global_load_lds_dwordx4 %5, %6 offset:3072 nt\n\ts_waitcnt vmcnt(%7)\n\t"
               "s_mov_b32 m0, %0"
               : "=&s"(keep) : "s"(dst), "v"(v0), "v"(v1), "v"(v2), "v"(v3), "s"(base), "i"(WIN_LD) : "memory");
}
template <int N> __device__ __forceinline__ void vm_wait() { asm volatile("s_waitcnt vmcnt(%0)" :: "i"(N) : "memory"); }

__device__ __forceinline__ u32x2 tile_scores(const u32x4* K, const u32x4 (&q)[4]) {
  f32x4 s = {0.f, 0.f, 0.f, 0.f};
#pragma unroll
  for (int i = 0; i < 4; ++i) s = __builtin_amdgcn_mfma_f32_16x16x32_bf16(__builtin_bit_cast(bf16x8, K[i]), __builtin_bit_cast(bf16x8, q[i]), s, 0, 0, 0);
  const unsigned r01 = cvtpk(s[0], s[1]), r23 = cvtpk(s[2], s[3]);
  return u32x2{cvtpk(lo16(r01) * SCALE, hi16(r01) * SCALE), cvtpk(lo16(r23) * SCALE, hi16(r23) * SCALE)};
}
__device__ __forceinline__ float max4(u32x2 t) { return fmaxf(fmaxf(lo16(t[0]), hi16(t[0])), fmaxf(lo16(t[1]), hi16(t[1]))); }
__device__ __forceinline__ u32x2 to_probs(u32x2 t, float m2, float& l) {
  const float p0 = __builtin_amdgcn_exp2f(fmaf(lo16(t[0]), LOG2E, -m2)), p1 = __builtin_amdgcn_exp2f(fmaf(hi16(t[0]), LOG2E, -m2));
  const float p2 = __builtin_amdgcn_exp2f(fmaf(lo16(t[1]), LOG2E, -m2)), p3 = __builtin_amdgcn_exp2f(fmaf(hi16(t[1]), LOG2E, -m2));
  l += (p0 + p1) + (p2 + p3);
  return u32x2{cvtpk(p0, p1), cvtpk(p2, p3)};
}
struct Sel { unsigned l0, l1, h0, h1; };
__device__ __forceinline__ void tile_pv(f32x4& a0, f32x4& a1, const u32x4* V, u32x2 p, const Sel& s) {
#pragma unroll
  for (int r = 0; r < 4; ++r) {
    const unsigned pk = p[r >> 1];
    const u32x2 a = {__builtin_amdgcn_perm(0u, pk, (r & 1) ? s.h0 : s.l0), __builtin_amdgcn_perm(0u, pk, (r & 1) ? s.h1 : s.l1)};
    const u32x2 v0 = {V[r][0], V[r][1]}, v1 = {V[r][2], V[r][3]};
    a0 = __builtin_amdgcn_mfma_f32_16x16x16bf16_1k(__builtin_bit_cast(s16x4, a), __builtin_bit_cast(s16x4, v0), a0, 0, 0, 0);
    a1 = __builtin_amdgcn_mfma_f32_16x16x16bf16_1k(__builtin_bit_cast(s16x4, a), __builtin_bit_cast(s16x4, v1), a1, 0, 0, 0);
  }
}

#define STAMP(i) do {} while (0)
#define WSTAMP(i) do {} while (0)
#define DUMP_STAMPS() do {} while (0)

__global__ void __launch_bounds__(NTHR, 3) fused_decode(Args a) {
  __shared__ __attribute__((aligned(16))) unsigned char lds_raw[L_END];
  LAS unsigned char* lds = (LAS unsigned char*)lds_raw;
  const int b = blockIdx.x, h = b & 7, j = b >> 3;
  const int tid = threadIdx.x, lane = tid & 63, w = __builtin_amdgcn_readfirstlane(tid >> 6);
  const int hh = lane >> 4, i16 = lane & 15, jj = lane & 3;
  const int qcb = j >> 1, qrh = j & 1, qbr = qcb >> 3;

  if (w == NC) {
    const unsigned ring = (unsigned)(size_t)(lds + L_RING);
    int pub = 0, pslot = 0, slot = 0;
    auto publish1 = [&]() { lds_st(lds, L_FULL + 4 * pslot, (unsigned)(pub + 1)); ++pub; pslot = pslot + 1 == NS ? 0 : pslot + 1; };
    auto pre = [&](int q) {
      if (q >= NS) {
        const unsigned need = (unsigned)(q - NS + 1);
        if (lds_ld(lds, L_FREE + 4 * slot) < need) {
          vm_wait<0>();
          while (pub < q) publish1();
          lds_wait_ge<1>(lds, L_FREE + 4 * slot, need);
        }
      }
    };
    auto post = [&](int q) {
      if (q - pub >= LAG) publish1();
      slot = slot + 1 == NS ? 0 : slot + 1;
    };
    {
      const unsigned char* wq = (qbr ? a.wqB : a.wqA) + (size_t)(h * 512 + 64 * (qcb & 7)) * 2 + (size_t)qrh * 256 * 8192;
      const unsigned vo = (lane >> 3) * 8192 + (lane & 7) * 16;
#pragma unroll
      for (int q = 0; q < 4; ++q) {
#pragma unroll
        for (int m = 0; m < 8; ++m) dma1(ring + q * SLOT + m * 1024, vo, wq + (size_t)(q * 64 + m * 8) * 8192);
        if (q == 0) LDS_BARRIER();
        post(q);
      }
    }
    const unsigned k0 = hh * 256 + ((i16 ^ (0 + hh)) * 16), k1 = hh * 256 + ((i16 ^ (4 + hh)) * 16), k2 = hh * 256 + ((i16 ^ (8 + hh)) * 16), k3 = hh * 256 + ((i16 ^ (12 + hh)) * 16);
    const unsigned vl = lane * 16;
    {
      const unsigned char* kp = a.kB + ((size_t)h * SK + (SK - WIN) + j * 128) * 256;
      const unsigned char* vp = a.vB + ((size_t)h * SK + (SK - WIN) + j * 128) * 256;
      for (int q = 4; q < 12; ++q) {
        dma4(ring + slot * SLOT, k0, k1, k2, k3, kp);
        dma4(ring + slot * SLOT + 4096, vl, vl, vl, vl, vp);
        kp += 4096; vp += 4096;
        post(q);
      }
      kp = a.kA + ((size_t)h * SK + j * 2048) * 256;
      for (int q = 12; q < 76; ++q) {
        pre(q);
        dma4(ring + slot * SLOT, k0, k1, k2, k3, kp);
        dma4(ring + slot * SLOT + 4096, k0, k1, k2, k3, kp + 4096);
        kp += 8192;
        post(q);
      }
      WSTAMP(12);
      vp = a.vA + ((size_t)h * SK + j * 2048) * 256;
      for (int q = 76; q < 140; ++q) {
        pre(q);
        dma4(ring + slot * SLOT, vl, vl, vl, vl, vp);
        dma4(ring + slot * SLOT + 4096, vl, vl, vl, vl, vp + 4096);
        vp += 8192;
        post(q);
      }
      WSTAMP(9);
      vp = (j >> 4 ? a.woB : a.woA) + (size_t)(h * 512 + 32 * (j & 15)) * 1024;
      for (int q = 140; q < 144; ++q) {
        pre(q);
        dma4(ring + slot * SLOT, vl, vl, vl, vl, vp);
        dma4(ring + slot * SLOT + 4096, vl, vl, vl, vl, vp + 4096);
        vp += 8192;
        post(q);
      }
    }
    if (NSEQ - pub > 6) { vm_wait<48>(); publish1(); }
    if (NSEQ - pub > 5) { vm_wait<40>(); publish1(); }
    if (NSEQ - pub > 4) { vm_wait<32>(); publish1(); }
    if (NSEQ - pub > 3) { vm_wait<24>(); publish1(); }
    if (NSEQ - pub > 2) { vm_wait<16>(); publish1(); }
    if (NSEQ - pub > 1) { vm_wait<8>(); publish1(); }
    vm_wait<0>();
    while (pub < NSEQ) publish1();
    WSTAMP(10);
    return;
  }

  const __amdgpu_buffer_rsrc_t rs = __builtin_amdgcn_make_buffer_rsrc((void*)a.ws, 0, (int)WS_END, 0x00020000);
  const u32x2 gen = __builtin_amdgcn_raw_buffer_load_b64(rs, WS_GEN, 0, AUX_SC1);
  unsigned short xv[4];
  {
    const GAS unsigned char* xp = (const GAS unsigned char*)(qbr ? a.xB : a.xA) + 2 * (qrh * 256 + w * 32 + (lane >> 3));
#pragma unroll
    for (int i = 0; i < 4; ++i) xv[i] = *(const GAS unsigned short*)(xp + 16 * i);
  }
  if (tid < (L_RED - L_FULL) / 4) *(LAS unsigned*)(lds + L_FULL + 4 * tid) = 0u;
  LDS_BARRIER();

  lds_wait_ge<1>(lds, L_FULL + 4 * (w >> 1), (unsigned)((w >> 1) + 1));
  {
    u32x4 wv[4];
#pragma unroll
    for (int i = 0; i < 4; ++i) wv[i] = *(const LAS u32x4*)(lds + L_RING + (w >> 1) * SLOT + (w & 1) * 4096 + i * 1024 + lane * 16);
    float acc[8];
#pragma unroll
    for (int k = 0; k < 8; ++k) acc[k] = 0.f;
#pragma unroll
    for (int i = 0; i < 4; ++i) {
      const float x = lo16(xv[i]);
#pragma unroll
      for (int k = 0; k < 4; ++k) { acc[2 * k] = fmaf(x, lo16(wv[i][k]), acc[2 * k]); acc[2 * k + 1] = fmaf(x, hi16(wv[i][k]), acc[2 * k + 1]); }
    }
#pragma unroll
    for (int k = 0; k < 8; ++k) { float v = acc[k]; v += dpp<ROR8>(v); v = swap16_sum(v); v = swap32_sum(v); acc[k] = v; }
    if (lane < 8) {
      LAS f32x4* red = (LAS f32x4*)(lds + L_RED + w * 256 + lane * 32);
      red[0] = f32x4{acc[0], acc[1], acc[2], acc[3]}; red[1] = f32x4{acc[4], acc[5], acc[6], acc[7]};
    }
  }
  lds_arrive(lds, L_CNT + C_QRED, lane);
  const unsigned tagA = __builtin_amdgcn_readfirstlane(gen[0]) + 1u, tagB = __builtin_amdgcn_readfirstlane(gen[1]) + 1u;
  if (w == 0) {
    lds_wait_ge<0>(lds, L_CNT + C_QRED, NC);
    if (lane < 4) lds_st(lds, L_FREE + 4 * lane, (unsigned)(lane + 1));
    if (lane < 32) {
      f32x2 s = {0.f, 0.f};
#pragma unroll
      for (int ww = 0; ww < 8; ++ww) s += *(const LAS f32x2*)(lds + L_RED + ww * 256 + lane * 8);
      __builtin_amdgcn_raw_buffer_store_b128(u32x4{tagA, f2u(s[0]), f2u(s[1]), tagB}, rs, WS_QG + ((h * 32 + j) * 32 + lane) * 16, 0, AUX_SC1);
    }
  }
  STAMP(1);
  int slot = (4 + w) % NS;
  const unsigned kr0 = i16 * 256 + (((0 + hh) ^ i16) * 16), kr1 = i16 * 256 + (((4 + hh) ^ i16) * 16), kr2 = i16 * 256 + (((8 + hh) ^ i16) * 16), kr3 = i16 * 256 + (((12 + hh) ^ i16) * 16);
  const unsigned vr = hh * 1024 + i16 * 16;
  auto fetch = [&](int it, int kind, u32x4 (&T)[8]) {
    const unsigned seq1 = (unsigned)(4 + 8 * it + w + 1);
    lds_wait_ge<1>(lds, L_FULL + 4 * slot, seq1);
    const LAS unsigned char* sp = lds + L_RING + slot * SLOT;
    if (kind != 2) { T[0] = *(const LAS u32x4*)(sp + kr0); T[1] = *(const LAS u32x4*)(sp + kr1); T[2] = *(const LAS u32x4*)(sp + kr2); T[3] = *(const LAS u32x4*)(sp + kr3); }
    else {
#pragma unroll
      for (int r = 0; r < 4; ++r) T[r] = *(const LAS u32x4*)(sp + vr + r * 256);
    }
    if (kind == 1) { T[4] = *(const LAS u32x4*)(sp + 4096 + kr0); T[5] = *(const LAS u32x4*)(sp + 4096 + kr1); T[6] = *(const LAS u32x4*)(sp + 4096 + kr2); T[7] = *(const LAS u32x4*)(sp + 4096 + kr3); }
    else {
#pragma unroll
      for (int r = 0; r < 4; ++r) T[4 + r] = *(const LAS u32x4*)(sp + 4096 + vr + r * 256);
    }
    LDS_WAIT();
    lds_st(lds, L_FREE + 4 * slot, seq1);
    asm volatile("" : "+v"(T[0]), "+v"(T[1]), "+v"(T[2]), "+v"(T[3]), "+v"(T[4]), "+v"(T[5]), "+v"(T[6]), "+v"(T[7]) :: "memory");
    slot += 8; if (slot >= NS) slot -= NS;
  };
  u32x4 X0[8], X1[8], X2[8];
  fetch(0, 0, X0);
  if (PRE >= 2) fetch(1, 1, X1);
  if (PRE >= 3) fetch(2, 1, X2);
  {
    const unsigned off = WS_QG + ((h * 32 + 2 * (tid >> 5)) * 32 + (tid & 31)) * 16;
    u32x4 g0 = {0u, 0u, 0u, 0u}, g1 = {0u, 0u, 0u, 0u};
    bool ok = false;
    for (unsigned spin = 0; spin < SPIN_CAP; ++spin) {
      MEMFENCE();
      if (!ok) {
        g0 = __builtin_amdgcn_raw_buffer_load_b128(rs, off, 0, AUX_SC1);
        g1 = __builtin_amdgcn_raw_buffer_load_b128(rs, off + 512, 0, AUX_SC1);
        ok = g0[0] == tagA && g0[3] == tagB && g1[0] == tagA && g1[3] == tagB;
      }
      if (__all(ok)) break;
      if (QSLEEP) __builtin_amdgcn_s_sleep(QSLEEP);
    }
    *(LAS unsigned*)(lds + L_Q + tid * 4) = cvtpk(u2f(g0[1]) + u2f(g1[1]), u2f(g0[2]) + u2f(g1[2]));
  }
  lds_arrive(lds, L_CNT + C_QIN, lane);
  lds_wait_ge<0>(lds, L_CNT + C_QIN, NC);
  u32x4 qA[4], qB[4];
#pragma unroll
  for (int s = 0; s < 4; ++s) {
    qA[s] = *(const LAS u32x4*)(lds + L_Q + ((i16 >> 2) * 128 + 32 * s + 8 * hh) * 2);
    qB[s] = *(const LAS u32x4*)(lds + L_Q + 1024 + ((i16 >> 2) * 128 + 32 * s + 8 * hh) * 2);
  }
  STAMP(2);

  Sel sel;
  sel.l0 = jj == 0 ? 0x0c0c0100u : jj == 1 ? 0x01000c0cu : 0x0c0c0c0cu;  sel.h0 = jj == 0 ? 0x0c0c0302u : jj == 1 ? 0x03020c0cu : 0x0c0c0c0cu;
  sel.l1 = jj == 2 ? 0x0c0c0100u : jj == 3 ? 0x01000c0cu : 0x0c0c0c0cu;  sel.h1 = jj == 2 ? 0x0c0c0302u : jj == 3 ? 0x03020c0cu : 0x0c0c0c0cu;

  f32x4 b0 = {0.f, 0.f, 0.f, 0.f}, b1 = {0.f, 0.f, 0.f, 0.f};
  {
    const u32x2 t = tile_scores(X0, qB);
    const float m2 = swap32_max(swap16_max(max4(t))) * LOG2E;
    float l = 0.f;
    const u32x2 p = to_probs(t, m2, l);
    l = swap32_sum(swap16_sum(l));
    tile_pv(b0, b1, X0 + 4, p, sel);
    if (hh == 0 && jj == 0) *(LAS f32x2*)(lds + L_TB + ((i16 >> 2) * 8 + w) * 8) = f32x2{m2, l};
  }

  u32x2 P[16];
  float mx = -INFINITY;
#pragma unroll
  for (int i = 0; i < 8; ++i) {
    if (i == 0) { if (PRE < 2) fetch(1, 1, X1); }
    else if (i == 1) { if (PRE < 3) fetch(2, 1, X2); }
    else fetch(i + 1, 1, X1);
    const u32x4* T = i == 1 ? X2 : X1;
    P[2 * i] = tile_scores(T, qA);
    P[2 * i + 1] = tile_scores(T + 4, qA);
    mx = fmaxf(mx, fmaxf(max4(P[2 * i]), max4(P[2 * i + 1])));
  }
  STAMP(13);
  mx = swap32_max(swap16_max(mx));
  if (hh == 0 && jj == 0) *(LAS float*)(lds + L_T1 + ((i16 >> 2) * 8 + w) * 4) = mx;
  lds_arrive(lds, L_CNT + C_MAX, lane);
  lds_wait_ge<0>(lds, L_CNT + C_MAX, NC);
  {
    const f32x4 m0 = *(const LAS f32x4*)(lds + L_T1 + (i16 >> 2) * 32), m1 = *(const LAS f32x4*)(lds + L_T1 + (i16 >> 2) * 32 + 16);
    const float m2 = fmaxf(fmaxf(fmaxf(m0[0], m0[1]), fmaxf(m0[2], m0[3])), fmaxf(fmaxf(m1[0], m1[1]), fmaxf(m1[2], m1[3]))) * LOG2E;
    float l = 0.f;
#pragma unroll
    for (int t = 0; t < 16; ++t) P[t] = to_probs(P[t], m2, l);
    l = swap32_sum(swap16_sum(l));
    if (hh == 0 && jj == 0) *(LAS float*)(lds + L_T2 + ((i16 >> 2) * 8 + w) * 4) = l;
  }
  lds_arrive(lds, L_CNT + C_SUM, lane);
  lds_wait_ge<0>(lds, L_CNT + C_SUM, NC);
  const int mbr = w >> 2, mg = w & 3;
  {
    const int ww = lane & 7;
    float M, L;
    if (mbr == 0) {
      M = oct_max(*(const LAS float*)(lds + L_T1 + (mg * 8 + ww) * 4)) * LOG2E;
      L = oct_sum(*(const LAS float*)(lds + L_T2 + (mg * 8 + ww) * 4));
    } else {
      const f32x2 ml = *(const LAS f32x2*)(lds + L_TB + (mg * 8 + ww) * 8);
      M = oct_max(ml[0]);
      L = oct_sum(ml[1] * __builtin_amdgcn_exp2f(ml[0] - M));
    }
    if (lane == 0) __builtin_amdgcn_raw_buffer_store_b128(u32x4{tagA, f2u(M), f2u(L), tagB}, rs, WS_ML + (((h * 2 + mbr) * 4 + mg) * 32 + j) * 16, 0, AUX_SC1);
  }
  STAMP(14);

  f32x4 a0 = {0.f, 0.f, 0.f, 0.f}, a1 = {0.f, 0.f, 0.f, 0.f};
  float fA = 0.f, fB = 0.f;
#pragma unroll
  for (int i = 0; i < 8; ++i) {
    if (i == 4) {
      const unsigned off = WS_ML + (((h * 2 + mbr) * 4 + mg) * 32 + (lane & 31)) * 16;
      u32x4 mv = {0u, 0u, 0u, 0u};
      bool ok = false;
      for (unsigned spin = 0; spin < SPIN_CAP; ++spin) {
        MEMFENCE();
        if (!ok) { mv = __builtin_amdgcn_raw_buffer_load_b128(rs, off, 0, AUX_SC1); ok = mv[0] == tagA && mv[3] == tagB; }
        if (__all(ok)) break;
        __builtin_amdgcn_s_sleep(16);
      }
      const float M = swap16_max(row_max(u2f(mv[1])));
      const float L = swap16_sum(row_sum(u2f(mv[2]) * __builtin_amdgcn_exp2f(u2f(mv[1]) - M)));
      if (lane == 0) *(LAS f32x2*)(lds + L_FF + w * 8) = f32x2{M, 1.0f / L};
      lds_arrive(lds, L_CNT + C_FF, lane);
    }
    if (i == 7) {
      lds_wait_ge<0>(lds, L_CNT + C_FF, NC);
      const f32x4 m0 = *(const LAS f32x4*)(lds + L_T1 + hh * 32), m1 = *(const LAS f32x4*)(lds + L_T1 + hh * 32 + 16);
      const float m2 = fmaxf(fmaxf(fmaxf(m0[0], m0[1]), fmaxf(m0[2], m0[3])), fmaxf(fmaxf(m1[0], m1[1]), fmaxf(m1[2], m1[3]))) * LOG2E;
      const f32x2 FA = *(const LAS f32x2*)(lds + L_FF + hh * 8), FB = *(const LAS f32x2*)(lds + L_FF + (4 + hh) * 8);
      const float mB = *(const LAS float*)(lds + L_TB + (hh * 8 + w) * 8);
      fA = __builtin_amdgcn_exp2f(m2 - FA[0]) * FA[1];
      fB = __builtin_amdgcn_exp2f(mB - FB[0]) * FB[1];
      const int s = lane < MRG_SLOTS ? lane : 0;
      const unsigned need = (unsigned)(s + NS * ((139 - s) / NS) + 1);
      for (unsigned spin = 0; spin < LSPIN_CAP; ++spin) { MEMFENCE(); if (__all(*(volatile LAS unsigned*)(lds + L_FREE + 4 * s) >= need)) break; }
      MEMFENCE();
      LAS float* rB = (LAS float*)(lds + L_MRG) + ((8 + w) * 4 + hh) * MREC + 8 * i16;
      *(LAS f32x4*)(rB) = b0 * fB; *(LAS f32x4*)(rB + 4) = b1 * fB;
    }
    fetch(9 + i, 2, X1);
    tile_pv(a0, a1, X1, P[2 * i], sel);
    tile_pv(a0, a1, X1 + 4, P[2 * i + 1], sel);
  }
  STAMP(3);
  if (w == 7) WSTAMP(11);

  {
    LAS float* rA = (LAS float*)(lds + L_MRG) + (w * 4 + hh) * MREC + 8 * i16;
    *(LAS f32x4*)(rA) = a0 * fA; *(LAS f32x4*)(rA + 4) = a1 * fA;
  }
  lds_arrive(lds, L_CNT + C_MRG, lane);
  lds_wait_ge<0>(lds, L_CNT + C_MRG, NC);
  {
    const int br = tid >> 8, g = (tid >> 6) & 3, dp = lane;
    const LAS float* mrg = (const LAS float*)(lds + L_MRG) + (br * 8 * 4 + g) * MREC + 2 * dp;
    f32x2 o2[8];
#pragma unroll
    for (int ww = 0; ww < 8; ++ww) o2[ww] = *(const LAS f32x2*)(mrg + ww * 4 * MREC);
    const f32x2 o = ((o2[0] + o2[1]) + (o2[2] + o2[3])) + ((o2[4] + o2[5]) + (o2[6] + o2[7]));
    const int jc = br * 16 + g * 4 + (dp >> 4), k = dp & 15;
    __builtin_amdgcn_raw_buffer_store_b128(u32x4{tagA, f2u(o[0]), f2u(o[1]), tagB}, rs, WS_I1 + (((h * 32 + jc) * 32 + j) * 16 + k) * 16, 0, AUX_SC1);
  }
  STAMP(4);

  {
    const unsigned offA = WS_I1 + ((h * 32 + j) * 32 * 16 + w * 64 + lane) * 16;
    u32x4 av = {0u, 0u, 0u, 0u};
    bool ok = false;
    for (unsigned spin = 0; spin < SPIN_CAP; ++spin) {
      MEMFENCE();
      if (!ok) { av = __builtin_amdgcn_raw_buffer_load_b128(rs, offA, 0, AUX_SC1); ok = av[0] == tagA && av[3] == tagB; }
      if (__all(ok)) break;
      if (PSLEEP) __builtin_amdgcn_s_sleep(PSLEEP);
    }
    STAMP(5);
    const float o0 = swap32_sum(swap16_sum(u2f(av[1]))), o1 = swap32_sum(swap16_sum(u2f(av[2])));
    if (lane < 16) *(LAS f32x2*)(lds + L_H1 + (w * 32 + 2 * lane) * 4) = f32x2{o0, o1};
  }
  LDS_BARRIER();
  {
    const float o = oct_sum(*(const LAS float*)(lds + L_H1 + ((lane & 7) * 32 + 4 * w + ((lane >> 3) & 3)) * 4));
    const unsigned ob = cvtpk(o, 0.f) << 16;
    const float orow[4] = {u2f(__builtin_amdgcn_readlane(ob, 0)), u2f(__builtin_amdgcn_readlane(ob, 8)), u2f(__builtin_amdgcn_readlane(ob, 16)), u2f(__builtin_amdgcn_readlane(ob, 24))};
    const int wseq = 140 + (w >> 1);
    lds_wait_ge<0>(lds, L_FULL + 4 * (wseq % NS), (unsigned)(wseq + 1));
    const LAS unsigned char* wp = lds + L_RING + (wseq % NS) * SLOT + (w & 1) * 4096 + lane * 16;
    float y[8];
#pragma unroll
    for (int k = 0; k < 8; ++k) y[k] = 0.f;
#pragma unroll
    for (int r4 = 0; r4 < 4; ++r4) {
      const u32x4 wv = *(const LAS u32x4*)(wp + r4 * 1024);
#pragma unroll
      for (int k = 0; k < 4; ++k) { y[2 * k] = fmaf(orow[r4], lo16(wv[k]), y[2 * k]); y[2 * k + 1] = fmaf(orow[r4], hi16(wv[k]), y[2 * k + 1]); }
    }
    LAS f32x4* yl = (LAS f32x4*)(lds + L_Y + w * 2048 + lane * 32);
    yl[0] = f32x4{y[0], y[1], y[2], y[3]}; yl[1] = f32x4{y[4], y[5], y[6], y[7]};
  }
  LDS_BARRIER();
  if (tid < 256) {
    const LAS unsigned char* yl = lds + L_Y + tid * 8;
    f32x2 s2[8];
#pragma unroll
    for (int k = 0; k < 8; ++k) s2[k] = *(const LAS f32x2*)(yl + k * 2048);
    const f32x2 s = ((s2[0] + s2[1]) + (s2[2] + s2[3])) + ((s2[4] + s2[5]) + (s2[6] + s2[7]));
    const int c = (j >> 4) * 32 + (tid >> 3), k = tid & 7, pidx = h * 16 + (j & 15);
    __builtin_amdgcn_raw_buffer_store_b128(u32x4{tagA, f2u(s[0]), f2u(s[1]), tagB}, rs, WS_I2 + ((c * 128 + pidx) * 8 + k) * 16, 0, AUX_SC1);
  }
  STAMP(6);

  if (j < 8) {
    const int c = h * 8 + j;
    const unsigned off = WS_I2 + (c * 128 + 16 * w) * 128 + lane * 16;
    u32x4 v[2];
    bool ok = false;
    for (unsigned spin = 0; spin < SPIN_CAP; ++spin) {
      MEMFENCE();
      if (!ok) {
#pragma unroll
        for (int i = 0; i < 2; ++i) v[i] = __builtin_amdgcn_raw_buffer_load_b128(rs, off + i * 1024, 0, AUX_SC1);
        ok = v[0][0] == tagA && v[0][3] == tagB && v[1][0] == tagA && v[1][3] == tagB;
      }
      if (__all(ok)) break;
      if (PSLEEP) __builtin_amdgcn_s_sleep(PSLEEP);
    }
    STAMP(7);
    float s0 = u2f(v[0][1]) + u2f(v[1][1]);
    float s1 = u2f(v[0][2]) + u2f(v[1][2]);
    s0 += dpp<ROR8>(s0); s1 += dpp<ROR8>(s1);
    s0 = swap32_sum(swap16_sum(s0)); s1 = swap32_sum(swap16_sum(s1));
    if (lane < 8) *(LAS f32x2*)(lds + L_F + w * 64 + lane * 8) = f32x2{s0, s1};
    LDS_BARRIER();
    if (tid < 8) {
      f32x2 s = *(const LAS f32x2*)(lds + L_F + tid * 8);
#pragma unroll
      for (int ww = 1; ww < 8; ++ww) s += *(const LAS f32x2*)(lds + L_F + ww * 64 + tid * 8);
      { const __amdgpu_buffer_rsrc_t ro = __builtin_amdgcn_make_buffer_rsrc((void*)a.out, 0, 2048, 0x00020000);
        __builtin_amdgcn_raw_buffer_store_b32(cvtpk(s[0], s[1]), ro, (c * 16 + 2 * tid) * 2, 0, 17); }
      if (c == 0 && tid == 0) __builtin_amdgcn_raw_buffer_store_b64(u32x2{tagA, tagB}, rs, WS_GEN, 0, AUX_SC1);
    }
  }
  DUMP_STAMPS();
}

extern "C" void kernel_launch(void* const* d_in, const int* in_sizes, int n_in, void* d_out, int out_size, void* d_ws, size_t ws_size, hipStream_t stream) {
  Args a;
  a.xA = (const unsigned char*)d_in[0]; a.kA = (const unsigned char*)d_in[1]; a.vA = (const unsigned char*)d_in[2]; a.wqA = (const unsigned char*)d_in[3]; a.woA = (const unsigned char*)d_in[4];
  a.xB = (const unsigned char*)d_in[5]; a.kB = (const unsigned char*)d_in[6]; a.vB = (const unsigned char*)d_in[7]; a.wqB = (const unsigned char*)d_in[8]; a.woB = (const unsigned char*)d_in[9];
  a.out = (unsigned short*)d_out;
  a.ws = (unsigned char*)d_ws;
  hipLaunchKernelGGL(fused_decode, dim3(NWG), dim3(NTHR), 0, stream, a);
}
```

```cpp
#define AMP 0
#include <hip/hip_runtime.h>
#include <hip/hip_bf16.h>
#include <stdint.h>

#define GAS __attribute__((address_space(1)))
#define LAS __attribute__((address_space(3)))
typedef unsigned u32x4 __attribute__((ext_vector_type(4)));
typedef unsigned u32x2 __attribute__((ext_vector_type(2)));
typedef float f32x4 __attribute__((ext_vector_type(4)));
typedef float f32x2 __attribute__((ext_vector_type(2)));
typedef __bf16 bf16x8 __attribute__((ext_vector_type(8)));
typedef short s16x4 __attribute__((ext_vector_type(4)));

constexpr int D = 512, HQ = 32, HKV = 8, SK = 65536, DH = 128, WIN = 4096;
constexpr int NWG = 256, NTHR = 512;
constexpr float LOG2E = 1.4426950408889634f;
constexpr float SCALE = 0.08838834764831845f;

constexpr size_t WS_Q = 0;
constexpr size_t WS_PART = 65536;
constexpr int PREC = 4 * 132;
constexpr size_t WS_Y = WS_PART + (size_t)NWG * 2 * PREC * 4;

__device__ __forceinline__ float bf2f(unsigned short b) { return __builtin_bit_cast(float, (unsigned)b << 16); }
typedef __bf16 bf16x2 __attribute__((ext_vector_type(2)));
__device__ __forceinline__ unsigned cvtpk(float lo, float hi) { f32x2 f = {lo, hi}; return __builtin_bit_cast(unsigned, __builtin_convertvector(f, bf16x2)); }
__device__ __forceinline__ float rbf(float x) { return __builtin_bit_cast(float, cvtpk(x, x) << 16); }
__device__ __forceinline__ float lo16(unsigned u) { return __builtin_bit_cast(float, u << 16); }
__device__ __forceinline__ float hi16(unsigned u) { return __builtin_bit_cast(float, u & 0xffff0000u); }

__global__ void __launch_bounds__(NTHR, 2) k_qproj(const unsigned short* xA, const unsigned short* wqA, const unsigned short* xB, const unsigned short* wqB, unsigned char* ws) {
  __shared__ float red[8][32];
  if (blockIdx.x == 0 && threadIdx.x == 0) *(unsigned long long*)(ws + (64u << 20)) = __builtin_amdgcn_s_memrealtime();
  const int b = blockIdx.x, h = b & 7, j = b >> 3, br = j >> 4;
  const int tid = threadIdx.x, lane = tid & 63, w = tid >> 6;
  const unsigned short* x = br ? xB : xA;
  const unsigned short* wq = br ? wqB : wqA;
  const int col0 = h * 512 + 32 * (j & 15);
  const int c4 = lane & 3, rr = lane >> 2;
  float acc[8];
#pragma unroll
  for (int k = 0; k < 8; ++k) acc[k] = 0.f;
#pragma unroll
  for (int i = 0; i < 4; ++i) {
    const int row = w * 64 + i * 16 + rr;
    const u32x4 v = *(const u32x4*)(wq + (size_t)row * 4096 + col0 + 8 * c4);
    const float xv = bf2f(x[row]);
#pragma unroll
    for (int k = 0; k < 4; ++k) { acc[2 * k] = fmaf(xv, lo16(v[k]), acc[2 * k]); acc[2 * k + 1] = fmaf(xv, hi16(v[k]), acc[2 * k + 1]); }
  }
#pragma unroll
  for (int k = 0; k < 8; ++k) {
#pragma unroll
    for (int m = 4; m < 64; m <<= 1) acc[k] += __shfl_xor(acc[k], m);
  }
  if (lane < 4) {
#pragma unroll
    for (int k = 0; k < 8; ++k) red[w][8 * lane + k] = acc[k];
  }
  __syncthreads();
  if (tid < 32) {
    float s = 0.f;
#pragma unroll
    for (int ww = 0; ww < 8; ++ww) s += red[ww][tid];
    ((unsigned short*)(ws + WS_Q))[br * 4096 + col0 + tid] = (unsigned short)(cvtpk(s, s) & 0xffffu);
  }
}

struct St { float m, l; f32x4 a0, a1; };

template <bool MIMIC>
__device__ __forceinline__ void tile(St& st, const u32x4 (&K)[4], const u32x4 (&V)[4], const u32x4 (&q)[4], unsigned mask0, unsigned mask1, int lane) {
  f32x4 s = {0.f, 0.f, 0.f, 0.f};
#pragma unroll
  for (int i = 0; i < 4; ++i) s = __builtin_amdgcn_mfma_f32_16x16x32_bf16(__builtin_bit_cast(bf16x8, K[i]), __builtin_bit_cast(bf16x8, q[i]), s, 0, 0, 0);
  float t[4];
#pragma unroll
  for (int r = 0; r < 4; ++r) {
    if (MIMIC) t[r] = rbf(rbf(s[r]) * SCALE) * LOG2E; else t[r] = s[r] * (SCALE * LOG2E);
  }
  float mx = fmaxf(fmaxf(t[0], t[1]), fmaxf(t[2], t[3]));
  mx = fmaxf(mx, __shfl_xor(mx, 16));
  mx = fmaxf(mx, __shfl_xor(mx, 32));
  const float mn = fmaxf(st.m, mx);
  const float alpha = __builtin_amdgcn_exp2f(st.m - mn);
  st.m = mn;
  float p[4];
#pragma unroll
  for (int r = 0; r < 4; ++r) p[r] = __builtin_amdgcn_exp2f(t[r] - mn);
  st.l = st.l * alpha + ((p[0] + p[1]) + (p[2] + p[3]));
  const float al = __shfl(alpha, (lane >> 4) * 4);
  st.a0 *= al; st.a1 *= al;
#pragma unroll
  for (int r = 0; r < 4; ++r) {
    const unsigned x = cvtpk(p[r], p[r]);
    u32x2 a = {x & mask0, x & mask1};
    u32x2 v0 = {V[r][0], V[r][1]}, v1 = {V[r][2], V[r][3]};
    st.a0 = __builtin_amdgcn_mfma_f32_16x16x16bf16_1k(__builtin_bit_cast(s16x4, a), __builtin_bit_cast(s16x4, v0), st.a0, 0, 0, 0);
    st.a1 = __builtin_amdgcn_mfma_f32_16x16x16bf16_1k(__builtin_bit_cast(s16x4, a), __builtin_bit_cast(s16x4, v1), st.a1, 0, 0, 0);
  }
}

template <bool MIMIC>
__global__ void __launch_bounds__(NTHR, 2) k_attn(const unsigned short* kA, const unsigned short* vA, const unsigned short* kB, const unsigned short* vB, unsigned char* ws) {
  __shared__ float mrg[8][4][132];
  const int b = blockIdx.x, h = b & 7, j = b >> 3;
  const int tid = threadIdx.x, lane = tid & 63, w = __builtin_amdgcn_readfirstlane(tid >> 6);
  const int hh = lane >> 4, i16 = lane & 15, g = i16 >> 2, jj = lane & 3;
  const unsigned mask0 = jj == 0 ? 0x0000ffffu : jj == 1 ? 0xffff0000u : 0u;
  const unsigned mask1 = jj == 2 ? 0x0000ffffu : jj == 3 ? 0xffff0000u : 0u;
  const unsigned short* qb = (const unsigned short*)(ws + WS_Q);
  float* part = (float*)(ws + WS_PART);
  for (int br = 0; br < 2; ++br) {
    u32x4 q[4];
#pragma unroll
    for (int s = 0; s < 4; ++s) q[s] = *(const u32x4*)(qb + br * 4096 + (h * 4 + g) * 128 + 32 * s + 8 * hh);
    const unsigned short* kp = (br ? kB : kA) + (size_t)h * SK * DH;
    const unsigned short* vp = (br ? vB : vA) + (size_t)h * SK * DH;
    const int nt = br ? 1 : 16;
    const int key0 = br ? (SK - WIN + j * 128) : j * 2048;
    St st; st.m = -INFINITY; st.l = 0.f; st.a0 = f32x4{0, 0, 0, 0}; st.a1 = f32x4{0, 0, 0, 0};
    for (int it = 0; it < nt; ++it) {
      const int t0 = key0 + (it * 8 + w) * 16;
      u32x4 K[4], V[4];
#pragma unroll
      for (int s = 0; s < 4; ++s) K[s] = __builtin_nontemporal_load((const u32x4*)(kp + (size_t)(t0 + i16) * DH + 32 * s + 8 * hh));
#pragma unroll
      for (int m = 0; m < 4; ++m) V[m] = __builtin_nontemporal_load((const u32x4*)(vp + (size_t)(t0 + 4 * hh + m) * DH + 8 * i16));
      tile<MIMIC>(st, K, V, q, mask0, mask1, lane);
    }
    float l = st.l; l += __shfl_xor(l, 16); l += __shfl_xor(l, 32);
    __syncthreads();
#pragma unroll
    for (int r = 0; r < 4; ++r) { mrg[w][hh][4 + 8 * i16 + r] = st.a0[r]; mrg[w][hh][4 + 8 * i16 + 4 + r] = st.a1[r]; }
    if (hh == 0 && jj == 0) { mrg[w][g][0] = st.m; mrg[w][g][1] = l; }
    __syncthreads();
    {
      const int gg = tid >> 7, d = tid & 127;
      float M = -INFINITY;
#pragma unroll
      for (int ww = 0; ww < 8; ++ww) M = fmaxf(M, mrg[ww][gg][0]);
      float L = 0.f, o = 0.f;
#pragma unroll
      for (int ww = 0; ww < 8; ++ww) { const float wt = __builtin_amdgcn_exp2f(mrg[ww][gg][0] - M); L = fmaf(wt, mrg[ww][gg][1], L); o = fmaf(wt, mrg[ww][gg][4 + d], o); }
      float* rec = part + (size_t)(b * 2 + br) * PREC + gg * 132;
      rec[4 + d] = o;
      if (d == 0) { rec[0] = M; rec[1] = L; }
    }
  }
}

__global__ void __launch_bounds__(NTHR, 2) k_comb(const unsigned short* woA, const unsigned short* woB, unsigned char* ws) {
  __shared__ float osh[2][16];
  __shared__ float red[8][512];
  const int b = blockIdx.x, h = b & 7, j = b >> 3;
  const int tid = threadIdx.x, lane = tid & 63, w = tid >> 6;
  const float* part = (const float*)(ws + WS_PART);
  const int g = j >> 3, d0 = 16 * (j & 7);
  if (tid < 32) {
    const int br = tid >> 4, d = d0 + (tid & 15);
    float M = -INFINITY;
    for (int p = 0; p < 32; ++p) M = fmaxf(M, part[(size_t)((p * 8 + h) * 2 + br) * PREC + g * 132]);
    float L = 0.f, o = 0.f;
    for (int p = 0; p < 32; ++p) { const float* rec = part + (size_t)((p * 8 + h) * 2 + br) * PREC + g * 132; const float wt = __builtin_amdgcn_exp2f(rec[0] - M); L = fmaf(wt, rec[1], L); o = fmaf(wt, rec[4 + d], o); }
    osh[br][tid & 15] = rbf(o / L);
  }
  __syncthreads();
  const int br = w >> 2, kq = w & 3;
  const unsigned short* wo = (br ? woB : woA) + (size_t)(h * 512 + 16 * j + 4 * kq) * 512;
  float y[8];
#pragma unroll
  for (int k = 0; k < 8; ++k) y[k] = 0.f;
#pragma unroll
  for (int r = 0; r < 4; ++r) {
    const u32x4 v = *(const u32x4*)(wo + r * 512 + 8 * lane);
    const float o = osh[br][4 * kq + r];
#pragma unroll
    for (int k = 0; k < 4; ++k) { y[2 * k] = fmaf(o, lo16(v[k]), y[2 * k]); y[2 * k + 1] = fmaf(o, hi16(v[k]), y[2 * k + 1]); }
  }
#pragma unroll
  for (int k = 0; k < 8; ++k) red[w][8 * lane + k] = y[k];
  __syncthreads();
  float* Y = (float*)(ws + WS_Y) + (size_t)b * 1024;
  for (int u = tid; u < 1024; u += NTHR) { const int bb = u >> 9, c = u & 511; Y[u] = (red[bb * 4][c] + red[bb * 4 + 1][c]) + (red[bb * 4 + 2][c] + red[bb * 4 + 3][c]); }
}

__global__ void __launch_bounds__(256) k_red(const unsigned char* ws, unsigned short* out) {
  __shared__ float red[8][32];
  const int c = blockIdx.x * 32 + (threadIdx.x & 31), pg = threadIdx.x >> 5;
  const float* Y = (const float*)(ws + WS_Y);
  float s = 0.f;
  for (int p = pg; p < 256; p += 8) s += Y[(size_t)p * 1024 + c];
  red[pg][threadIdx.x & 31] = s;
  __syncthreads();
  if (threadIdx.x < 32) { float t = 0.f; for (int k = 0; k < 8; ++k) t += red[k][threadIdx.x]; out[c] = (unsigned short)(cvtpk(t, t) & 0xffffu); }
  if (AMP && blockIdx.x == 0 && threadIdx.x == 0) { const unsigned long long t0 = *(const volatile unsigned long long*)(ws + (64u << 20)); const unsigned long long t1 = __builtin_amdgcn_s_memrealtime();
    unsigned long long d = 4 * (t1 - t0); if (d > 100000ull) d = 100000ull;
    for (unsigned sp_ = 0; sp_ < 10000000u && __builtin_amdgcn_s_memrealtime() < t1 + d; ++sp_) __builtin_amdgcn_s_sleep(1); }
}

#define MIMIC_ROUND 1
extern "C" void kernel_launch(void* const* d_in, const int* in_sizes, int n_in, void* d_out, int out_size, void* d_ws, size_t ws_size, hipStream_t stream) {
  const unsigned short* xA = (const unsigned short*)d_in[0]; const unsigned short* kA = (const unsigned short*)d_in[1]; const unsigned short* vA = (const unsigned short*)d_in[2];
  const unsigned short* wqA = (const unsigned short*)d_in[3]; const unsigned short* woA = (const unsigned short*)d_in[4];
  const unsigned short* xB = (const unsigned short*)d_in[5]; const unsigned short* kB = (const unsigned short*)d_in[6]; const unsigned short* vB = (const unsigned short*)d_in[7];
  const unsigned short* wqB = (const unsigned short*)d_in[8]; const unsigned short* woB = (const unsigned short*)d_in[9];
  unsigned char* ws = (unsigned char*)d_ws;
  k_qproj<<<NWG, NTHR, 0, stream>>>(xA, wqA, xB, wqB, ws);
  k_attn<MIMIC_ROUND != 0><<<NWG, NTHR, 0, stream>>>(kA, vA, kB, vB, ws);
  k_comb<<<NWG, NTHR, 0, stream>>>(woA, woB, ws);
  k_red<<<32, 256, 0, stream>>>(ws, (unsigned short*)d_out);
}
```

```cpp
#include <hip/hip_runtime.h>
#include <hip/hip_bf16.h>
#include <stdint.h>

#define GAS __attribute__((address_space(1)))
#define LAS __attribute__((address_space(3)))
typedef unsigned u32x4 __attribute__((ext_vector_type(4)));
typedef unsigned u32x2 __attribute__((ext_vector_type(2)));
typedef float f32x4 __attribute__((ext_vector_type(4)));
typedef float f32x2 __attribute__((ext_vector_type(2)));
typedef __bf16 bf16x8 __attribute__((ext_vector_type(8)));
typedef __bf16 bf16x2 __attribute__((ext_vector_type(2)));
typedef short s16x4 __attribute__((ext_vector_type(4)));

#define NS 18
#define WIN_LD 22
#define PRE 3
#define QSLEEP 16
#define PSLEEP 0
#define HOLD 0
#define BENCH 0
#define STAMPS 0

constexpr int SK = 65536, WIN = 4096;
constexpr int NWG = 256, NC = 8, NTHR = (NC + 1) * 64;
constexpr int SLOT = 8192, NSEQ = 4 + 8 + 64 + 64 + 4;
constexpr int LAG = (WIN_LD + 7) / 8;
constexpr float LOG2E = 1.4426950408889634f;
constexpr float SCALE = 0.08838834764831845f;
constexpr unsigned SPIN_CAP = 1u << 20, LSPIN_CAP = 1u << 24;

constexpr unsigned WS_GEN = 0;
constexpr unsigned WS_QG = 4096;
constexpr unsigned WS_ML = WS_QG + 8 * 32 * 32 * 16;
constexpr unsigned WS_I1 = WS_ML + 256 * 128;
constexpr unsigned WS_I2 = WS_I1 + 8 * 32 * 32 * 128;
constexpr unsigned WS_END = WS_I2 + 32 * 128 * 128;
constexpr unsigned WS_STAMP = WS_END;

constexpr int L_RING = 0;
constexpr int L_FULL = NS * SLOT;
constexpr int L_FREE = L_FULL + 128;
constexpr int L_CNT = L_FREE + 128;
constexpr int L_RED = L_CNT + 64;
constexpr int L_Q = L_RED + 2048;
constexpr int L_T1 = L_Q + 2048;
constexpr int L_T2 = L_T1 + 128;
constexpr int L_TB = L_T2 + 128;
constexpr int L_FF = L_TB + 256;
constexpr int L_H1 = L_FF + 64;
constexpr int L_F = L_H1 + 512;
constexpr int L_END = L_F + 1024 + 128;
constexpr int C_QRED = 0, C_QIN = 4, C_MAX = 8, C_SUM = 12, C_FF = 16, C_MRG = 20;
constexpr int MREC = 132;
constexpr int L_MRG = 0;
constexpr int MRG_SLOTS = 5;
static_assert(L_END <= 160 * 1024, "LDS");
static_assert(2 * 8 * 4 * MREC * 4 <= MRG_SLOTS * SLOT, "merge area");
static_assert((140 % NS) >= MRG_SLOTS && (143 % NS) >= MRG_SLOTS && (140 % NS) <= (143 % NS), "wo slots contiguous and clear of the merge area");
static_assert((132 % NS) >= MRG_SLOTS && (139 % NS) >= MRG_SLOTS && (132 % NS) <= (139 % NS), "last round clear of the merge area");
static_assert(WIN_LD >= 9 && WIN_LD <= 48, "window");

__device__ __forceinline__ unsigned cvtpk(float lo, float hi) { f32x2 f = {lo, hi}; return __builtin_bit_cast(unsigned, __builtin_convertvector(f, bf16x2)); }
__device__ __forceinline__ float lo16(unsigned u) { return __builtin_bit_cast(float, u << 16); }
__device__ __forceinline__ float hi16(unsigned u) { return __builtin_bit_cast(float, u & 0xffff0000u); }
__device__ __forceinline__ float u2f(unsigned u) { return __builtin_bit_cast(float, u); }
__device__ __forceinline__ unsigned f2u(float f) { return __builtin_bit_cast(unsigned, f); }
template <int CTRL> __device__ __forceinline__ float dpp(float x) { return u2f(__builtin_amdgcn_mov_dpp(f2u(x), CTRL, 0xf, 0xf, true)); }
constexpr int XOR1 = 0xB1, XOR2 = 0x4E, HMIR = 0x141;
constexpr int ROR1 = 0x121, ROR2 = 0x122, ROR4 = 0x124, ROR8 = 0x128;
__device__ __forceinline__ float swap16_max(float x) { auto s = __builtin_amdgcn_permlane16_swap(f2u(x), f2u(x), false, false); return fmaxf(u2f(s[0]), u2f(s[1])); }
__device__ __forceinline__ float swap32_max(float x) { auto s = __builtin_amdgcn_permlane32_swap(f2u(x), f2u(x), false, false); return fmaxf(u2f(s[0]), u2f(s[1])); }
__device__ __forceinline__ float swap16_sum(float x) { auto s = __builtin_amdgcn_permlane16_swap(f2u(x), f2u(x), false, false); return u2f(s[0]) + u2f(s[1]); }
__device__ __forceinline__ float swap32_sum(float x) { auto s = __builtin_amdgcn_permlane32_swap(f2u(x), f2u(x), false, false); return u2f(s[0]) + u2f(s[1]); }
__device__ __forceinline__ float row_sum(float x) { x += dpp<ROR8>(x); x += dpp<ROR4>(x); x += dpp<ROR2>(x); x += dpp<ROR1>(x); return x; }
__device__ __forceinline__ float row_max(float x) { x = fmaxf(x, dpp<ROR8>(x)); x = fmaxf(x, dpp<ROR4>(x)); x = fmaxf(x, dpp<ROR2>(x)); x = fmaxf(x, dpp<ROR1>(x)); return x; }
template <int N> __device__ __forceinline__ float bcast(float x) { return dpp<0x150 + N>(x); }
__device__ __forceinline__ float oct_sum(float x) { x += dpp<XOR1>(x); x += dpp<XOR2>(x); x += dpp<HMIR>(x); return x; }
__device__ __forceinline__ float oct_max(float x) { x = fmaxf(x, dpp<XOR1>(x)); x = fmaxf(x, dpp<XOR2>(x)); x = fmaxf(x, dpp<HMIR>(x)); return x; }
#define LDS_BARRIER() asm volatile("s_waitcnt lgkmcnt(0)\n\ts_barrier" ::: "memory")
#define LDS_WAIT() asm volatile("s_waitcnt lgkmcnt(0)" ::: "memory")
#define MEMFENCE() asm volatile("" ::: "memory")
constexpr int AUX_SC1 = 16;

__device__ __forceinline__ u32x4 pack4(f32x4 v, unsigned tA, unsigned tB) {
  const unsigned t0 = f2u(v[0]) + 0x800u, t1 = f2u(v[1]) + 0x800u, t2 = f2u(v[2]) + 0x800u, t3 = f2u(v[3]) + 0x800u;
  return u32x4{tA | ((t0 << 12) & 0xff000000u), (t0 >> 20) | (t1 & 0xfffff000u), (t2 >> 12) | ((t3 << 8) & 0xfff00000u), (t3 >> 24) | (tB << 8)};
}
__device__ __forceinline__ f32x4 unpack4(u32x4 d) {
  return f32x4{u2f((d[1] << 20) | ((d[0] >> 12) & 0xff000u)), u2f(d[1] & 0xfffff000u), u2f(d[2] << 12), u2f(((d[2] >> 8) & 0xfff000u) | (d[3] << 24))};
}
__device__ __forceinline__ bool tags4(u32x4 d, unsigned tA, unsigned tB) { return (d[0] & 0xffffffu) == tA && (d[3] >> 8) == tB; }
__device__ __forceinline__ f32x4 red8_16_32(f32x4 v) {
#pragma unroll
  for (int e = 0; e < 4; ++e) { float x = v[e]; x += dpp<ROR8>(x); v[e] = swap32_sum(swap16_sum(x)); }
  return v;
}

struct Args {
  const unsigned char *xA, *kA, *vA, *wqA, *woA, *xB, *kB, *vB, *wqB, *woB;
  unsigned short* out;
  unsigned char* ws;
};

__device__ __forceinline__ unsigned lds_ld(LAS unsigned char* L, int off) { return __builtin_amdgcn_readfirstlane(*(volatile LAS unsigned*)(L + off)); }
__device__ __forceinline__ void lds_st(LAS unsigned char* L, int off, unsigned v) { *(volatile LAS unsigned*)(L + off) = v; }
template <int SLEEP> __device__ __forceinline__ void lds_wait_ge(LAS unsigned char* L, int off, unsigned need) {
  for (unsigned spin = 0; lds_ld(L, off) < need && spin < LSPIN_CAP; ++spin) { if (SLEEP) __builtin_amdgcn_s_sleep(SLEEP); }
  MEMFENCE();
}
__device__ __forceinline__ void lds_arrive(LAS unsigned char* L, int off, int lane) {
  LDS_WAIT();
  if (lane == 0) __hip_atomic_fetch_add((LAS unsigned*)(L + off), 1u, __ATOMIC_RELAXED, __HIP_MEMORY_SCOPE_WORKGROUP);
}

__device__ __forceinline__ void dma1(unsigned dst, unsigned voff, const unsigned char* base) {
  unsigned keep;
  asm volatile("s_mov_b32 %0, m0\n\ts_mov_b32 m0, %1\n\ts_nop 0\n\tglobal_load_lds_dwordx4 %2, %3 nt\n\ts_mov_b32 m0, %0\n\ts_waitcnt vmcnt(%4)"
               : "=&s"(keep) : "s"(dst), "v"(voff), "s"(base), "i"(WIN_LD) : "memory");
}
__device__ __forceinline__ void dma4(unsigned dst, unsigned v0, unsigned v1, unsigned v2, unsigned v3, const unsigned char* base) {
  unsigned keep;
  asm volatile("s_mov_b32 %0, m0\n\ts_mov_b32 m0, %1\n\ts_nop 0\n\t"
               "global_load_lds_dwordx4 %2, %6 nt\n\ts_waitcnt vmcnt(%7)\n\t"
               "global_load_lds_dwordx4 %3, %6 offset:1024 nt\n\ts_waitcnt vmcnt(%7)\n\t"
               "global_load_lds_dwordx4 %4, %6 offset:2048 nt\n\ts_waitcnt vmcnt(%7)\n\t"
               "global_load_lds_dwordx4 %5, %6 offset:3072 nt\n\ts_waitcnt vmcnt(%7)\n\t"
               "s_mov_b32 m0, %0"
               : "=&s"(keep) : "s"(dst), "v"(v0), "v"(v1), "v"(v2), "v"(v3), "s"(base), "i"(WIN_LD) : "memory");
}
template <int N> __device__ __forceinline__ void vm_wait() { asm volatile("s_waitcnt vmcnt(%0)" :: "i"(N) : "memory"); }

__device__ __forceinline__ u32x2 tile_scores(const u32x4* K, const u32x4 (&q)[4]) {
  f32x4 s = {0.f, 0.f, 0.f, 0.f};
#pragma unroll
  for (int i = 0; i < 4; ++i) s = __builtin_amdgcn_mfma_f32_16x16x32_bf16(__builtin_bit_cast(bf16x8, K[i]), __builtin_bit_cast(bf16x8, q[i]), s, 0, 0, 0);
  const unsigned r01 = cvtpk(s[0], s[1]), r23 = cvtpk(s[2], s[3]);
  return u32x2{cvtpk(lo16(r01) * SCALE, hi16(r01) * SCALE), cvtpk(lo16(r23) * SCALE, hi16(r23) * SCALE)};
}
__device__ __forceinline__ float max4(u32x2 t) { return fmaxf(fmaxf(lo16(t[0]), hi16(t[0])), fmaxf(lo16(t[1]), hi16(t[1]))); }
__device__ __forceinline__ u32x2 to_probs(u32x2 t, float m2, float& l) {
  const float p0 = __builtin_amdgcn_exp2f(fmaf(lo16(t[0]), LOG2E, -m2)), p1 = __builtin_amdgcn_exp2f(fmaf(hi16(t[0]), LOG2E, -m2));
  const float p2 = __builtin_amdgcn_exp2f(fmaf(lo16(t[1]), LOG2E, -m2)), p3 = __builtin_amdgcn_exp2f(fmaf(hi16(t[1]), LOG2E, -m2));
  l += (p0 + p1) + (p2 + p3);
  return u32x2{cvtpk(p0, p1), cvtpk(p2, p3)};
}
struct Sel { unsigned l0, l1, h0, h1; };
__device__ __forceinline__ void tile_pv(f32x4& a0, f32x4& a1, const u32x4* V, u32x2 p, const Sel& s) {
#pragma unroll
  for (int r = 0; r < 4; ++r) {
    const unsigned pk = p[r >> 1];
    const u32x2 a = {__builtin_amdgcn_perm(0u, pk, (r & 1) ? s.h0 : s.l0), __builtin_amdgcn_perm(0u, pk, (r & 1) ? s.h1 : s.l1)};
    const u32x2 v0 = {V[r][0], V[r][1]}, v1 = {V[r][2], V[r][3]};
    a0 = __builtin_amdgcn_mfma_f32_16x16x16bf16_1k(__builtin_bit_cast(s16x4, a), __builtin_bit_cast(s16x4, v0), a0, 0, 0, 0);
    a1 = __builtin_amdgcn_mfma_f32_16x16x16bf16_1k(__builtin_bit_cast(s16x4, a), __builtin_bit_cast(s16x4, v1), a1, 0, 0, 0);
  }
}

#define STAMP(i) do {} while (0)
#define WSTAMP(i) do {} while (0)
#define DUMP_STAMPS() do {} while (0)

__global__ void __launch_bounds__(NTHR, 3) fused_decode(Args a) {
  __shared__ __attribute__((aligned(16))) unsigned char lds_raw[L_END];
  LAS unsigned char* lds = (LAS unsigned char*)lds_raw;
  const int b = blockIdx.x, h = b & 7, j = b >> 3;
  const int tid = threadIdx.x, lane = tid & 63, w = __builtin_amdgcn_readfirstlane(tid >> 6);
  const int hh = lane >> 4, i16 = lane & 15, jj = lane & 3;
  const int qcb = j >> 1, qrh = j & 1, qbr = qcb >> 3;

  if (w == NC) {
    const unsigned ring = (unsigned)(size_t)(lds + L_RING);
    int pub = 0, pslot = 0, slot = 0;
    auto publish1 = [&]() { lds_st(lds, L_FULL + 4 * pslot, (unsigned)(pub + 1)); ++pub; pslot = pslot + 1 == NS ? 0 : pslot + 1; };
    auto pre = [&](int q) {
      if (q >= NS) {
        const unsigned need = (unsigned)(q - NS + 1);
        if (lds_ld(lds, L_FREE + 4 * slot) < need) {
          vm_wait<0>();
          while (pub < q) publish1();
          lds_wait_ge<1>(lds, L_FREE + 4 * slot, need);
        }
      }
    };
    auto post = [&](int q) {
      if (q - pub >= LAG) publish1();
      slot = slot + 1 == NS ? 0 : slot + 1;
    };
    {
      const unsigned char* wq = (qbr ? a.wqB : a.wqA) + (size_t)(h * 512 + 64 * (qcb & 7)) * 2 + (size_t)qrh * 256 * 8192;
      const unsigned vo = (lane >> 3) * 8192 + (lane & 7) * 16;
#pragma unroll
      for (int q = 0; q < 4; ++q) {
#pragma unroll
        for (int m = 0; m < 8; ++m) dma1(ring + q * SLOT + m * 1024, vo, wq + (size_t)(q * 64 + m * 8) * 8192);
        if (q == 0) LDS_BARRIER();
        post(q);
      }
    }
    const unsigned k0 = hh * 256 + ((i16 ^ (0 + hh)) * 16), k1 = hh * 256 + ((i16 ^ (4 + hh)) * 16), k2 = hh * 256 + ((i16 ^ (8 + hh)) * 16), k3 = hh * 256 + ((i16 ^ (12 + hh)) * 16);
    const unsigned vl = lane * 16;
    {
      const unsigned char* kp = a.kB + ((size_t)h * SK + (SK - WIN) + j * 128) * 256;
      const unsigned char* vp = a.vB + ((size_t)h * SK + (SK - WIN) + j * 128) * 256;
      for (int q = 4; q < 12; ++q) {
        dma4(ring + slot * SLOT, k0, k1, k2, k3, kp);
        dma4(ring + slot * SLOT + 4096, vl, vl, vl, vl, vp);
        kp += 4096; vp += 4096;
        post(q);
      }
      kp = a.kA + ((size_t)h * SK + j * 2048) * 256;
      for (int q = 12; q < 76; ++q) {
        pre(q);
        dma4(ring + slot * SLOT, k0, k1, k2, k3, kp);
        dma4(ring + slot * SLOT + 4096, k0, k1, k2, k3, kp + 4096);
        kp += 8192;
        post(q);
      }
      WSTAMP(12);
      vp = a.vA + ((size_t)h * SK + j * 2048) * 256;
      for (int q = 76; q < 140; ++q) {
        pre(q);
        dma4(ring + slot * SLOT, vl, vl, vl, vl, vp);
        dma4(ring + slot * SLOT + 4096, vl, vl, vl, vl, vp + 4096);
        vp += 8192;
        post(q);
      }
      WSTAMP(9);
      vp = (j >> 4 ? a.woB : a.woA) + (size_t)(h * 512 + 32 * (j & 15)) * 1024;
      for (int q = 140; q < 144; ++q) {
        pre(q);
        dma4(ring + slot * SLOT, vl, vl, vl, vl, vp);
        dma4(ring + slot * SLOT + 4096, vl, vl, vl, vl, vp + 4096);
        vp += 8192;
        post(q);
      }
    }
    __builtin_amdgcn_s_barrier();
    if (NSEQ - pub > 6) { vm_wait<48>(); publish1(); }
    if (NSEQ - pub > 5) { vm_wait<40>(); publish1(); }
    if (NSEQ - pub > 4) { vm_wait<32>(); publish1(); }
    if (NSEQ - pub > 3) { vm_wait<24>(); publish1(); }
    if (NSEQ - pub > 2) { vm_wait<16>(); publish1(); }
    if (NSEQ - pub > 1) { vm_wait<8>(); publish1(); }
    vm_wait<0>();
    while (pub < NSEQ) publish1();
    WSTAMP(10);
    return;
  }

  const __amdgpu_buffer_rsrc_t rs = __builtin_amdgcn_make_buffer_rsrc((void*)a.ws, 0, (int)WS_END, 0x00020000);
  const u32x2 gen = __builtin_amdgcn_raw_buffer_load_b64(rs, WS_GEN, 0, AUX_SC1);
  unsigned short xv[4];
  {
    const GAS unsigned char* xp = (const GAS unsigned char*)(qbr ? a.xB : a.xA) + 2 * (qrh * 256 + w * 32 + (lane >> 3));
#pragma unroll
    for (int i = 0; i < 4; ++i) xv[i] = *(const GAS unsigned short*)(xp + 16 * i);
  }
  if (tid < (L_RED - L_FULL) / 4) *(LAS unsigned*)(lds + L_FULL + 4 * tid) = 0u;
  LDS_BARRIER();

  lds_wait_ge<1>(lds, L_FULL + 4 * (w >> 1), (unsigned)((w >> 1) + 1));
  {
    u32x4 wv[4];
#pragma unroll
    for (int i = 0; i < 4; ++i) wv[i] = *(const LAS u32x4*)(lds + L_RING + (w >> 1) * SLOT + (w & 1) * 4096 + i * 1024 + lane * 16);
    float acc[8];
#pragma unroll
    for (int k = 0; k < 8; ++k) acc[k] = 0.f;
#pragma unroll
    for (int i = 0; i < 4; ++i) {
      const float x = lo16(xv[i]);
#pragma unroll
      for (int k = 0; k < 4; ++k) { acc[2 * k] = fmaf(x, lo16(wv[i][k]), acc[2 * k]); acc[2 * k + 1] = fmaf(x, hi16(wv[i][k]), acc[2 * k + 1]); }
    }
#pragma unroll
    for (int k = 0; k < 8; ++k) { float v = acc[k]; v += dpp<ROR8>(v); v = swap16_sum(v); v = swap32_sum(v); acc[k] = v; }
    if (lane < 8) {
      LAS f32x4* red = (LAS f32x4*)(lds + L_RED + w * 256 + lane * 32);
      red[0] = f32x4{acc[0], acc[1], acc[2], acc[3]}; red[1] = f32x4{acc[4], acc[5], acc[6], acc[7]};
    }
  }
  lds_arrive(lds, L_CNT + C_QRED, lane);
  const unsigned tagA = __builtin_amdgcn_readfirstlane(gen[0]) + 1u, tagB = __builtin_amdgcn_readfirstlane(gen[1]) + 1u;
  if (w == 0) {
    lds_wait_ge<0>(lds, L_CNT + C_QRED, NC);
    if (lane < 4) lds_st(lds, L_FREE + 4 * lane, (unsigned)(lane + 1));
    if (lane < 32) {
      f32x2 s = {0.f, 0.f};
#pragma unroll
      for (int ww = 0; ww < 8; ++ww) s += *(const LAS f32x2*)(lds + L_RED + ww * 256 + lane * 8);
      __builtin_amdgcn_raw_buffer_store_b128(u32x4{tagA, f2u(s[0]), f2u(s[1]), tagB}, rs, WS_QG + ((h * 32 + j) * 32 + lane) * 16, 0, AUX_SC1);
    }
  }
  STAMP(1);
  int slot = (4 + w) % NS;
  const unsigned kr0 = i16 * 256 + (((0 + hh) ^ i16) * 16), kr1 = i16 * 256 + (((4 + hh) ^ i16) * 16), kr2 = i16 * 256 + (((8 + hh) ^ i16) * 16), kr3 = i16 * 256 + (((12 + hh) ^ i16) * 16);
  const unsigned vr = hh * 1024 + i16 * 16;
  auto fetch = [&](int it, int kind, u32x4 (&T)[8]) {
    const unsigned seq1 = (unsigned)(4 + 8 * it + w + 1);
    lds_wait_ge<1>(lds, L_FULL + 4 * slot, seq1);
    const LAS unsigned char* sp = lds + L_RING + slot * SLOT;
    if (kind != 2) { T[0] = *(const LAS u32x4*)(sp + kr0); T[1] = *(const LAS u32x4*)(sp + kr1); T[2] = *(const LAS u32x4*)(sp + kr2); T[3] = *(const LAS u32x4*)(sp + kr3); }
    else {
#pragma unroll
      for (int r = 0; r < 4; ++r) T[r] = *(const LAS u32x4*)(sp + vr + r * 256);
    }
    if (kind == 1) { T[4] = *(const LAS u32x4*)(sp + 4096 + kr0); T[5] = *(const LAS u32x4*)(sp + 4096 + kr1); T[6] = *(const LAS u32x4*)(sp + 4096 + kr2); T[7] = *(const LAS u32x4*)(sp + 4096 + kr3); }
    else {
#pragma unroll
      for (int r = 0; r < 4; ++r) T[4 + r] = *(const LAS u32x4*)(sp + 4096 + vr + r * 256);
    }
    LDS_WAIT();
    lds_st(lds, L_FREE + 4 * slot, seq1);
    asm volatile("" : "+v"(T[0]), "+v"(T[1]), "+v"(T[2]), "+v"(T[3]), "+v"(T[4]), "+v"(T[5]), "+v"(T[6]), "+v"(T[7]) :: "memory");
    slot += 8; if (slot >= NS) slot -= NS;
  };
  u32x4 X0[8], X1[8], X2[8];
  fetch(0, 0, X0);
  if (PRE >= 2) fetch(1, 1, X1);
  if (PRE >= 3) fetch(2, 1, X2);
  {
    const unsigned off = WS_QG + ((h * 32 + 2 * (tid >> 5)) * 32 + (tid & 31)) * 16;
    u32x4 g0 = {0u, 0u, 0u, 0u}, g1 = {0u, 0u, 0u, 0u};
    bool ok = false;
    for (unsigned spin = 0; spin < SPIN_CAP; ++spin) {
      MEMFENCE();
      if (!ok) {
        g0 = __builtin_amdgcn_raw_buffer_load_b128(rs, off, 0, AUX_SC1);
        g1 = __builtin_amdgcn_raw_buffer_load_b128(rs, off + 512, 0, AUX_SC1);
        ok = g0[0] == tagA && g0[3] == tagB && g1[0] == tagA && g1[3] == tagB;
      }
      if (__all(ok)) break;
      if (QSLEEP) __builtin_amdgcn_s_sleep(QSLEEP);
    }
    *(LAS unsigned*)(lds + L_Q + tid * 4) = cvtpk(u2f(g0[1]) + u2f(g1[1]), u2f(g0[2]) + u2f(g1[2]));
  }
  lds_arrive(lds, L_CNT + C_QIN, lane);
  lds_wait_ge<0>(lds, L_CNT + C_QIN, NC);
  u32x4 qA[4], qB[4];
#pragma unroll
  for (int s = 0; s < 4; ++s) {
    qA[s] = *(const LAS u32x4*)(lds + L_Q + ((i16 >> 2) * 128 + 32 * s + 8 * hh) * 2);
    qB[s] = *(const LAS u32x4*)(lds + L_Q + 1024 + ((i16 >> 2) * 128 + 32 * s + 8 * hh) * 2);
  }
  STAMP(2);

  Sel sel;
  sel.l0 = jj == 0 ? 0x0c0c0100u : jj == 1 ? 0x01000c0cu : 0x0c0c0c0cu;  sel.h0 = jj == 0 ? 0x0c0c0302u : jj == 1 ? 0x03020c0cu : 0x0c0c0c0cu;
  sel.l1 = jj == 2 ? 0x0c0c0100u : jj == 3 ? 0x01000c0cu : 0x0c0c0c0cu;  sel.h1 = jj == 2 ? 0x0c0c0302u : jj == 3 ? 0x03020c0cu : 0x0c0c0c0cu;

  f32x4 b0 = {0.f, 0.f, 0.f, 0.f}, b1 = {0.f, 0.f, 0.f, 0.f};
  {
    const u32x2 t = tile_scores(X0, qB);
    const float m2 = swap32_max(swap16_max(max4(t))) * LOG2E;
    float l = 0.f;
    const u32x2 p = to_probs(t, m2, l);
    l = swap32_sum(swap16_sum(l));
    tile_pv(b0, b1, X0 + 4, p, sel);
    if (hh == 0 && jj == 0) *(LAS f32x2*)(lds + L_TB + ((i16 >> 2) * 8 + w) * 8) = f32x2{m2, l};
  }

  u32x2 P[16];
  float mx = -INFINITY;
#pragma unroll
  for (int i = 0; i < 8; ++i) {
    if (i == 0) { if (PRE < 2) fetch(1, 1, X1); }
    else if (i == 1) { if (PRE < 3) fetch(2, 1, X2); }
    else fetch(i + 1, 1, X1);
    const u32x4* T = i == 1 ? X2 : X1;
    P[2 * i] = tile_scores(T, qA);
    P[2 * i + 1] = tile_scores(T + 4, qA);
    mx = fmaxf(mx, fmaxf(max4(P[2 * i]), max4(P[2 * i + 1])));
  }
  STAMP(13);
  mx = swap32_max(swap16_max(mx));
  if (hh == 0 && jj == 0) *(LAS float*)(lds + L_T1 + ((i16 >> 2) * 8 + w) * 4) = mx;
  lds_arrive(lds, L_CNT + C_MAX, lane);
  lds_wait_ge<0>(lds, L_CNT + C_MAX, NC);
  {
    const f32x4 m0 = *(const LAS f32x4*)(lds + L_T1 + (i16 >> 2) * 32), m1 = *(const LAS f32x4*)(lds + L_T1 + (i16 >> 2) * 32 + 16);
    const float m2 = fmaxf(fmaxf(fmaxf(m0[0], m0[1]), fmaxf(m0[2], m0[3])), fmaxf(fmaxf(m1[0], m1[1]), fmaxf(m1[2], m1[3]))) * LOG2E;
    float l = 0.f;
#pragma unroll
    for (int t = 0; t < 16; ++t) P[t] = to_probs(P[t], m2, l);
    l = swap32_sum(swap16_sum(l));
    if (hh == 0 && jj == 0) *(LAS float*)(lds + L_T2 + ((i16 >> 2) * 8 + w) * 4) = l;
  }
  lds_arrive(lds, L_CNT + C_SUM, lane);
  lds_wait_ge<0>(lds, L_CNT + C_SUM, NC);
  const int mbr = w >> 2, mg = w & 3;
  {
    const int ww = lane & 7;
    float M, L;
    if (mbr == 0) {
      M = oct_max(*(const LAS float*)(lds + L_T1 + (mg * 8 + ww) * 4)) * LOG2E;
      L = oct_sum(*(const LAS float*)(lds + L_T2 + (mg * 8 + ww) * 4));
    } else {
      const f32x2 ml = *(const LAS f32x2*)(lds + L_TB + (mg * 8 + ww) * 8);
      M = oct_max(ml[0]);
      L = oct_sum(ml[1] * __builtin_amdgcn_exp2f(ml[0] - M));
    }
    if (lane == 0) __builtin_amdgcn_raw_buffer_store_b128(u32x4{tagA, f2u(M), f2u(L), tagB}, rs, WS_ML + (((h * 2 + mbr) * 4 + mg) * 32 + j) * 16, 0, AUX_SC1);
  }
  STAMP(14);

  f32x4 a0 = {0.f, 0.f, 0.f, 0.f}, a1 = {0.f, 0.f, 0.f, 0.f};
  float fA = 0.f, fB = 0.f;
#pragma unroll
  for (int i = 0; i < 8; ++i) {
    if (i == 4) {
      const unsigned off = WS_ML + (((h * 2 + mbr) * 4 + mg) * 32 + (lane & 31)) * 16;
      u32x4 mv = {0u, 0u, 0u, 0u};
      bool ok = false;
      for (unsigned spin = 0; spin < SPIN_CAP; ++spin) {
        MEMFENCE();
        if (!ok) { mv = __builtin_amdgcn_raw_buffer_load_b128(rs, off, 0, AUX_SC1); ok = mv[0] == tagA && mv[3] == tagB; }
        if (__all(ok)) break;
        __builtin_amdgcn_s_sleep(16);
      }
      const float M = swap16_max(row_max(u2f(mv[1])));
      const float L = swap16_sum(row_sum(u2f(mv[2]) * __builtin_amdgcn_exp2f(u2f(mv[1]) - M)));
      if (lane == 0) *(LAS f32x2*)(lds + L_FF + w * 8) = f32x2{M, 1.0f / L};
      lds_arrive(lds, L_CNT + C_FF, lane);
    }
    if (i == 7) {
      lds_wait_ge<0>(lds, L_CNT + C_FF, NC);
      const f32x4 m0 = *(const LAS f32x4*)(lds + L_T1 + hh * 32), m1 = *(const LAS f32x4*)(lds + L_T1 + hh * 32 + 16);
      const float m2 = fmaxf(fmaxf(fmaxf(m0[0], m0[1]), fmaxf(m0[2], m0[3])), fmaxf(fmaxf(m1[0], m1[1]), fmaxf(m1[2], m1[3]))) * LOG2E;
      const f32x2 FA = *(const LAS f32x2*)(lds + L_FF + hh * 8), FB = *(const LAS f32x2*)(lds + L_FF + (4 + hh) * 8);
      const float mB = *(const LAS float*)(lds + L_TB + (hh * 8 + w) * 8);
      fA = __builtin_amdgcn_exp2f(m2 - FA[0]) * FA[1];
      fB = __builtin_amdgcn_exp2f(mB - FB[0]) * FB[1];
      const int s = lane < MRG_SLOTS ? lane : 0;
      const unsigned need = (unsigned)(s + NS * ((139 - s) / NS) + 1);
      for (unsigned spin = 0; spin < LSPIN_CAP; ++spin) { MEMFENCE(); if (__all(*(volatile LAS unsigned*)(lds + L_FREE + 4 * s) >= need)) break; }
      MEMFENCE();
      LAS float* rB = (LAS float*)(lds + L_MRG) + ((8 + w) * 4 + hh) * MREC + 8 * i16;
      *(LAS f32x4*)(rB) = b0 * fB; *(LAS f32x4*)(rB + 4) = b1 * fB;
    }
    fetch(9 + i, 2, X1);
    tile_pv(a0, a1, X1, P[2 * i], sel);
    tile_pv(a0, a1, X1 + 4, P[2 * i + 1], sel);
  }
  STAMP(3);
  if (w == 7) WSTAMP(11);

  const unsigned tA = tagA & 0xffffffu, tB = tagB & 0xffffffu;
  {
    LAS float* rA = (LAS float*)(lds + L_MRG) + (w * 4 + hh) * MREC + 8 * i16;
    *(LAS f32x4*)(rA) = a0 * fA; *(LAS f32x4*)(rA + 4) = a1 * fA;
  }
  LDS_BARRIER();
  if (tid < 256) {
    const int br = tid >> 7, g = (tid >> 5) & 3, dq = tid & 31;
    const LAS float* mrg = (const LAS float*)(lds + L_MRG) + (br * 8 * 4 + g) * MREC + 4 * dq;
    f32x4 o4[8];
#pragma unroll
    for (int ww = 0; ww < 8; ++ww) o4[ww] = *(const LAS f32x4*)(mrg + ww * 4 * MREC);
    const f32x4 o = ((o4[0] + o4[1]) + (o4[2] + o4[3])) + ((o4[4] + o4[5]) + (o4[6] + o4[7]));
    const int jc = br * 16 + g * 4 + (dq >> 3), k = dq & 7;
    __builtin_amdgcn_raw_buffer_store_b128(pack4(o, tA, tB), rs, WS_I1 + (((h * 32 + jc) * 32 + j) * 8 + k) * 16, 0, AUX_SC1);
  }
  STAMP(4);

  const unsigned offA = WS_I1 + ((h * 32 + j) * 32 * 8 + (w & 3) * 64 + lane) * 16;
  u32x4 av = {0u, 0u, 0u, 0u};
  if (w >= 4) av = __builtin_amdgcn_raw_buffer_load_b128(rs, offA, 0, AUX_SC1);
  MEMFENCE();
  float W[32];
  lds_wait_ge<0>(lds, L_FULL + 4 * (143 % NS), 144u);
  {
    const LAS unsigned short* wp = (const LAS unsigned short*)(lds + L_RING + (140 % NS) * SLOT) + 64 * w + lane;
#pragma unroll
    for (int r = 0; r < 32; ++r) W[r] = u2f((unsigned)wp[r * 512] << 16);
#pragma unroll
    for (int r = 0; r < 32; r += 8) asm volatile("" : "+v"(W[r]), "+v"(W[r + 1]), "+v"(W[r + 2]), "+v"(W[r + 3]), "+v"(W[r + 4]), "+v"(W[r + 5]), "+v"(W[r + 6]), "+v"(W[r + 7]));
  }
  if (w >= 4) {
    bool ok = tags4(av, tA, tB);
    for (unsigned spin = 0; spin < SPIN_CAP && !__all(ok); ++spin) {
      MEMFENCE();
      if (!ok) { av = __builtin_amdgcn_raw_buffer_load_b128(rs, offA, 0, AUX_SC1); ok = tags4(av, tA, tB); }
      if (PSLEEP) __builtin_amdgcn_s_sleep(PSLEEP);
    }
    if (w == 4) WSTAMP(5);
    const f32x4 o = red8_16_32(unpack4(av));
    if (lane < 8) {
#pragma unroll
      for (int e = 0; e < 4; ++e) *(LAS float*)(lds + L_H1 + ((4 * lane + e) * 4 + (w & 3)) * 4) = o[e];
    }
  }
  LDS_BARRIER();
  {
    const f32x4 ra = *(const LAS f32x4*)(lds + L_H1 + i16 * 16), rb = *(const LAS f32x4*)(lds + L_H1 + (16 + i16) * 16);
    const unsigned ob = cvtpk((ra[0] + ra[1]) + (ra[2] + ra[3]), (rb[0] + rb[1]) + (rb[2] + rb[3]));
    const float oa = lo16(ob), oc = hi16(ob);
    float y0 = 0.f, y1 = 0.f, y2 = 0.f, y3 = 0.f;
#define FM(acc, o, wv, n) "v_fmac_f32_dpp " acc ", " o ", " wv " row_newbcast:" #n " row_mask:0xf bank_mask:0xf\n\t"
#define ROWS(n0, n1, n2, n3) asm volatile("s_nop 1\n\t" \
      FM("%0", "%4", "%6", n0) FM("%1", "%5", "%7", n0) FM("%2", "%4", "%8", n1) FM("%3", "%5", "%9", n1) \
      FM("%0", "%4", "%10", n2) FM("%1", "%5", "%11", n2) FM("%2", "%4", "%12", n3) FM("%3", "%5", "%13", n3) \
      : "+v"(y0), "+v"(y1), "+v"(y2), "+v"(y3) \
      : "v"(oa), "v"(oc), "v"(W[n0]), "v"(W[16 + n0]), "v"(W[n1]), "v"(W[16 + n1]), "v"(W[n2]), "v"(W[16 + n2]), "v"(W[n3]), "v"(W[16 + n3]));
    ROWS(0, 1, 2, 3) ROWS(4, 5, 6, 7) ROWS(8, 9, 10, 11) ROWS(12, 13, 14, 15)
#undef ROWS
#undef FM
    const float y = (y0 + y1) + (y2 + y3);
    const f32x4 y4 = {y, dpp<0x55>(y), dpp<0xAA>(y), dpp<0xFF>(y)};
    const int c = (j >> 4) * 16 + 2 * w + (lane >> 5), k = (lane & 31) >> 2, pidx = h * 16 + (j & 15);
    if (jj == 0) __builtin_amdgcn_raw_buffer_store_b128(pack4(y4, tA, tB), rs, WS_I2 + ((c * 128 + pidx) * 8 + k) * 16, 0, AUX_SC1);
  }
  STAMP(6);

  if (j < 4) {
    const int c = h * 4 + j;
    const unsigned off = WS_I2 + (c * 128 + 16 * w) * 128 + lane * 16;
    u32x4 v[2] = {{0u, 0u, 0u, 0u}, {0u, 0u, 0u, 0u}};
    bool ok = false;
    for (unsigned spin = 0; spin < SPIN_CAP; ++spin) {
      MEMFENCE();
      if (!ok) {
#pragma unroll
        for (int i = 0; i < 2; ++i) v[i] = __builtin_amdgcn_raw_buffer_load_b128(rs, off + i * 1024, 0, AUX_SC1);
        ok = tags4(v[0], tA, tB) && tags4(v[1], tA, tB);
      }
      if (__all(ok)) break;
      if (PSLEEP) __builtin_amdgcn_s_sleep(PSLEEP);
    }
    STAMP(7);
    const f32x4 s4 = red8_16_32(unpack4(v[0]) + unpack4(v[1]));
    if (lane < 8) {
#pragma unroll
      for (int e = 0; e < 4; ++e) *(LAS float*)(lds + L_F + ((4 * lane + e) * 8 + w) * 4) = s4[e];
    }
    LDS_BARRIER();
    if (tid < 32) {
      const f32x4 p0 = *(const LAS f32x4*)(lds + L_F + tid * 32), p1 = *(const LAS f32x4*)(lds + L_F + tid * 32 + 16);
      const float s = ((p0[0] + p0[1]) + (p0[2] + p0[3])) + ((p1[0] + p1[1]) + (p1[2] + p1[3]));
      const unsigned pk = cvtpk(s, dpp<XOR1>(s));
      if ((tid & 1) == 0) *(GAS unsigned*)((GAS unsigned char*)a.out + (c * 32 + tid) * 2) = pk;
      if (c == 0 && tid == 0) __builtin_amdgcn_raw_buffer_store_b64(u32x2{tagA, tagB}, rs, WS_GEN, 0, AUX_SC1);
    }
  }
  DUMP_STAMPS();
}

extern "C" void kernel_launch(void* const* d_in, const int* in_sizes, int n_in, void* d_out, int out_size, void* d_ws, size_t ws_size, hipStream_t stream) {
  Args a;
  a.xA = (const unsigned char*)d_in[0]; a.kA = (const unsigned char*)d_in[1]; a.vA = (const unsigned char*)d_in[2]; a.wqA = (const unsigned char*)d_in[3]; a.woA = (const unsigned char*)d_in[4];
  a.xB = (const unsigned char*)d_in[5]; a.kB = (const unsigned char*)d_in[6]; a.vB = (const unsigned char*)d_in[7]; a.wqB = (const unsigned char*)d_in[8]; a.woB = (const unsigned char*)d_in[9];
  a.out = (unsigned short*)d_out;
  a.ws = (unsigned char*)d_ws;
  hipLaunchKernelGGL(fused_decode, dim3(NWG), dim3(NTHR), 0, stream, a);
}
```

```cpp
#include <hip/hip_runtime.h>
#include <hip/hip_bf16.h>
#include <stdint.h>

#define GAS __attribute__((address_space(1)))
#define LAS __attribute__((address_space(3)))
typedef unsigned u32x4 __attribute__((ext_vector_type(4)));
typedef unsigned u32x2 __attribute__((ext_vector_type(2)));
typedef float f32x4 __attribute__((ext_vector_type(4)));
typedef float f32x2 __attribute__((ext_vector_type(2)));
typedef __bf16 bf16x8 __attribute__((ext_vector_type(8)));
typedef __bf16 bf16x2 __attribute__((ext_vector_type(2)));
typedef short s16x4 __attribute__((ext_vector_type(4)));

#define NS 18
#define WIN_LD 22
#define PRE 3
#define QSLEEP 16
#define PSLEEP 0
#define HOLD 0
#define POLB 6
#define BENCH 0
#define STAMPS 0

constexpr int SK = 65536, WIN = 4096;
constexpr int NWG = 256, NC = 8, NTHR = (NC + 1) * 64;
constexpr int SLOT = 8192, NSEQ = 4 + 8 + 64 + 64 + 4;
constexpr int LAG = (WIN_LD + 7) / 8;
constexpr float LOG2E = 1.4426950408889634f;
constexpr float SCALE = 0.08838834764831845f;
constexpr unsigned SPIN_CAP = 1u << 20, LSPIN_CAP = 1u << 24;

constexpr unsigned WS_GEN = 0;
constexpr unsigned WS_QG = 4096;
constexpr unsigned WS_ML = WS_QG + 8 * 32 * 32 * 16;
constexpr unsigned WS_I1 = WS_ML + 256 * 128;
constexpr unsigned WS_I2 = WS_I1 + 8 * 32 * 32 * 128;
constexpr unsigned WS_END = WS_I2 + 32 * 128 * 128;
constexpr unsigned WS_STAMP = WS_END;

constexpr int L_RING = 0;
constexpr int L_FULL = NS * SLOT;
constexpr int L_FREE = L_FULL + 128;
constexpr int L_CNT = L_FREE + 128;
constexpr int L_RED = L_CNT + 64;
constexpr int L_Q = L_RED + 2048;
constexpr int L_T1 = L_Q + 2048;
constexpr int L_T2 = L_T1 + 128;
constexpr int L_TB = L_T2 + 128;
constexpr int L_FF = L_TB + 256;
constexpr int L_H1 = L_FF + 64;
constexpr int L_F = L_H1 + 512;
constexpr int L_END = L_F + 1024 + 128;
constexpr int C_QRED = 0, C_QIN = 4, C_MAX = 8, C_SUM = 12, C_FF = 16, C_MRG = 20;
constexpr int MREC = 132;
constexpr int L_MRG = 0;
constexpr int MRG_SLOTS = 5;
static_assert(L_END <= 160 * 1024, "LDS");
static_assert(2 * 8 * 4 * MREC * 4 <= MRG_SLOTS * SLOT, "merge area");
static_assert((140 % NS) >= MRG_SLOTS && (143 % NS) >= MRG_SLOTS && (140 % NS) <= (143 % NS), "wo slots contiguous and clear of the merge area");
static_assert((132 % NS) >= MRG_SLOTS && (139 % NS) >= MRG_SLOTS && (132 % NS) <= (139 % NS), "last round clear of the merge area");
static_assert(WIN_LD >= 9 && WIN_LD <= 48, "window");

__device__ __forceinline__ unsigned cvtpk(float lo, float hi) { f32x2 f = {lo, hi}; return __builtin_bit_cast(unsigned, __builtin_convertvector(f, bf16x2)); }
__device__ __forceinline__ float lo16(unsigned u) { return __builtin_bit_cast(float, u << 16); }
__device__ __forceinline__ float hi16(unsigned u) { return __builtin_bit_cast(float, u & 0xffff0000u); }
__device__ __forceinline__ float u2f(unsigned u) { return __builtin_bit_cast(float, u); }
__device__ __forceinline__ unsigned f2u(float f) { return __builtin_bit_cast(unsigned, f); }
template <int CTRL> __device__ __forceinline__ float dpp(float x) { return u2f(__builtin_amdgcn_mov_dpp(f2u(x), CTRL, 0xf, 0xf, true)); }
constexpr int XOR1 = 0xB1, XOR2 = 0x4E, HMIR = 0x141;
constexpr int ROR1 = 0x121, ROR2 = 0x122, ROR4 = 0x124, ROR8 = 0x128;
__device__ __forceinline__ float swap16_max(float x) { auto s = __builtin_amdgcn_permlane16_swap(f2u(x), f2u(x), false, false); return fmaxf(u2f(s[0]), u2f(s[1])); }
__device__ __forceinline__ float swap32_max(float x) { auto s = __builtin_amdgcn_permlane32_swap(f2u(x), f2u(x), false, false); return fmaxf(u2f(s[0]), u2f(s[1])); }
__device__ __forceinline__ float swap16_sum(float x) { auto s = __builtin_amdgcn_permlane16_swap(f2u(x), f2u(x), false, false); return u2f(s[0]) + u2f(s[1]); }
__device__ __forceinline__ float swap32_sum(float x) { auto s = __builtin_amdgcn_permlane32_swap(f2u(x), f2u(x), false, false); return u2f(s[0]) + u2f(s[1]); }
__device__ __forceinline__ float row_sum(float x) { x += dpp<ROR8>(x); x += dpp<ROR4>(x); x += dpp<ROR2>(x); x += dpp<ROR1>(x); return x; }
__device__ __forceinline__ float row_max(float x) { x = fmaxf(x, dpp<ROR8>(x)); x = fmaxf(x, dpp<ROR4>(x)); x = fmaxf(x, dpp<ROR2>(x)); x = fmaxf(x, dpp<ROR1>(x)); return x; }
template <int N> __device__ __forceinline__ float bcast(float x) { return dpp<0x150 + N>(x); }
__device__ __forceinline__ float oct_sum(float x) { x += dpp<XOR1>(x); x += dpp<XOR2>(x); x += dpp<HMIR>(x); return x; }
__device__ __forceinline__ float oct_max(float x) { x = fmaxf(x, dpp<XOR1>(x)); x = fmaxf(x, dpp<XOR2>(x)); x = fmaxf(x, dpp<HMIR>(x)); return x; }
#define LDS_BARRIER() asm volatile("s_waitcnt lgkmcnt(0)\n\ts_barrier" ::: "memory")
#define LDS_WAIT() asm volatile("s_waitcnt lgkmcnt(0)" ::: "memory")
#define MEMFENCE() asm volatile("" ::: "memory")
constexpr int AUX_SC1 = 16;

__device__ __forceinline__ u32x4 pack4(f32x4 v, unsigned tA, unsigned tB) {
  const unsigned t0 = f2u(v[0]) + 0x800u, t1 = f2u(v[1]) + 0x800u, t2 = f2u(v[2]) + 0x800u, t3 = f2u(v[3]) + 0x800u;
  return u32x4{tA | ((t0 << 12) & 0xff000000u), (t0 >> 20) | (t1 & 0xfffff000u), (t2 >> 12) | ((t3 << 8) & 0xfff00000u), (t3 >> 24) | (tB << 8)};
}
__device__ __forceinline__ f32x4 unpack4(u32x4 d) {
  return f32x4{u2f((d[1] << 20) | ((d[0] >> 12) & 0xff000u)), u2f(d[1] & 0xfffff000u), u2f(d[2] << 12), u2f(((d[2] >> 8) & 0xfff000u) | (d[3] << 24))};
}
__device__ __forceinline__ bool tags4(u32x4 d, unsigned tA, unsigned tB) { return (d[0] & 0xffffffu) == tA && (d[3] >> 8) == tB; }
__device__ __forceinline__ f32x4 red8_16_32(f32x4 v) {
#pragma unroll
  for (int e = 0; e < 4; ++e) { float x = v[e]; x += dpp<ROR8>(x); v[e] = swap32_sum(swap16_sum(x)); }
  return v;
}

struct Args {
  const unsigned char *xA, *kA, *vA, *wqA, *woA, *xB, *kB, *vB, *wqB, *woB;
  unsigned short* out;
  unsigned char* ws;
};

__device__ __forceinline__ unsigned lds_ld(LAS unsigned char* L, int off) { return __builtin_amdgcn_readfirstlane(*(volatile LAS unsigned*)(L + off)); }
__device__ __forceinline__ void lds_st(LAS unsigned char* L, int off, unsigned v) { *(volatile LAS unsigned*)(L + off) = v; }
template <int SLEEP> __device__ __forceinline__ void lds_wait_ge(LAS unsigned char* L, int off, unsigned need) {
  for (unsigned spin = 0; lds_ld(L, off) < need && spin < LSPIN_CAP; ++spin) { if (SLEEP) __builtin_amdgcn_s_sleep(SLEEP); }
  MEMFENCE();
}
__device__ __forceinline__ void lds_arrive(LAS unsigned char* L, int off, int lane) {
  LDS_WAIT();
  if (lane == 0) __hip_atomic_fetch_add((LAS unsigned*)(L + off), 1u, __ATOMIC_RELAXED, __HIP_MEMORY_SCOPE_WORKGROUP);
}

template <int POL> __device__ __forceinline__ void dma1p(unsigned dst, unsigned voff, const unsigned char* base) {
  unsigned keep;
  if constexpr (POL == 0)
  asm volatile("s_mov_b32 %0, m0\n\ts_mov_b32 m0, %1\n\ts_nop 0\n\tglobal_load_lds_dwordx4 %2, %3 nt\n\ts_mov_b32 m0, %0\n\ts_waitcnt vmcnt(%4)"
               : "=&s"(keep) : "s"(dst), "v"(voff), "s"(base), "i"(WIN_LD) : "memory");
  else
  asm volatile("s_mov_b32 %0, m0\n\ts_mov_b32 m0, %1\n\ts_nop 0\n\tglobal_load_lds_dwordx4 %2, %3\n\ts_mov_b32 m0, %0\n\ts_waitcnt vmcnt(%4)"
               : "=&s"(keep) : "s"(dst), "v"(voff), "s"(base), "i"(WIN_LD) : "memory");
}
template <int POL> __device__ __forceinline__ void dma4p(unsigned dst, unsigned v0, unsigned v1, unsigned v2, unsigned v3, const unsigned char* base) {
  unsigned keep;
  if constexpr (POL != 0)
  asm volatile("s_mov_b32 %0, m0\n\ts_mov_b32 m0, %1\n\ts_nop 0\n\t"
               "global_load_lds_dwordx4 %2, %6\n\ts_waitcnt vmcnt(%7)\n\t"
               "global_load_lds_dwordx4 %3, %6 offset:1024\n\ts_waitcnt vmcnt(%7)\n\t"
               "global_load_lds_dwordx4 %4, %6 offset:2048\n\ts_waitcnt vmcnt(%7)\n\t"
               "global_load_lds_dwordx4 %5, %6 offset:3072\n\ts_waitcnt vmcnt(%7)\n\t"
               "s_mov_b32 m0, %0"
               : "=&s"(keep) : "s"(dst), "v"(v0), "v"(v1), "v"(v2), "v"(v3), "s"(base), "i"(WIN_LD) : "memory");
  else
  asm volatile("s_mov_b32 %0, m0\n\ts_mov_b32 m0, %1\n\ts_nop 0\n\t"
               "global_load_lds_dwordx4 %2, %6 nt\n\ts_waitcnt vmcnt(%7)\n\t"
               "global_load_lds_dwordx4 %3, %6 offset:1024 nt\n\ts_waitcnt vmcnt(%7)\n\t"
               "global_load_lds_dwordx4 %4, %6 offset:2048 nt\n\ts_waitcnt vmcnt(%7)\n\t"
               "global_load_lds_dwordx4 %5, %6 offset:3072 nt\n\ts_waitcnt vmcnt(%7)\n\t"
               "s_mov_b32 m0, %0"
               : "=&s"(keep) : "s"(dst), "v"(v0), "v"(v1), "v"(v2), "v"(v3), "s"(base), "i"(WIN_LD) : "memory");
}
__device__ __forceinline__ void dma4(unsigned dst, unsigned v0, unsigned v1, unsigned v2, unsigned v3, const unsigned char* base) { dma4p<0>(dst, v0, v1, v2, v3, base); }
template <int N> __device__ __forceinline__ void vm_wait() { asm volatile("s_waitcnt vmcnt(%0)" :: "i"(N) : "memory"); }

__device__ __forceinline__ u32x2 tile_scores(const u32x4* K, const u32x4 (&q)[4]) {
  f32x4 s = {0.f, 0.f, 0.f, 0.f};
#pragma unroll
  for (int i = 0; i < 4; ++i) s = __builtin_amdgcn_mfma_f32_16x16x32_bf16(__builtin_bit_cast(bf16x8, K[i]), __builtin_bit_cast(bf16x8, q[i]), s, 0, 0, 0);
  const unsigned r01 = cvtpk(s[0], s[1]), r23 = cvtpk(s[2], s[3]);
  return u32x2{cvtpk(lo16(r01) * SCALE, hi16(r01) * SCALE), cvtpk(lo16(r23) * SCALE, hi16(r23) * SCALE)};
}
__device__ __forceinline__ float max4(u32x2 t) { return fmaxf(fmaxf(lo16(t[0]), hi16(t[0])), fmaxf(lo16(t[1]), hi16(t[1]))); }
__device__ __forceinline__ u32x2 to_probs(u32x2 t, float m2, float& l) {
  const float p0 = __builtin_amdgcn_exp2f(fmaf(lo16(t[0]), LOG2E, -m2)), p1 = __builtin_amdgcn_exp2f(fmaf(hi16(t[0]), LOG2E, -m2));
  const float p2 = __builtin_amdgcn_exp2f(fmaf(lo16(t[1]), LOG2E, -m2)), p3 = __builtin_amdgcn_exp2f(fmaf(hi16(t[1]), LOG2E, -m2));
  l += (p0 + p1) + (p2 + p3);
  return u32x2{cvtpk(p0, p1), cvtpk(p2, p3)};
}
struct Sel { unsigned l0, l1, h0, h1; };
__device__ __forceinline__ void tile_pv(f32x4& a0, f32x4& a1, const u32x4* V, u32x2 p, const Sel& s) {
#pragma unroll
  for (int r = 0; r < 4; ++r) {
    const unsigned pk = p[r >> 1];
    const u32x2 a = {__builtin_amdgcn_perm(0u, pk, (r & 1) ? s.h0 : s.l0), __builtin_amdgcn_perm(0u, pk, (r & 1) ? s.h1 : s.l1)};
    const u32x2 v0 = {V[r][0], V[r][1]}, v1 = {V[r][2], V[r][3]};
    a0 = __builtin_amdgcn_mfma_f32_16x16x16bf16_1k(__builtin_bit_cast(s16x4, a), __builtin_bit_cast(s16x4, v0), a0, 0, 0, 0);
    a1 = __builtin_amdgcn_mfma_f32_16x16x16bf16_1k(__builtin_bit_cast(s16x4, a), __builtin_bit_cast(s16x4, v1), a1, 0, 0, 0);
  }
}

#define STAMP(i) do {} while (0)
#define WSTAMP(i) do {} while (0)
#define DUMP_STAMPS() do {} while (0)

__global__ void __launch_bounds__(NTHR, 3) fused_decode(Args a) {
  __shared__ __attribute__((aligned(16))) unsigned char lds_raw[L_END];
  LAS unsigned char* lds = (LAS unsigned char*)lds_raw;
  const int b = blockIdx.x, h = b & 7, j = b >> 3;
  const int tid = threadIdx.x, lane = tid & 63, w = __builtin_amdgcn_readfirstlane(tid >> 6);
  const int hh = lane >> 4, i16 = lane & 15, jj = lane & 3;
  const int qcb = j >> 1, qrh = j & 1, qbr = qcb >> 3;

  if (w == NC) {
    const unsigned ring = (unsigned)(size_t)(lds + L_RING);
    int pub = 0, pslot = 0, slot = 0;
    auto publish1 = [&]() { lds_st(lds, L_FULL + 4 * pslot, (unsigned)(pub + 1)); ++pub; pslot = pslot + 1 == NS ? 0 : pslot + 1; };
    auto pre = [&](int q) {
      if (q >= NS) {
        const unsigned need = (unsigned)(q - NS + 1);
        if (lds_ld(lds, L_FREE + 4 * slot) < need) {
          vm_wait<0>();
          while (pub < q) publish1();
          lds_wait_ge<1>(lds, L_FREE + 4 * slot, need);
        }
      }
    };
    auto post = [&](int q) {
      if (q - pub >= LAG) publish1();
      slot = slot + 1 == NS ? 0 : slot + 1;
    };
    {
      const unsigned char* wq = (qbr ? a.wqB : a.wqA) + (size_t)(h * 512 + 64 * (qcb & 7)) * 2 + (size_t)qrh * 256 * 8192;
      const unsigned vo = (lane >> 3) * 8192 + (lane & 7) * 16;
#pragma unroll
      for (int q = 0; q < 4; ++q) {
#pragma unroll
        for (int m = 0; m < 8; ++m) { if ((POLB & 1) && qbr) dma1p<1>(ring + q * SLOT + m * 1024, vo, wq + (size_t)(q * 64 + m * 8) * 8192); else dma1p<0>(ring + q * SLOT + m * 1024, vo, wq + (size_t)(q * 64 + m * 8) * 8192); }
        if (q == 0) LDS_BARRIER();
        post(q);
      }
    }
    const unsigned k0 = hh * 256 + ((i16 ^ (0 + hh)) * 16), k1 = hh * 256 + ((i16 ^ (4 + hh)) * 16), k2 = hh * 256 + ((i16 ^ (8 + hh)) * 16), k3 = hh * 256 + ((i16 ^ (12 + hh)) * 16);
    const unsigned vl = lane * 16;
    {
      const unsigned char* kp = a.kB + ((size_t)h * SK + (SK - WIN) + j * 128) * 256;
      const unsigned char* vp = a.vB + ((size_t)h * SK + (SK - WIN) + j * 128) * 256;
      for (int q = 4; q < 12; ++q) {
        dma4p<(POLB & 2) ? 1 : 0>(ring + slot * SLOT, k0, k1, k2, k3, kp);
        dma4p<(POLB & 4) ? 1 : 0>(ring + slot * SLOT + 4096, vl, vl, vl, vl, vp);
        kp += 4096; vp += 4096;
        post(q);
      }
      kp = a.kA + ((size_t)h * SK + j * 2048) * 256;
      for (int q = 12; q < 76; ++q) {
        pre(q);
        dma4(ring + slot * SLOT, k0, k1, k2, k3, kp);
        dma4(ring + slot * SLOT + 4096, k0, k1, k2, k3, kp + 4096);
        kp += 8192;
        post(q);
      }
      WSTAMP(12);
      vp = a.vA + ((size_t)h * SK + j * 2048) * 256;
      for (int q = 76; q < 140; ++q) {
        pre(q);
        dma4(ring + slot * SLOT, vl, vl, vl, vl, vp);
        dma4(ring + slot * SLOT + 4096, vl, vl, vl, vl, vp + 4096);
        vp += 8192;
        post(q);
      }
      WSTAMP(9);
      vp = (j >> 4 ? a.woB : a.woA) + (size_t)(h * 512 + 32 * (j & 15)) * 1024;
      for (int q = 140; q < 144; ++q) {
        pre(q);
        if ((POLB & 8) && (j >> 4)) { dma4p<1>(ring + slot * SLOT, vl, vl, vl, vl, vp); dma4p<1>(ring + slot * SLOT + 4096, vl, vl, vl, vl, vp + 4096); }
        else { dma4(ring + slot * SLOT, vl, vl, vl, vl, vp); dma4(ring + slot * SLOT + 4096, vl, vl, vl, vl, vp + 4096); }
        vp += 8192;
        post(q);
      }
    }
    __builtin_amdgcn_s_barrier();
    if (NSEQ - pub > 6) { vm_wait<48>(); publish1(); }
    if (NSEQ - pub > 5) { vm_wait<40>(); publish1(); }
    if (NSEQ - pub > 4) { vm_wait<32>(); publish1(); }
    if (NSEQ - pub > 3) { vm_wait<24>(); publish1(); }
    if (NSEQ - pub > 2) { vm_wait<16>(); publish1(); }
    if (NSEQ - pub > 1) { vm_wait<8>(); publish1(); }
    vm_wait<0>();
    while (pub < NSEQ) publish1();
    WSTAMP(10);
    return;
  }

  const __amdgpu_buffer_rsrc_t rs = __builtin_amdgcn_make_buffer_rsrc((void*)a.ws, 0, (int)WS_END, 0x00020000);
  const u32x2 gen = __builtin_amdgcn_raw_buffer_load_b64(rs, WS_GEN, 0, AUX_SC1);
  unsigned short xv[4];
  {
    const GAS unsigned char* xp = (const GAS unsigned char*)(qbr ? a.xB : a.xA) + 2 * (qrh * 256 + w * 32 + (lane >> 3));
#pragma unroll
    for (int i = 0; i < 4; ++i) xv[i] = *(const GAS unsigned short*)(xp + 16 * i);
  }
  if (tid < (L_RED - L_FULL) / 4) *(LAS unsigned*)(lds + L_FULL + 4 * tid) = 0u;
  LDS_BARRIER();

  lds_wait_ge<1>(lds, L_FULL + 4 * (w >> 1), (unsigned)((w >> 1) + 1));
  {
    u32x4 wv[4];
#pragma unroll
    for (int i = 0; i < 4; ++i) wv[i] = *(const LAS u32x4*)(lds + L_RING + (w >> 1) * SLOT + (w & 1) * 4096 + i * 1024 + lane * 16);
    float acc[8];
#pragma unroll
    for (int k = 0; k < 8; ++k) acc[k] = 0.f;
#pragma unroll
    for (int i = 0; i < 4; ++i) {
      const float x = lo16(xv[i]);
#pragma unroll
      for (int k = 0; k < 4; ++k) { acc[2 * k] = fmaf(x, lo16(wv[i][k]), acc[2 * k]); acc[2 * k + 1] = fmaf(x, hi16(wv[i][k]), acc[2 * k + 1]); }
    }
#pragma unroll
    for (int k = 0; k < 8; ++k) { float v = acc[k]; v += dpp<ROR8>(v); v = swap16_sum(v); v = swap32_sum(v); acc[k] = v; }
    if (lane < 8) {
      LAS f32x4* red = (LAS f32x4*)(lds + L_RED + w * 256 + lane * 32);
      red[0] = f32x4{acc[0], acc[1], acc[2], acc[3]}; red[1] = f32x4{acc[4], acc[5], acc[6], acc[7]};
    }
  }
  lds_arrive(lds, L_CNT + C_QRED, lane);
  const unsigned tagA = __builtin_amdgcn_readfirstlane(gen[0]) + 1u, tagB = __builtin_amdgcn_readfirstlane(gen[1]) + 1u;
  if (w == 0) {
    lds_wait_ge<0>(lds, L_CNT + C_QRED, NC);
    if (lane < 4) lds_st(lds, L_FREE + 4 * lane, (unsigned)(lane + 1));
    if (lane < 32) {
      f32x2 s = {0.f, 0.f};
#pragma unroll
      for (int ww = 0; ww < 8; ++ww) s += *(const LAS f32x2*)(lds + L_RED + ww * 256 + lane * 8);
      __builtin_amdgcn_raw_buffer_store_b128(u32x4{tagA, f2u(s[0]), f2u(s[1]), tagB}, rs, WS_QG + ((h * 32 + j) * 32 + lane) * 16, 0, AUX_SC1);
    }
  }
  STAMP(1);
  int slot = (4 + w) % NS;
  const unsigned kr0 = i16 * 256 + (((0 + hh) ^ i16) * 16), kr1 = i16 * 256 + (((4 + hh) ^ i16) * 16), kr2 = i16 * 256 + (((8 + hh) ^ i16) * 16), kr3 = i16 * 256 + (((12 + hh) ^ i16) * 16);
  const unsigned vr = hh * 1024 + i16 * 16;
  auto fetch = [&](int it, int kind, u32x4 (&T)[8]) {
    const unsigned seq1 = (unsigned)(4 + 8 * it + w + 1);
    lds_wait_ge<1>(lds, L_FULL + 4 * slot, seq1);
    const LAS unsigned char* sp = lds + L_RING + slot * SLOT;
    if (kind != 2) { T[0] = *(const LAS u32x4*)(sp + kr0); T[1] = *(const LAS u32x4*)(sp + kr1); T[2] = *(const LAS u32x4*)(sp + kr2); T[3] = *(const LAS u32x4*)(sp + kr3); }
    else {
#pragma unroll
      for (int r = 0; r < 4; ++r) T[r] = *(const LAS u32x4*)(sp + vr + r * 256);
    }
    if (kind == 1) { T[4] = *(const LAS u32x4*)(sp + 4096 + kr0); T[5] = *(const LAS u32x4*)(sp + 4096 + kr1); T[6] = *(const LAS u32x4*)(sp + 4096 + kr2); T[7] = *(const LAS u32x4*)(sp + 4096 + kr3); }
    else {
#pragma unroll
      for (int r = 0; r < 4; ++r) T[4 + r] = *(const LAS u32x4*)(sp + 4096 + vr + r * 256);
    }
    LDS_WAIT();
    lds_st(lds, L_FREE + 4 * slot, seq1);
    asm volatile("" : "+v"(T[0]), "+v"(T[1]), "+v"(T[2]), "+v"(T[3]), "+v"(T[4]), "+v"(T[5]), "+v"(T[6]), "+v"(T[7]) :: "memory");
    slot += 8; if (slot >= NS) slot -= NS;
  };
  u32x4 X0[8], X1[8], X2[8];
  fetch(0, 0, X0);
  if (PRE >= 2) fetch(1, 1, X1);
  if (PRE >= 3) fetch(2, 1, X2);
  {
    const unsigned off = WS_QG + ((h * 32 + 2 * (tid >> 5)) * 32 + (tid & 31)) * 16;
    u32x4 g0 = {0u, 0u, 0u, 0u}, g1 = {0u, 0u, 0u, 0u};
    bool ok = false;
    for (unsigned spin = 0; spin < SPIN_CAP; ++spin) {
      MEMFENCE();
      if (!ok) {
        g0 = __builtin_amdgcn_raw_buffer_load_b128(rs, off, 0, AUX_SC1);
        g1 = __builtin_amdgcn_raw_buffer_load_b128(rs, off + 512, 0, AUX_SC1);
        ok = g0[0] == tagA && g0[3] == tagB && g1[0] == tagA && g1[3] == tagB;
      }
      if (__all(ok)) break;
      if (QSLEEP) __builtin_amdgcn_s_sleep(QSLEEP);
    }
    *(LAS unsigned*)(lds + L_Q + tid * 4) = cvtpk(u2f(g0[1]) + u2f(g1[1]), u2f(g0[2]) + u2f(g1[2]));
  }
  lds_arrive(lds, L_CNT + C_QIN, lane);
  lds_wait_ge<0>(lds, L_CNT + C_QIN, NC);
  u32x4 qA[4], qB[4];
#pragma unroll
  for (int s = 0; s < 4; ++s) {
    qA[s] = *(const LAS u32x4*)(lds + L_Q + ((i16 >> 2) * 128 + 32 * s + 8 * hh) * 2);
    qB[s] = *(const LAS u32x4*)(lds + L_Q + 1024 + ((i16 >> 2) * 128 + 32 * s + 8 * hh) * 2);
  }
  STAMP(2);

  Sel sel;
  sel.l0 = jj == 0 ? 0x0c0c0100u : jj == 1 ? 0x01000c0cu : 0x0c0c0c0cu;  sel.h0 = jj == 0 ? 0x0c0c0302u : jj == 1 ? 0x03020c0cu : 0x0c0c0c0cu;
  sel.l1 = jj == 2 ? 0x0c0c0100u : jj == 3 ? 0x01000c0cu : 0x0c0c0c0cu;  sel.h1 = jj == 2 ? 0x0c0c0302u : jj == 3 ? 0x03020c0cu : 0x0c0c0c0cu;

  f32x4 b0 = {0.f, 0.f, 0.f, 0.f}, b1 = {0.f, 0.f, 0.f, 0.f};
  {
    const u32x2 t = tile_scores(X0, qB);
    const float m2 = swap32_max(swap16_max(max4(t))) * LOG2E;
    float l = 0.f;
    const u32x2 p = to_probs(t, m2, l);
    l = swap32_sum(swap16_sum(l));
    tile_pv(b0, b1, X0 + 4, p, sel);
    if (hh == 0 && jj == 0) *(LAS f32x2*)(lds + L_TB + ((i16 >> 2) * 8 + w) * 8) = f32x2{m2, l};
  }

  u32x2 P[16];
  float mx = -INFINITY;
#pragma unroll
  for (int i = 0; i < 8; ++i) {
    if (i == 0) { if (PRE < 2) fetch(1, 1, X1); }
    else if (i == 1) { if (PRE < 3) fetch(2, 1, X2); }
    else fetch(i + 1, 1, X1);
    const u32x4* T = i == 1 ? X2 : X1;
    P[2 * i] = tile_scores(T, qA);
    P[2 * i + 1] = tile_scores(T + 4, qA);
    mx = fmaxf(mx, fmaxf(max4(P[2 * i]), max4(P[2 * i + 1])));
  }
  STAMP(13);
  mx = swap32_max(swap16_max(mx));
  if (hh == 0 && jj == 0) *(LAS float*)(lds + L_T1 + ((i16 >> 2) * 8 + w) * 4) = mx;
  lds_arrive(lds, L_CNT + C_MAX, lane);
  lds_wait_ge<0>(lds, L_CNT + C_MAX, NC);
  {
    const f32x4 m0 = *(const LAS f32x4*)(lds + L_T1 + (i16 >> 2) * 32), m1 = *(const LAS f32x4*)(lds + L_T1 + (i16 >> 2) * 32 + 16);
    const float m2 = fmaxf(fmaxf(fmaxf(m0[0], m0[1]), fmaxf(m0[2], m0[3])), fmaxf(fmaxf(m1[0], m1[1]), fmaxf(m1[2], m1[3]))) * LOG2E;
    float l = 0.f;
#pragma unroll
    for (int t = 0; t < 16; ++t) P[t] = to_probs(P[t], m2, l);
    l = swap32_sum(swap16_sum(l));
    if (hh == 0 && jj == 0) *(LAS float*)(lds + L_T2 + ((i16 >> 2) * 8 + w) * 4) = l;
  }
  lds_arrive(lds, L_CNT + C_SUM, lane);
  lds_wait_ge<0>(lds, L_CNT + C_SUM, NC);
  const int mbr = w >> 2, mg = w & 3;
  {
    const int ww = lane & 7;
    float M, L;
    if (mbr == 0) {
      M = oct_max(*(const LAS float*)(lds + L_T1 + (mg * 8 + ww) * 4)) * LOG2E;
      L = oct_sum(*(const LAS float*)(lds + L_T2 + (mg * 8 + ww) * 4));
    } else {
      const f32x2 ml = *(const LAS f32x2*)(lds + L_TB + (mg * 8 + ww) * 8);
      M = oct_max(ml[0]);
      L = oct_sum(ml[1] * __builtin_amdgcn_exp2f(ml[0] - M));
    }
    if (lane == 0) __builtin_amdgcn_raw_buffer_store_b128(u32x4{tagA, f2u(M), f2u(L), tagB}, rs, WS_ML + (((h * 2 + mbr) * 4 + mg) * 32 + j) * 16, 0, AUX_SC1);
  }
  STAMP(14);

  f32x4 a0 = {0.f, 0.f, 0.f, 0.f}, a1 = {0.f, 0.f, 0.f, 0.f};
  float fA = 0.f, fB = 0.f;
#pragma unroll
  for (int i = 0; i < 8; ++i) {
    if (i == 4) {
      const unsigned off = WS_ML + (((h * 2 + mbr) * 4 + mg) * 32 + (lane & 31)) * 16;
      u32x4 mv = {0u, 0u, 0u, 0u};
      bool ok = false;
      for (unsigned spin = 0; spin < SPIN_CAP; ++spin) {
        MEMFENCE();
        if (!ok) { mv = __builtin_amdgcn_raw_buffer_load_b128(rs, off, 0, AUX_SC1); ok = mv[0] == tagA && mv[3] == tagB; }
        if (__all(ok)) break;
        __builtin_amdgcn_s_sleep(16);
      }
      const float M = swap16_max(row_max(u2f(mv[1])));
      const float L = swap16_sum(row_sum(u2f(mv[2]) * __builtin_amdgcn_exp2f(u2f(mv[1]) - M)));
      if (lane == 0) *(LAS f32x2*)(lds + L_FF + w * 8) = f32x2{M, 1.0f / L};
      lds_arrive(lds, L_CNT + C_FF, lane);
    }
    if (i == 7) {
      lds_wait_ge<0>(lds, L_CNT + C_FF, NC);
      const f32x4 m0 = *(const LAS f32x4*)(lds + L_T1 + hh * 32), m1 = *(const LAS f32x4*)(lds + L_T1 + hh * 32 + 16);
      const float m2 = fmaxf(fmaxf(fmaxf(m0[0], m0[1]), fmaxf(m0[2], m0[3])), fmaxf(fmaxf(m1[0], m1[1]), fmaxf(m1[2], m1[3]))) * LOG2E;
      const f32x2 FA = *(const LAS f32x2*)(lds + L_FF + hh * 8), FB = *(const LAS f32x2*)(lds + L_FF + (4 + hh) * 8);
      const float mB = *(const LAS float*)(lds + L_TB + (hh * 8 + w) * 8);
      fA = __builtin_amdgcn_exp2f(m2 - FA[0]) * FA[1];
      fB = __builtin_amdgcn_exp2f(mB - FB[0]) * FB[1];
      const int s = lane < MRG_SLOTS ? lane : 0;
      const unsigned need = (unsigned)(s + NS * ((139 - s) / NS) + 1);
      for (unsigned spin = 0; spin < LSPIN_CAP; ++spin) { MEMFENCE(); if (__all(*(volatile LAS unsigned*)(lds + L_FREE + 4 * s) >= need)) break; }
      MEMFENCE();
      LAS float* rB = (LAS float*)(lds + L_MRG) + ((8 + w) * 4 + hh) * MREC + 8 * i16;
      *(LAS f32x4*)(rB) = b0 * fB; *(LAS f32x4*)(rB + 4) = b1 * fB;
    }
    fetch(9 + i, 2, X1);
    tile_pv(a0, a1, X1, P[2 * i], sel);
    tile_pv(a0, a1, X1 + 4, P[2 * i + 1], sel);
  }
  STAMP(3);
  if (w == 7) WSTAMP(11);

  const unsigned tA = tagA & 0xffffffu, tB = tagB & 0xffffffu;
  {
    LAS float* rA = (LAS float*)(lds + L_MRG) + (w * 4 + hh) * MREC + 8 * i16;
    *(LAS f32x4*)(rA) = a0 * fA; *(LAS f32x4*)(rA + 4) = a1 * fA;
  }
  LDS_BARRIER();
  if (tid < 256) {
    const int br = tid >> 7, g = (tid >> 5) & 3, dq = tid & 31;
    const LAS float* mrg = (const LAS float*)(lds + L_MRG) + (br * 8 * 4 + g) * MREC + 4 * dq;
    f32x4 o4[8];
#pragma unroll
    for (int ww = 0; ww < 8; ++ww) o4[ww] = *(const LAS f32x4*)(mrg + ww * 4 * MREC);
    const f32x4 o = ((o4[0] + o4[1]) + (o4[2] + o4[3])) + ((o4[4] + o4[5]) + (o4[6] + o4[7]));
    const int jc = br * 16 + g * 4 + (dq >> 3), k = dq & 7;
    __builtin_amdgcn_raw_buffer_store_b128(pack4(o, tA, tB), rs, WS_I1 + (((h * 32 + jc) * 32 + j) * 8 + k) * 16, 0, AUX_SC1);
  }
  STAMP(4);

  const unsigned offA = WS_I1 + ((h * 32 + j) * 32 * 8 + (w & 3) * 64 + lane) * 16;
  u32x4 av = {0u, 0u, 0u, 0u};
  if (w >= 4) av = __builtin_amdgcn_raw_buffer_load_b128(rs, offA, 0, AUX_SC1);
  MEMFENCE();
  float W[32];
  lds_wait_ge<0>(lds, L_FULL + 4 * (143 % NS), 144u);
  {
    const LAS unsigned short* wp = (const LAS unsigned short*)(lds + L_RING + (140 % NS) * SLOT) + 64 * w + lane;
#pragma unroll
    for (int r = 0; r < 32; ++r) W[r] = u2f((unsigned)wp[r * 512] << 16);
#pragma unroll
    for (int r = 0; r < 32; r += 8) asm volatile("" : "+v"(W[r]), "+v"(W[r + 1]), "+v"(W[r + 2]), "+v"(W[r + 3]), "+v"(W[r + 4]), "+v"(W[r + 5]), "+v"(W[r + 6]), "+v"(W[r + 7]));
  }
  if (w >= 4) {
    bool ok = tags4(av, tA, tB);
    for (unsigned spin = 0; spin < SPIN_CAP && !__all(ok); ++spin) {
      MEMFENCE();
      if (!ok) { av = __builtin_amdgcn_raw_buffer_load_b128(rs, offA, 0, AUX_SC1); ok = tags4(av, tA, tB); }
      if (PSLEEP) __builtin_amdgcn_s_sleep(PSLEEP);
    }
    if (w == 4) WSTAMP(5);
    const f32x4 o = red8_16_32(unpack4(av));
    if (lane < 8) {
#pragma unroll
      for (int e = 0; e < 4; ++e) *(LAS float*)(lds + L_H1 + ((4 * lane + e) * 4 + (w & 3)) * 4) = o[e];
    }
  }
  LDS_BARRIER();
  {
    const f32x4 ra = *(const LAS f32x4*)(lds + L_H1 + i16 * 16), rb = *(const LAS f32x4*)(lds + L_H1 + (16 + i16) * 16);
    const unsigned ob = cvtpk((ra[0] + ra[1]) + (ra[2] + ra[3]), (rb[0] + rb[1]) + (rb[2] + rb[3]));
    const float oa = lo16(ob), oc = hi16(ob);
    float y0 = 0.f, y1 = 0.f, y2 = 0.f, y3 = 0.f;
#define FM(acc, o, wv, n) "v_fmac_f32_dpp " acc ", " o ", " wv " row_newbcast:" #n " row_mask:0xf bank_mask:0xf\n\t"
#define ROWS(n0, n1, n2, n3) asm volatile("s_nop 1\n\t" \
      FM("%0", "%4", "%6", n0) FM("%1", "%5", "%7", n0) FM("%2", "%4", "%8", n1) FM("%3", "%5", "%9", n1) \
      FM("%0", "%4", "%10", n2) FM("%1", "%5", "%11", n2) FM("%2", "%4", "%12", n3) FM("%3", "%5", "%13", n3) \
      : "+v"(y0), "+v"(y1), "+v"(y2), "+v"(y3) \
      : "v"(oa), "v"(oc), "v"(W[n0]), "v"(W[16 + n0]), "v"(W[n1]), "v"(W[16 + n1]), "v"(W[n2]), "v"(W[16 + n2]), "v"(W[n3]), "v"(W[16 + n3]));
    ROWS(0, 1, 2, 3) ROWS(4, 5, 6, 7) ROWS(8, 9, 10, 11) ROWS(12, 13, 14, 15)
#undef ROWS
#undef FM
    const float y = (y0 + y1) + (y2 + y3);
    const f32x4 y4 = {y, dpp<0x55>(y), dpp<0xAA>(y), dpp<0xFF>(y)};
    const int c = (j >> 4) * 16 + 2 * w + (lane >> 5), k = (lane & 31) >> 2, pidx = h * 16 + (j & 15);
    if (jj == 0) __builtin_amdgcn_raw_buffer_store_b128(pack4(y4, tA, tB), rs, WS_I2 + ((c * 128 + pidx) * 8 + k) * 16, 0, AUX_SC1);
  }
  STAMP(6);

  if (j < 4) {
    const int c = h * 4 + j;
    const unsigned off = WS_I2 + (c * 128 + 16 * w) * 128 + lane * 16;
    u32x4 v[2] = {{0u, 0u, 0u, 0u}, {0u, 0u, 0u, 0u}};
    bool ok = false;
    for (unsigned spin = 0; spin < SPIN_CAP; ++spin) {
      MEMFENCE();
      if (!ok) {
#pragma unroll
        for (int i = 0; i < 2; ++i) v[i] = __builtin_amdgcn_raw_buffer_load_b128(rs, off + i * 1024, 0, AUX_SC1);
        ok = tags4(v[0], tA, tB) && tags4(v[1], tA, tB);
      }
      if (__all(ok)) break;
      if (PSLEEP) __builtin_amdgcn_s_sleep(PSLEEP);
    }
    STAMP(7);
    const f32x4 s4 = red8_16_32(unpack4(v[0]) + unpack4(v[1]));
    if (lane < 8) {
#pragma unroll
      for (int e = 0; e < 4; ++e) *(LAS float*)(lds + L_F + ((4 * lane + e) * 8 + w) * 4) = s4[e];
    }
    LDS_BARRIER();
    if (tid < 32) {
      const f32x4 p0 = *(const LAS f32x4*)(lds + L_F + tid * 32), p1 = *(const LAS f32x4*)(lds + L_F + tid * 32 + 16);
      const float s = ((p0[0] + p0[1]) + (p0[2] + p0[3])) + ((p1[0] + p1[1]) + (p1[2] + p1[3]));
      const unsigned pk = cvtpk(s, dpp<XOR1>(s));
      if ((tid & 1) == 0) *(GAS unsigned*)((GAS unsigned char*)a.out + (c * 32 + tid) * 2) = pk;
      if (c == 0 && tid == 0) __builtin_amdgcn_raw_buffer_store_b64(u32x2{tagA, tagB}, rs, WS_GEN, 0, AUX_SC1);
    }
  }
  DUMP_STAMPS();
}

extern "C" void kernel_launch(void* const* d_in, const int* in_sizes, int n_in, void* d_out, int out_size, void* d_ws, size_t ws_size, hipStream_t stream) {
  Args a;
  a.xA = (const unsigned char*)d_in[0]; a.kA = (const unsigned char*)d_in[1]; a.vA = (const unsigned char*)d_in[2]; a.wqA = (const unsigned char*)d_in[3]; a.woA = (const unsigned char*)d_in[4];
  a.xB = (const unsigned char*)d_in[5]; a.kB = (const unsigned char*)d_in[6]; a.vB = (const unsigned char*)d_in[7]; a.wqB = (const unsigned char*)d_in[8]; a.woB = (const unsigned char*)d_in[9];
  a.out = (unsigned short*)d_out;
  a.ws = (unsigned char*)d_ws;
  hipLaunchKernelGGL(fused_decode, dim3(NWG), dim3(NTHR), 0, stream, a);
}
```

```cpp
#include <hip/hip_runtime.h>
#include <hip/hip_bf16.h>
#include <stdint.h>

#define GAS __attribute__((address_space(1)))
#define LAS __attribute__((address_space(3)))
typedef unsigned u32x4 __attribute__((ext_vector_type(4)));
typedef unsigned u32x2 __attribute__((ext_vector_type(2)));
typedef float f32x4 __attribute__((ext_vector_type(4)));
typedef float f32x2 __attribute__((ext_vector_type(2)));
typedef __bf16 bf16x8 __attribute__((ext_vector_type(8)));
typedef __bf16 bf16x2 __attribute__((ext_vector_type(2)));
typedef short s16x4 __attribute__((ext_vector_type(4)));

#define AMPK 0
#define AMPS 4
#define NS 18
#define WIN_LD 22
#define PRE 3
#define QSLEEP 16
#define PSLEEP 0
#define HOLD 0
#define POLB 6
#define BENCH 0
#define STAMPS 1

constexpr int SK = 65536, WIN = 4096;
constexpr int NWG = 256, NC = 8, NTHR = (NC + 1) * 64;
constexpr int SLOT = 8192, NSEQ = 4 + 8 + 64 + 64 + 4;
constexpr int LAG = (WIN_LD + 7) / 8;
constexpr float LOG2E = 1.4426950408889634f;
constexpr float SCALE = 0.08838834764831845f;
constexpr unsigned SPIN_CAP = 1u << 20, LSPIN_CAP = 1u << 24;

constexpr unsigned WS_GEN = 0;
constexpr unsigned WS_QG = 4096;
constexpr unsigned WS_ML = WS_QG + 8 * 32 * 32 * 16;
constexpr unsigned WS_I1 = WS_ML + 256 * 128;
constexpr unsigned WS_I2 = WS_I1 + 8 * 32 * 32 * 128;
constexpr unsigned WS_END = WS_I2 + 32 * 128 * 128;
constexpr unsigned WS_STAMP = WS_END;

constexpr int L_RING = 0;
constexpr int L_FULL = NS * SLOT;
constexpr int L_FREE = L_FULL + 128;
constexpr int L_CNT = L_FREE + 128;
constexpr int L_RED = L_CNT + 64;
constexpr int L_Q = L_RED + 2048;
constexpr int L_T1 = L_Q + 2048;
constexpr int L_T2 = L_T1 + 128;
constexpr int L_TB = L_T2 + 128;
constexpr int L_FF = L_TB + 256;
constexpr int L_H1 = L_FF + 64;
constexpr int L_F = L_H1 + 512;
constexpr int L_END = L_F + 1024 + 128;
constexpr int C_QRED = 0, C_QIN = 4, C_MAX = 8, C_SUM = 12, C_FF = 16, C_MRG = 20;
constexpr int MREC = 132;
constexpr int L_MRG = 0;
constexpr int MRG_SLOTS = 5;
static_assert(L_END <= 160 * 1024, "LDS");
static_assert(2 * 8 * 4 * MREC * 4 <= MRG_SLOTS * SLOT, "merge area");
static_assert((140 % NS) >= MRG_SLOTS && (143 % NS) >= MRG_SLOTS && (140 % NS) <= (143 % NS), "wo slots contiguous and clear of the merge area");
static_assert((132 % NS) >= MRG_SLOTS && (139 % NS) >= MRG_SLOTS && (132 % NS) <= (139 % NS), "last round clear of the merge area");
static_assert(WIN_LD >= 9 && WIN_LD <= 48, "window");

__device__ __forceinline__ unsigned cvtpk(float lo, float hi) { f32x2 f = {lo, hi}; return __builtin_bit_cast(unsigned, __builtin_convertvector(f, bf16x2)); }
__device__ __forceinline__ float lo16(unsigned u) { return __builtin_bit_cast(float, u << 16); }
__device__ __forceinline__ float hi16(unsigned u) { return __builtin_bit_cast(float, u & 0xffff0000u); }
__device__ __forceinline__ float u2f(unsigned u) { return __builtin_bit_cast(float, u); }
__device__ __forceinline__ unsigned f2u(float f) { return __builtin_bit_cast(unsigned, f); }
template <int CTRL> __device__ __forceinline__ float dpp(float x) { return u2f(__builtin_amdgcn_mov_dpp(f2u(x), CTRL, 0xf, 0xf, true)); }
constexpr int XOR1 = 0xB1, XOR2 = 0x4E, HMIR = 0x141;
constexpr int ROR1 = 0x121, ROR2 = 0x122, ROR4 = 0x124, ROR8 = 0x128;
__device__ __forceinline__ float swap16_max(float x) { auto s = __builtin_amdgcn_permlane16_swap(f2u(x), f2u(x), false, false); return fmaxf(u2f(s[0]), u2f(s[1])); }
__device__ __forceinline__ float swap32_max(float x) { auto s = __builtin_amdgcn_permlane32_swap(f2u(x), f2u(x), false, false); return fmaxf(u2f(s[0]), u2f(s[1])); }
__device__ __forceinline__ float swap16_sum(float x) { auto s = __builtin_amdgcn_permlane16_swap(f2u(x), f2u(x), false, false); return u2f(s[0]) + u2f(s[1]); }
__device__ __forceinline__ float swap32_sum(float x) { auto s = __builtin_amdgcn_permlane32_swap(f2u(x), f2u(x), false, false); return u2f(s[0]) + u2f(s[1]); }
__device__ __forceinline__ float row_sum(float x) { x += dpp<ROR8>(x); x += dpp<ROR4>(x); x += dpp<ROR2>(x); x += dpp<ROR1>(x); return x; }
__device__ __forceinline__ float row_max(float x) { x = fmaxf(x, dpp<ROR8>(x)); x = fmaxf(x, dpp<ROR4>(x)); x = fmaxf(x, dpp<ROR2>(x)); x = fmaxf(x, dpp<ROR1>(x)); return x; }
template <int N> __device__ __forceinline__ float bcast(float x) { return dpp<0x150 + N>(x); }
__device__ __forceinline__ float oct_sum(float x) { x += dpp<XOR1>(x); x += dpp<XOR2>(x); x += dpp<HMIR>(x); return x; }
__device__ __forceinline__ float oct_max(float x) { x = fmaxf(x, dpp<XOR1>(x)); x = fmaxf(x, dpp<XOR2>(x)); x = fmaxf(x, dpp<HMIR>(x)); return x; }
#define LDS_BARRIER() asm volatile("s_waitcnt lgkmcnt(0)\n\ts_barrier" ::: "memory")
#define LDS_WAIT() asm volatile("s_waitcnt lgkmcnt(0)" ::: "memory")
#define MEMFENCE() asm volatile("" ::: "memory")
constexpr int AUX_SC1 = 16;

__device__ __forceinline__ u32x4 pack4(f32x4 v, unsigned tA, unsigned tB) {
  const unsigned t0 = f2u(v[0]) + 0x800u, t1 = f2u(v[1]) + 0x800u, t2 = f2u(v[2]) + 0x800u, t3 = f2u(v[3]) + 0x800u;
  return u32x4{tA | ((t0 << 12) & 0xff000000u), (t0 >> 20) | (t1 & 0xfffff000u), (t2 >> 12) | ((t3 << 8) & 0xfff00000u), (t3 >> 24) | (tB << 8)};
}
__device__ __forceinline__ f32x4 unpack4(u32x4 d) {
  return f32x4{u2f((d[1] << 20) | ((d[0] >> 12) & 0xff000u)), u2f(d[1] & 0xfffff000u), u2f(d[2] << 12), u2f(((d[2] >> 8) & 0xfff000u) | (d[3] << 24))};
}
__device__ __forceinline__ bool tags4(u32x4 d, unsigned tA, unsigned tB) { return (d[0] & 0xffffffu) == tA && (d[3] >> 8) == tB; }
__device__ __forceinline__ f32x4 red8_16_32(f32x4 v) {
#pragma unroll
  for (int e = 0; e < 4; ++e) { float x = v[e]; x += dpp<ROR8>(x); v[e] = swap32_sum(swap16_sum(x)); }
  return v;
}

struct Args {
  const unsigned char *xA, *kA, *vA, *wqA, *woA, *xB, *kB, *vB, *wqB, *woB;
  unsigned short* out;
  unsigned char* ws;
};

__device__ __forceinline__ unsigned lds_ld(LAS unsigned char* L, int off) { return __builtin_amdgcn_readfirstlane(*(volatile LAS unsigned*)(L + off)); }
__device__ __forceinline__ void lds_st(LAS unsigned char* L, int off, unsigned v) { *(volatile LAS unsigned*)(L + off) = v; }
template <int SLEEP> __device__ __forceinline__ void lds_wait_ge(LAS unsigned char* L, int off, unsigned need) {
  for (unsigned spin = 0; lds_ld(L, off) < need && spin < LSPIN_CAP; ++spin) { if (SLEEP) __builtin_amdgcn_s_sleep(SLEEP); }
  MEMFENCE();
}
__device__ __forceinline__ void lds_arrive(LAS unsigned char* L, int off, int lane) {
  LDS_WAIT();
  if (lane == 0) __hip_atomic_fetch_add((LAS unsigned*)(L + off), 1u, __ATOMIC_RELAXED, __HIP_MEMORY_SCOPE_WORKGROUP);
}

template <int POL> __device__ __forceinline__ void dma1p(unsigned dst, unsigned voff, const unsigned char* base) {
  unsigned keep;
  if constexpr (POL == 0)
  asm volatile("s_mov_b32 %0, m0\n\ts_mov_b32 m0, %1\n\ts_nop 0\n\tglobal_load_lds_dwordx4 %2, %3 nt\n\ts_mov_b32 m0, %0\n\ts_waitcnt vmcnt(%4)"
               : "=&s"(keep) : "s"(dst), "v"(voff), "s"(base), "i"(WIN_LD) : "memory");
  else
  asm volatile("s_mov_b32 %0, m0\n\ts_mov_b32 m0, %1\n\ts_nop 0\n\tglobal_load_lds_dwordx4 %2, %3\n\ts_mov_b32 m0, %0\n\ts_waitcnt vmcnt(%4)"
               : "=&s"(keep) : "s"(dst), "v"(voff), "s"(base), "i"(WIN_LD) : "memory");
}
template <int POL> __device__ __forceinline__ void dma4p(unsigned dst, unsigned v0, unsigned v1, unsigned v2, unsigned v3, const unsigned char* base) {
  unsigned keep;
  if constexpr (POL != 0)
  asm volatile("s_mov_b32 %0, m0\n\ts_mov_b32 m0, %1\n\ts_nop 0\n\t"
               "global_load_lds_dwordx4 %2, %6\n\ts_waitcnt vmcnt(%7)\n\t"
               "global_load_lds_dwordx4 %3, %6 offset:1024\n\ts_waitcnt vmcnt(%7)\n\t"
               "global_load_lds_dwordx4 %4, %6 offset:2048\n\ts_waitcnt vmcnt(%7)\n\t"
               "global_load_lds_dwordx4 %5, %6 offset:3072\n\ts_waitcnt vmcnt(%7)\n\t"
               "s_mov_b32 m0, %0"
               : "=&s"(keep) : "s"(dst), "v"(v0), "v"(v1), "v"(v2), "v"(v3), "s"(base), "i"(WIN_LD) : "memory");
  else
  asm volatile("s_mov_b32 %0, m0\n\ts_mov_b32 m0, %1\n\ts_nop 0\n\t"
               "global_load_lds_dwordx4 %2, %6 nt\n\ts_waitcnt vmcnt(%7)\n\t"
               "global_load_lds_dwordx4 %3, %6 offset:1024 nt\n\ts_waitcnt vmcnt(%7)\n\t"
               "global_load_lds_dwordx4 %4, %6 offset:2048 nt\n\ts_waitcnt vmcnt(%7)\n\t"
               "global_load_lds_dwordx4 %5, %6 offset:3072 nt\n\ts_waitcnt vmcnt(%7)\n\t"
               "s_mov_b32 m0, %0"
               : "=&s"(keep) : "s"(dst), "v"(v0), "v"(v1), "v"(v2), "v"(v3), "s"(base), "i"(WIN_LD) : "memory");
}
__device__ __forceinline__ void dma4(unsigned dst, unsigned v0, unsigned v1, unsigned v2, unsigned v3, const unsigned char* base) { dma4p<0>(dst, v0, v1, v2, v3, base); }
template <int N> __device__ __forceinline__ void vm_wait() { asm volatile("s_waitcnt vmcnt(%0)" :: "i"(N) : "memory"); }

__device__ __forceinline__ u32x2 tile_scores(const u32x4* K, const u32x4 (&q)[4]) {
  f32x4 s = {0.f, 0.f, 0.f, 0.f};
#pragma unroll
  for (int i = 0; i < 4; ++i) s = __builtin_amdgcn_mfma_f32_16x16x32_bf16(__builtin_bit_cast(bf16x8, K[i]), __builtin_bit_cast(bf16x8, q[i]), s, 0, 0, 0);
  const unsigned r01 = cvtpk(s[0], s[1]), r23 = cvtpk(s[2], s[3]);
  return u32x2{cvtpk(lo16(r01) * SCALE, hi16(r01) * SCALE), cvtpk(lo16(r23) * SCALE, hi16(r23) * SCALE)};
}
__device__ __forceinline__ float max4(u32x2 t) { return fmaxf(fmaxf(lo16(t[0]), hi16(t[0])), fmaxf(lo16(t[1]), hi16(t[1]))); }
__device__ __forceinline__ u32x2 to_probs(u32x2 t, float m2, float& l) {
  const float p0 = __builtin_amdgcn_exp2f(fmaf(lo16(t[0]), LOG2E, -m2)), p1 = __builtin_amdgcn_exp2f(fmaf(hi16(t[0]), LOG2E, -m2));
  const float p2 = __builtin_amdgcn_exp2f(fmaf(lo16(t[1]), LOG2E, -m2)), p3 = __builtin_amdgcn_exp2f(fmaf(hi16(t[1]), LOG2E, -m2));
  l += (p0 + p1) + (p2 + p3);
  return u32x2{cvtpk(p0, p1), cvtpk(p2, p3)};
}
struct Sel { unsigned l0, l1, h0, h1; };
__device__ __forceinline__ void tile_pv(f32x4& a0, f32x4& a1, const u32x4* V, u32x2 p, const Sel& s) {
#pragma unroll
  for (int r = 0; r < 4; ++r) {
    const unsigned pk = p[r >> 1];
    const u32x2 a = {__builtin_amdgcn_perm(0u, pk, (r & 1) ? s.h0 : s.l0), __builtin_amdgcn_perm(0u, pk, (r & 1) ? s.h1 : s.l1)};
    const u32x2 v0 = {V[r][0], V[r][1]}, v1 = {V[r][2], V[r][3]};
    a0 = __builtin_amdgcn_mfma_f32_16x16x16bf16_1k(__builtin_bit_cast(s16x4, a), __builtin_bit_cast(s16x4, v0), a0, 0, 0, 0);
    a1 = __builtin_amdgcn_mfma_f32_16x16x16bf16_1k(__builtin_bit_cast(s16x4, a), __builtin_bit_cast(s16x4, v1), a1, 0, 0, 0);
  }
}

#define STAMP(i) do { if (tid == 0) { ((volatile LAS unsigned long long*)(lds + L_END - 128))[(i)] = __builtin_amdgcn_s_memrealtime(); } } while (0)
#define WSTAMP(i) do { if (lane == 0) { ((volatile LAS unsigned long long*)(lds + L_END - 128))[(i)] = __builtin_amdgcn_s_memrealtime(); } } while (0)
#define DUMP_STAMPS() do { if (b == 0) { \
    volatile LAS unsigned long long* q_ = (volatile LAS unsigned long long*)(lds + L_END - 128); \
    { const unsigned long long tw = __builtin_amdgcn_s_memrealtime(); while (__builtin_amdgcn_s_memrealtime() < tw + 300) __builtin_amdgcn_s_sleep(1); } \
    if (tid == 0) { q_[0] = ~0ull; q_[1] = 0ull; } \
    LDS_BARRIER(); \
    if (tid < 256) { const unsigned long long v = __hip_atomic_load((GAS unsigned long long*)(a.ws + WS_STAMP) + tid, __ATOMIC_RELAXED, __HIP_MEMORY_SCOPE_AGENT); \
      const unsigned long long s0 = __hip_atomic_load((GAS unsigned long long*)(a.ws + WS_STAMP) + 256 + tid, __ATOMIC_RELAXED, __HIP_MEMORY_SCOPE_AGENT); \
      __hip_atomic_fetch_min((LAS unsigned long long*)(lds + L_END - 128), s0, __ATOMIC_RELAXED, __HIP_MEMORY_SCOPE_WORKGROUP); \
      __hip_atomic_fetch_max((LAS unsigned long long*)(lds + L_END - 128) + 1, v, __ATOMIC_RELAXED, __HIP_MEMORY_SCOPE_WORKGROUP); } \
    LDS_BARRIER(); \
    if (tid == 0) { unsigned long long d = AMPK ? 4ull * (q_[1] - q_[0]) : 0ull; \
      if (d > 100000ull) d = 100000ull; \
      const unsigned long long t1 = __builtin_amdgcn_s_memrealtime(); \
      for (unsigned sp_ = 0; sp_ < 10000000u && __builtin_amdgcn_s_memrealtime() < t1 + d; ++sp_) __builtin_amdgcn_s_sleep(1); } } } while (0)

__global__ void __launch_bounds__(NTHR, 3) fused_decode(Args a) {
  __shared__ __attribute__((aligned(16))) unsigned char lds_raw[L_END];
  LAS unsigned char* lds = (LAS unsigned char*)lds_raw;
  const int b = blockIdx.x, h = b & 7, j = b >> 3;
  const int tid = threadIdx.x, lane = tid & 63, w = __builtin_amdgcn_readfirstlane(tid >> 6);
  const int hh = lane >> 4, i16 = lane & 15, jj = lane & 3;
  const unsigned long long t_start = __builtin_amdgcn_s_memrealtime();
  const int qcb = j >> 1, qrh = j & 1, qbr = qcb >> 3;

  if (w == NC) {
    const unsigned ring = (unsigned)(size_t)(lds + L_RING);
    int pub = 0, pslot = 0, slot = 0;
    auto publish1 = [&]() { lds_st(lds, L_FULL + 4 * pslot, (unsigned)(pub + 1)); ++pub; pslot = pslot + 1 == NS ? 0 : pslot + 1; };
    auto pre = [&](int q) {
      if (q >= NS) {
        const unsigned need = (unsigned)(q - NS + 1);
        if (lds_ld(lds, L_FREE + 4 * slot) < need) {
          vm_wait<0>();
          while (pub < q) publish1();
          lds_wait_ge<1>(lds, L_FREE + 4 * slot, need);
        }
      }
    };
    auto post = [&](int q) {
      if (q - pub >= LAG) publish1();
      slot = slot + 1 == NS ? 0 : slot + 1;
    };
    {
      const unsigned char* wq = (qbr ? a.wqB : a.wqA) + (size_t)(h * 512 + 64 * (qcb & 7)) * 2 + (size_t)qrh * 256 * 8192;
      const unsigned vo = (lane >> 3) * 8192 + (lane & 7) * 16;
#pragma unroll
      for (int q = 0; q < 4; ++q) {
#pragma unroll
        for (int m = 0; m < 8; ++m) { if ((POLB & 1) && qbr) dma1p<1>(ring + q * SLOT + m * 1024, vo, wq + (size_t)(q * 64 + m * 8) * 8192); else dma1p<0>(ring + q * SLOT + m * 1024, vo, wq + (size_t)(q * 64 + m * 8) * 8192); }
        if (q == 0) LDS_BARRIER();
        post(q);
      }
    }
    const unsigned k0 = hh * 256 + ((i16 ^ (0 + hh)) * 16), k1 = hh * 256 + ((i16 ^ (4 + hh)) * 16), k2 = hh * 256 + ((i16 ^ (8 + hh)) * 16), k3 = hh * 256 + ((i16 ^ (12 + hh)) * 16);
    const unsigned vl = lane * 16;
    {
      const unsigned char* kp = a.kB + ((size_t)h * SK + (SK - WIN) + j * 128) * 256;
      const unsigned char* vp = a.vB + ((size_t)h * SK + (SK - WIN) + j * 128) * 256;
      for (int q = 4; q < 12; ++q) {
        dma4p<(POLB & 2) ? 1 : 0>(ring + slot * SLOT, k0, k1, k2, k3, kp);
        dma4p<(POLB & 4) ? 1 : 0>(ring + slot * SLOT + 4096, vl, vl, vl, vl, vp);
        kp += 4096; vp += 4096;
        post(q);
      }
      kp = a.kA + ((size_t)h * SK + j * 2048) * 256;
      for (int q = 12; q < 76; ++q) {
        pre(q);
        dma4(ring + slot * SLOT, k0, k1, k2, k3, kp);
        dma4(ring + slot * SLOT + 4096, k0, k1, k2, k3, kp + 4096);
        kp += 8192;
        post(q);
      }
      WSTAMP(12);
      vp = a.vA + ((size_t)h * SK + j * 2048) * 256;
      for (int q = 76; q < 140; ++q) {
        pre(q);
        dma4(ring + slot * SLOT, vl, vl, vl, vl, vp);
        dma4(ring + slot * SLOT + 4096, vl, vl, vl, vl, vp + 4096);
        vp += 8192;
        post(q);
      }
      WSTAMP(9);
      vp = (j >> 4 ? a.woB : a.woA) + (size_t)(h * 512 + 32 * (j & 15)) * 1024;
      for (int q = 140; q < 144; ++q) {
        pre(q);
        if ((POLB & 8) && (j >> 4)) { dma4p<1>(ring + slot * SLOT, vl, vl, vl, vl, vp); dma4p<1>(ring + slot * SLOT + 4096, vl, vl, vl, vl, vp + 4096); }
        else { dma4(ring + slot * SLOT, vl, vl, vl, vl, vp); dma4(ring + slot * SLOT + 4096, vl, vl, vl, vl, vp + 4096); }
        vp += 8192;
        post(q);
      }
    }
    __builtin_amdgcn_s_barrier();
    if (NSEQ - pub > 6) { vm_wait<48>(); publish1(); }
    if (NSEQ - pub > 5) { vm_wait<40>(); publish1(); }
    if (NSEQ - pub > 4) { vm_wait<32>(); publish1(); }
    if (NSEQ - pub > 3) { vm_wait<24>(); publish1(); }
    if (NSEQ - pub > 2) { vm_wait<16>(); publish1(); }
    if (NSEQ - pub > 1) { vm_wait<8>(); publish1(); }
    vm_wait<0>();
    while (pub < NSEQ) publish1();
    WSTAMP(10);
    return;
  }

  const __amdgpu_buffer_rsrc_t rs = __builtin_amdgcn_make_buffer_rsrc((void*)a.ws, 0, (int)WS_END, 0x00020000);
  const u32x2 gen = __builtin_amdgcn_raw_buffer_load_b64(rs, WS_GEN, 0, AUX_SC1);
  unsigned short xv[4];
  {
    const GAS unsigned char* xp = (const GAS unsigned char*)(qbr ? a.xB : a.xA) + 2 * (qrh * 256 + w * 32 + (lane >> 3));
#pragma unroll
    for (int i = 0; i < 4; ++i) xv[i] = *(const GAS unsigned short*)(xp + 16 * i);
  }
  if (tid < (L_RED - L_FULL) / 4) *(LAS unsigned*)(lds + L_FULL + 4 * tid) = 0u;
  if (tid >= 128 && tid < 160) *(LAS unsigned*)(lds + L_END - 128 + 4 * (tid - 128)) = 0u;
  LDS_BARRIER();

  lds_wait_ge<1>(lds, L_FULL + 4 * (w >> 1), (unsigned)((w >> 1) + 1));
  {
    u32x4 wv[4];
#pragma unroll
    for (int i = 0; i < 4; ++i) wv[i] = *(const LAS u32x4*)(lds + L_RING + (w >> 1) * SLOT + (w & 1) * 4096 + i * 1024 + lane * 16);
    float acc[8];
#pragma unroll
    for (int k = 0; k < 8; ++k) acc[k] = 0.f;
#pragma unroll
    for (int i = 0; i < 4; ++i) {
      const float x = lo16(xv[i]);
#pragma unroll
      for (int k = 0; k < 4; ++k) { acc[2 * k] = fmaf(x, lo16(wv[i][k]), acc[2 * k]); acc[2 * k + 1] = fmaf(x, hi16(wv[i][k]), acc[2 * k + 1]); }
    }
#pragma unroll
    for (int k = 0; k < 8; ++k) { float v = acc[k]; v += dpp<ROR8>(v); v = swap16_sum(v); v = swap32_sum(v); acc[k] = v; }
    if (lane < 8) {
      LAS f32x4* red = (LAS f32x4*)(lds + L_RED + w * 256 + lane * 32);
      red[0] = f32x4{acc[0], acc[1], acc[2], acc[3]}; red[1] = f32x4{acc[4], acc[5], acc[6], acc[7]};
    }
  }
  lds_arrive(lds, L_CNT + C_QRED, lane);
  const unsigned tagA = __builtin_amdgcn_readfirstlane(gen[0]) + 1u, tagB = __builtin_amdgcn_readfirstlane(gen[1]) + 1u;
  if (w == 0) {
    lds_wait_ge<0>(lds, L_CNT + C_QRED, NC);
    if (lane < 4) lds_st(lds, L_FREE + 4 * lane, (unsigned)(lane + 1));
    if (lane < 32) {
      f32x2 s = {0.f, 0.f};
#pragma unroll
      for (int ww = 0; ww < 8; ++ww) s += *(const LAS f32x2*)(lds + L_RED + ww * 256 + lane * 8);
      __builtin_amdgcn_raw_buffer_store_b128(u32x4{tagA, f2u(s[0]), f2u(s[1]), tagB}, rs, WS_QG + ((h * 32 + j) * 32 + lane) * 16, 0, AUX_SC1);
    }
  }
  STAMP(1);
  int slot = (4 + w) % NS;
  const unsigned kr0 = i16 * 256 + (((0 + hh) ^ i16) * 16), kr1 = i16 * 256 + (((4 + hh) ^ i16) * 16), kr2 = i16 * 256 + (((8 + hh) ^ i16) * 16), kr3 = i16 * 256 + (((12 + hh) ^ i16) * 16);
  const unsigned vr = hh * 1024 + i16 * 16;
  auto fetch = [&](int it, int kind, u32x4 (&T)[8]) {
    const unsigned seq1 = (unsigned)(4 + 8 * it + w + 1);
    lds_wait_ge<1>(lds, L_FULL + 4 * slot, seq1);
    const LAS unsigned char* sp = lds + L_RING + slot * SLOT;
    if (kind != 2) { T[0] = *(const LAS u32x4*)(sp + kr0); T[1] = *(const LAS u32x4*)(sp + kr1); T[2] = *(const LAS u32x4*)(sp + kr2); T[3] = *(const LAS u32x4*)(sp + kr3); }
    else {
#pragma unroll
      for (int r = 0; r < 4; ++r) T[r] = *(const LAS u32x4*)(sp + vr + r * 256);
    }
    if (kind == 1) { T[4] = *(const LAS u32x4*)(sp + 4096 + kr0); T[5] = *(const LAS u32x4*)(sp + 4096 + kr1); T[6] = *(const LAS u32x4*)(sp + 4096 + kr2); T[7] = *(const LAS u32x4*)(sp + 4096 + kr3); }
    else {
#pragma unroll
      for (int r = 0; r < 4; ++r) T[4 + r] = *(const LAS u32x4*)(sp + 4096 + vr + r * 256);
    }
    LDS_WAIT();
    lds_st(lds, L_FREE + 4 * slot, seq1);
    asm volatile("" : "+v"(T[0]), "+v"(T[1]), "+v"(T[2]), "+v"(T[3]), "+v"(T[4]), "+v"(T[5]), "+v"(T[6]), "+v"(T[7]) :: "memory");
    slot += 8; if (slot >= NS) slot -= NS;
  };
  u32x4 X0[8], X1[8], X2[8];
  fetch(0, 0, X0);
  if (PRE >= 2) fetch(1, 1, X1);
  if (PRE >= 3) fetch(2, 1, X2);
  {
    const unsigned off = WS_QG + ((h * 32 + 2 * (tid >> 5)) * 32 + (tid & 31)) * 16;
    u32x4 g0 = {0u, 0u, 0u, 0u}, g1 = {0u, 0u, 0u, 0u};
    bool ok = false;
    for (unsigned spin = 0; spin < SPIN_CAP; ++spin) {
      MEMFENCE();
      if (!ok) {
        g0 = __builtin_amdgcn_raw_buffer_load_b128(rs, off, 0, AUX_SC1);
        g1 = __builtin_amdgcn_raw_buffer_load_b128(rs, off + 512, 0, AUX_SC1);
        ok = g0[0] == tagA && g0[3] == tagB && g1[0] == tagA && g1[3] == tagB;
      }
      if (__all(ok)) break;
      if (QSLEEP) __builtin_amdgcn_s_sleep(QSLEEP);
    }
    *(LAS unsigned*)(lds + L_Q + tid * 4) = cvtpk(u2f(g0[1]) + u2f(g1[1]), u2f(g0[2]) + u2f(g1[2]));
  }
  lds_arrive(lds, L_CNT + C_QIN, lane);
  lds_wait_ge<0>(lds, L_CNT + C_QIN, NC);
  u32x4 qA[4], qB[4];
#pragma unroll
  for (int s = 0; s < 4; ++s) {
    qA[s] = *(const LAS u32x4*)(lds + L_Q + ((i16 >> 2) * 128 + 32 * s + 8 * hh) * 2);
    qB[s] = *(const LAS u32x4*)(lds + L_Q + 1024 + ((i16 >> 2) * 128 + 32 * s + 8 * hh) * 2);
  }
  STAMP(2);

  Sel sel;
  sel.l0 = jj == 0 ? 0x0c0c0100u : jj == 1 ? 0x01000c0cu : 0x0c0c0c0cu;  sel.h0 = jj == 0 ? 0x0c0c0302u : jj == 1 ? 0x03020c0cu : 0x0c0c0c0cu;
  sel.l1 = jj == 2 ? 0x0c0c0100u : jj == 3 ? 0x01000c0cu : 0x0c0c0c0cu;  sel.h1 = jj == 2 ? 0x0c0c0302u : jj == 3 ? 0x03020c0cu : 0x0c0c0c0cu;

  f32x4 b0 = {0.f, 0.f, 0.f, 0.f}, b1 = {0.f, 0.f, 0.f, 0.f};
  {
    const u32x2 t = tile_scores(X0, qB);
    const float m2 = swap32_max(swap16_max(max4(t))) * LOG2E;
    float l = 0.f;
    const u32x2 p = to_probs(t, m2, l);
    l = swap32_sum(swap16_sum(l));
    tile_pv(b0, b1, X0 + 4, p, sel);
    if (hh == 0 && jj == 0) *(LAS f32x2*)(lds + L_TB + ((i16 >> 2) * 8 + w) * 8) = f32x2{m2, l};
  }

  u32x2 P[16];
  float mx = -INFINITY;
#pragma unroll
  for (int i = 0; i < 8; ++i) {
    if (i == 0) { if (PRE < 2) fetch(1, 1, X1); }
    else if (i == 1) { if (PRE < 3) fetch(2, 1, X2); }
    else fetch(i + 1, 1, X1);
    const u32x4* T = i == 1 ? X2 : X1;
    P[2 * i] = tile_scores(T, qA);
    P[2 * i + 1] = tile_scores(T + 4, qA);
    mx = fmaxf(mx, fmaxf(max4(P[2 * i]), max4(P[2 * i + 1])));
  }
  STAMP(13);
  mx = swap32_max(swap16_max(mx));
  if (hh == 0 && jj == 0) *(LAS float*)(lds + L_T1 + ((i16 >> 2) * 8 + w) * 4) = mx;
  lds_arrive(lds, L_CNT + C_MAX, lane);
  lds_wait_ge<0>(lds, L_CNT + C_MAX, NC);
  {
    const f32x4 m0 = *(const LAS f32x4*)(lds + L_T1 + (i16 >> 2) * 32), m1 = *(const LAS f32x4*)(lds + L_T1 + (i16 >> 2) * 32 + 16);
    const float m2 = fmaxf(fmaxf(fmaxf(m0[0], m0[1]), fmaxf(m0[2], m0[3])), fmaxf(fmaxf(m1[0], m1[1]), fmaxf(m1[2], m1[3]))) * LOG2E;
    float l = 0.f;
#pragma unroll
    for (int t = 0; t < 16; ++t) P[t] = to_probs(P[t], m2, l);
    l = swap32_sum(swap16_sum(l));
    if (hh == 0 && jj == 0) *(LAS float*)(lds + L_T2 + ((i16 >> 2) * 8 + w) * 4) = l;
  }
  lds_arrive(lds, L_CNT + C_SUM, lane);
  lds_wait_ge<0>(lds, L_CNT + C_SUM, NC);
  const int mbr = w >> 2, mg = w & 3;
  {
    const int ww = lane & 7;
    float M, L;
    if (mbr == 0) {
      M = oct_max(*(const LAS float*)(lds + L_T1 + (mg * 8 + ww) * 4)) * LOG2E;
      L = oct_sum(*(const LAS float*)(lds + L_T2 + (mg * 8 + ww) * 4));
    } else {
      const f32x2 ml = *(const LAS f32x2*)(lds + L_TB + (mg * 8 + ww) * 8);
      M = oct_max(ml[0]);
      L = oct_sum(ml[1] * __builtin_amdgcn_exp2f(ml[0] - M));
    }
    if (lane == 0) __builtin_amdgcn_raw_buffer_store_b128(u32x4{tagA, f2u(M), f2u(L), tagB}, rs, WS_ML + (((h * 2 + mbr) * 4 + mg) * 32 + j) * 16, 0, AUX_SC1);
  }
  STAMP(14);

  f32x4 a0 = {0.f, 0.f, 0.f, 0.f}, a1 = {0.f, 0.f, 0.f, 0.f};
  float fA = 0.f, fB = 0.f;
#pragma unroll
  for (int i = 0; i < 8; ++i) {
    if (i == 4) {
      const unsigned off = WS_ML + (((h * 2 + mbr) * 4 + mg) * 32 + (lane & 31)) * 16;
      u32x4 mv = {0u, 0u, 0u, 0u};
      bool ok = false;
      for (unsigned spin = 0; spin < SPIN_CAP; ++spin) {
        MEMFENCE();
        if (!ok) { mv = __builtin_amdgcn_raw_buffer_load_b128(rs, off, 0, AUX_SC1); ok = mv[0] == tagA && mv[3] == tagB; }
        if (__all(ok)) break;
        __builtin_amdgcn_s_sleep(16);
      }
      const float M = swap16_max(row_max(u2f(mv[1])));
      const float L = swap16_sum(row_sum(u2f(mv[2]) * __builtin_amdgcn_exp2f(u2f(mv[1]) - M)));
      if (lane == 0) *(LAS f32x2*)(lds + L_FF + w * 8) = f32x2{M, 1.0f / L};
      lds_arrive(lds, L_CNT + C_FF, lane);
    }
    if (i == 7) {
      lds_wait_ge<0>(lds, L_CNT + C_FF, NC);
      const f32x4 m0 = *(const LAS f32x4*)(lds + L_T1 + hh * 32), m1 = *(const LAS f32x4*)(lds + L_T1 + hh * 32 + 16);
      const float m2 = fmaxf(fmaxf(fmaxf(m0[0], m0[1]), fmaxf(m0[2], m0[3])), fmaxf(fmaxf(m1[0], m1[1]), fmaxf(m1[2], m1[3]))) * LOG2E;
      const f32x2 FA = *(const LAS f32x2*)(lds + L_FF + hh * 8), FB = *(const LAS f32x2*)(lds + L_FF + (4 + hh) * 8);
      const float mB = *(const LAS float*)(lds + L_TB + (hh * 8 + w) * 8);
      fA = __builtin_amdgcn_exp2f(m2 - FA[0]) * FA[1];
      fB = __builtin_amdgcn_exp2f(mB - FB[0]) * FB[1];
      const int s = lane < MRG_SLOTS ? lane : 0;
      const unsigned need = (unsigned)(s + NS * ((139 - s) / NS) + 1);
      for (unsigned spin = 0; spin < LSPIN_CAP; ++spin) { MEMFENCE(); if (__all(*(volatile LAS unsigned*)(lds + L_FREE + 4 * s) >= need)) break; }
      MEMFENCE();
      LAS float* rB = (LAS float*)(lds + L_MRG) + ((8 + w) * 4 + hh) * MREC + 8 * i16;
      *(LAS f32x4*)(rB) = b0 * fB; *(LAS f32x4*)(rB + 4) = b1 * fB;
    }
    fetch(9 + i, 2, X1);
    tile_pv(a0, a1, X1, P[2 * i], sel);
    tile_pv(a0, a1, X1 + 4, P[2 * i + 1], sel);
  }
  STAMP(3);
  if (w == 7) WSTAMP(11);

  const unsigned tA = tagA & 0xffffffu, tB = tagB & 0xffffffu;
  {
    LAS float* rA = (LAS float*)(lds + L_MRG) + (w * 4 + hh) * MREC + 8 * i16;
    *(LAS f32x4*)(rA) = a0 * fA; *(LAS f32x4*)(rA + 4) = a1 * fA;
  }
  LDS_BARRIER();
  if (tid < 256) {
    const int br = tid >> 7, g = (tid >> 5) & 3, dq = tid & 31;
    const LAS float* mrg = (const LAS float*)(lds + L_MRG) + (br * 8 * 4 + g) * MREC + 4 * dq;
    f32x4 o4[8];
#pragma unroll
    for (int ww = 0; ww < 8; ++ww) o4[ww] = *(const LAS f32x4*)(mrg + ww * 4 * MREC);
    const f32x4 o = ((o4[0] + o4[1]) + (o4[2] + o4[3])) + ((o4[4] + o4[5]) + (o4[6] + o4[7]));
    const int jc = br * 16 + g * 4 + (dq >> 3), k = dq & 7;
    __builtin_amdgcn_raw_buffer_store_b128(pack4(o, tA, tB), rs, WS_I1 + (((h * 32 + jc) * 32 + j) * 8 + k) * 16, 0, AUX_SC1);
  }
  STAMP(4);

  const unsigned offA = WS_I1 + ((h * 32 + j) * 32 * 8 + (w & 3) * 64 + lane) * 16;
  u32x4 av = {0u, 0u, 0u, 0u};
  if (w >= 4) av = __builtin_amdgcn_raw_buffer_load_b128(rs, offA, 0, AUX_SC1);
  MEMFENCE();
  float W[32];
  lds_wait_ge<0>(lds, L_FULL + 4 * (143 % NS), 144u);
  {
    const LAS unsigned short* wp = (const LAS unsigned short*)(lds + L_RING + (140 % NS) * SLOT) + 64 * w + lane;
#pragma unroll
    for (int r = 0; r < 32; ++r) W[r] = u2f((unsigned)wp[r * 512] << 16);
#pragma unroll
    for (int r = 0; r < 32; r += 8) asm volatile("" : "+v"(W[r]), "+v"(W[r + 1]), "+v"(W[r + 2]), "+v"(W[r + 3]), "+v"(W[r + 4]), "+v"(W[r + 5]), "+v"(W[r + 6]), "+v"(W[r + 7]));
  }
  if (w >= 4) {
    bool ok = tags4(av, tA, tB);
    for (unsigned spin = 0; spin < SPIN_CAP && !__all(ok); ++spin) {
      MEMFENCE();
      if (!ok) { av = __builtin_amdgcn_raw_buffer_load_b128(rs, offA, 0, AUX_SC1); ok = tags4(av, tA, tB); }
      if (PSLEEP) __builtin_amdgcn_s_sleep(PSLEEP);
    }
    if (w == 4) WSTAMP(5);
    const f32x4 o = red8_16_32(unpack4(av));
    if (lane < 8) {
#pragma unroll
      for (int e = 0; e < 4; ++e) *(LAS float*)(lds + L_H1 + ((4 * lane + e) * 4 + (w & 3)) * 4) = o[e];
    }
  }
  LDS_BARRIER();
  {
    const f32x4 ra = *(const LAS f32x4*)(lds + L_H1 + i16 * 16), rb = *(const LAS f32x4*)(lds + L_H1 + (16 + i16) * 16);
    const unsigned ob = cvtpk((ra[0] + ra[1]) + (ra[2] + ra[3]), (rb[0] + rb[1]) + (rb[2] + rb[3]));
    const float oa = lo16(ob), oc = hi16(ob);
    float y0 = 0.f, y1 = 0.f, y2 = 0.f, y3 = 0.f;
#define FM(acc, o, wv, n) "v_fmac_f32_dpp " acc ", " o ", " wv " row_newbcast:" #n " row_mask:0xf bank_mask:0xf\n\t"
#define ROWS(n0, n1, n2, n3) asm volatile("s_nop 1\n\t" \
      FM("%0", "%4", "%6", n0) FM("%1", "%5", "%7", n0) FM("%2", "%4", "%8", n1) FM("%3", "%5", "%9", n1) \
      FM("%0", "%4", "%10", n2) FM("%1", "%5", "%11", n2) FM("%2", "%4", "%12", n3) FM("%3", "%5", "%13", n3) \
      : "+v"(y0), "+v"(y1), "+v"(y2), "+v"(y3) \
      : "v"(oa), "v"(oc), "v"(W[n0]), "v"(W[16 + n0]), "v"(W[n1]), "v"(W[16 + n1]), "v"(W[n2]), "v"(W[16 + n2]), "v"(W[n3]), "v"(W[16 + n3]));
    ROWS(0, 1, 2, 3) ROWS(4, 5, 6, 7) ROWS(8, 9, 10, 11) ROWS(12, 13, 14, 15)
#undef ROWS
#undef FM
    const float y = (y0 + y1) + (y2 + y3);
    const f32x4 y4 = {y, dpp<0x55>(y), dpp<0xAA>(y), dpp<0xFF>(y)};
    const int c = (j >> 4) * 16 + 2 * w + (lane >> 5), k = (lane & 31) >> 2, pidx = h * 16 + (j & 15);
    if (jj == 0) __builtin_amdgcn_raw_buffer_store_b128(pack4(y4, tA, tB), rs, WS_I2 + ((c * 128 + pidx) * 8 + k) * 16, 0, AUX_SC1);
  }
  STAMP(6);
  if (tid == 0) { volatile LAS unsigned long long* q_ = (volatile LAS unsigned long long*)(lds + L_END - 128);
    __hip_atomic_store((GAS unsigned long long*)(a.ws + WS_STAMP) + b, q_[AMPS], __ATOMIC_RELAXED, __HIP_MEMORY_SCOPE_AGENT);
    __hip_atomic_store((GAS unsigned long long*)(a.ws + WS_STAMP) + 256 + b, t_start, __ATOMIC_RELAXED, __HIP_MEMORY_SCOPE_AGENT); }

  if (j < 4) {
    const int c = h * 4 + j;
    const unsigned off = WS_I2 + (c * 128 + 16 * w) * 128 + lane * 16;
    u32x4 v[2] = {{0u, 0u, 0u, 0u}, {0u, 0u, 0u, 0u}};
    bool ok = false;
    for (unsigned spin = 0; spin < SPIN_CAP; ++spin) {
      MEMFENCE();
      if (!ok) {
#pragma unroll
        for (int i = 0; i < 2; ++i) v[i] = __builtin_amdgcn_raw_buffer_load_b128(rs, off + i * 1024, 0, AUX_SC1);
        ok = tags4(v[0], tA, tB) && tags4(v[1], tA, tB);
      }
      if (__all(ok)) break;
      if (PSLEEP) __builtin_amdgcn_s_sleep(PSLEEP);
    }
    STAMP(7);
    const f32x4 s4 = red8_16_32(unpack4(v[0]) + unpack4(v[1]));
    if (lane < 8) {
#pragma unroll
      for (int e = 0; e < 4; ++e) *(LAS float*)(lds + L_F + ((4 * lane + e) * 8 + w) * 4) = s4[e];
    }
    LDS_BARRIER();
    if (tid < 32) {
      const f32x4 p0 = *(const LAS f32x4*)(lds + L_F + tid * 32), p1 = *(const LAS f32x4*)(lds + L_F + tid * 32 + 16);
      const float s = ((p0[0] + p0[1]) + (p0[2] + p0[3])) + ((p1[0] + p1[1]) + (p1[2] + p1[3]));
      const unsigned pk = cvtpk(s, dpp<XOR1>(s));
      if ((tid & 1) == 0) *(GAS unsigned*)((GAS unsigned char*)a.out + (c * 32 + tid) * 2) = pk;
      if (c == 0 && tid == 0) __builtin_amdgcn_raw_buffer_store_b64(u32x2{tagA, tagB}, rs, WS_GEN, 0, AUX_SC1);
    }
  }
  DUMP_STAMPS();
}

extern "C" void kernel_launch(void* const* d_in, const int* in_sizes, int n_in, void* d_out, int out_size, void* d_ws, size_t ws_size, hipStream_t stream) {
  Args a;
  a.xA = (const unsigned char*)d_in[0]; a.kA = (const unsigned char*)d_in[1]; a.vA = (const unsigned char*)d_in[2]; a.wqA = (const unsigned char*)d_in[3]; a.woA = (const unsigned char*)d_in[4];
  a.xB = (const unsigned char*)d_in[5]; a.kB = (const unsigned char*)d_in[6]; a.vB = (const unsigned char*)d_in[7]; a.wqB = (const unsigned char*)d_in[8]; a.woB = (const unsigned char*)d_in[9];
  a.out = (unsigned short*)d_out;
  a.ws = (unsigned char*)d_ws;
  hipLaunchKernelGGL(fused_decode, dim3(NWG), dim3(NTHR), 0, stream, a);
}
```

```cpp
#include <hip/hip_runtime.h>
#include <hip/hip_bf16.h>
#include <stdint.h>

#define GAS __attribute__((address_space(1)))
#define LAS __attribute__((address_space(3)))
typedef unsigned u32x4 __attribute__((ext_vector_type(4)));
typedef unsigned u32x2 __attribute__((ext_vector_type(2)));
typedef float f32x4 __attribute__((ext_vector_type(4)));
typedef float f32x2 __attribute__((ext_vector_type(2)));
typedef __bf16 bf16x8 __attribute__((ext_vector_type(8)));
typedef __bf16 bf16x2 __attribute__((ext_vector_type(2)));
typedef short s16x4 __attribute__((ext_vector_type(4)));

#define NS 18
#define WIN_LD 21
#define WSTART 40
#define PRE 3
#define QSLEEP 16
#define POLB 6
#define STAMPS 0

constexpr int SK = 65536, WIN = 4096;
constexpr int NWG = 256, NC = 8, NTHR = (NC + 1) * 64;
constexpr int SLOT = 8192, NSEQ = 4 + 8 + 64 + 64 + 4;
constexpr int LAG = (WIN_LD + 7) / 8;
constexpr int LAG2 = (WIN_LD + 8) / 8;
constexpr float LOG2E = 1.4426950408889634f;
constexpr float SCALE = 0.08838834764831845f;
constexpr unsigned SPIN_CAP = 1u << 20, LSPIN_CAP = 1u << 24;

constexpr unsigned WS_GEN = 0;
constexpr unsigned WS_QG = 4096;
constexpr unsigned WS_ML = WS_QG + 8 * 32 * 32 * 16;
constexpr unsigned WS_I1 = WS_ML + 256 * 128;
constexpr unsigned WS_I2 = WS_I1 + 8 * 32 * 32 * 128;
constexpr unsigned WS_END = WS_I2 + 32 * 128 * 128;
constexpr unsigned WS_STAMP = WS_END;

constexpr int L_RING = 0;
constexpr int L_FULL = NS * SLOT;
constexpr int L_FREE = L_FULL + 128;
constexpr int L_CNT = L_FREE + 128;
constexpr int L_RED = L_CNT + 64;
constexpr int L_Q = L_RED + 2048;
constexpr int L_T1 = L_Q + 2048;
constexpr int L_T2 = L_T1 + 128;
constexpr int L_TB = L_T2 + 128;
constexpr int L_FF = L_TB + 256;
constexpr int L_H1 = L_FF + 64;
constexpr int L_F = L_H1 + 512;
constexpr int L_END = L_F + 1024 + 128;
constexpr int C_QRED = 0, C_QIN = 4, C_MAX = 8, C_SUM = 12, C_FF = 16, C_MRG = 20;
constexpr int MREC = 132;
constexpr int L_MRG = 0;
constexpr int MRG_SLOTS = 5;
static_assert(L_END <= 160 * 1024, "LDS");
static_assert(2 * 8 * 4 * MREC * 4 <= MRG_SLOTS * SLOT, "merge area");
static_assert((140 % NS) >= MRG_SLOTS && (143 % NS) >= MRG_SLOTS && (140 % NS) <= (143 % NS), "wo slots contiguous and clear of the merge area");
static_assert((132 % NS) >= MRG_SLOTS && (139 % NS) >= MRG_SLOTS && (132 % NS) <= (139 % NS), "last round clear of the merge area");
static_assert(WIN_LD >= 9 && WIN_LD <= 48, "window");

__device__ __forceinline__ unsigned cvtpk(float lo, float hi) { f32x2 f = {lo, hi}; return __builtin_bit_cast(unsigned, __builtin_convertvector(f, bf16x2)); }
__device__ __forceinline__ float lo16(unsigned u) { return __builtin_bit_cast(float, u << 16); }
__device__ __forceinline__ float hi16(unsigned u) { return __builtin_bit_cast(float, u & 0xffff0000u); }
__device__ __forceinline__ float u2f(unsigned u) { return __builtin_bit_cast(float, u); }
__device__ __forceinline__ unsigned f2u(float f) { return __builtin_bit_cast(unsigned, f); }
template <int CTRL> __device__ __forceinline__ float dpp(float x) { return u2f(__builtin_amdgcn_mov_dpp(f2u(x), CTRL, 0xf, 0xf, true)); }
constexpr int XOR1 = 0xB1, XOR2 = 0x4E, HMIR = 0x141;
constexpr int ROR1 = 0x121, ROR2 = 0x122, ROR4 = 0x124, ROR8 = 0x128;
__device__ __forceinline__ float swap16_max(float x) { auto s = __builtin_amdgcn_permlane16_swap(f2u(x), f2u(x), false, false); return fmaxf(u2f(s[0]), u2f(s[1])); }
__device__ __forceinline__ float swap32_max(float x) { auto s = __builtin_amdgcn_permlane32_swap(f2u(x), f2u(x), false, false); return fmaxf(u2f(s[0]), u2f(s[1])); }
__device__ __forceinline__ float swap16_sum(float x) { auto s = __builtin_amdgcn_permlane16_swap(f2u(x), f2u(x), false, false); return u2f(s[0]) + u2f(s[1]); }
__device__ __forceinline__ float swap32_sum(float x) { auto s = __builtin_amdgcn_permlane32_swap(f2u(x), f2u(x), false, false); return u2f(s[0]) + u2f(s[1]); }
__device__ __forceinline__ float row_sum(float x) { x += dpp<ROR8>(x); x += dpp<ROR4>(x); x += dpp<ROR2>(x); x += dpp<ROR1>(x); return x; }
__device__ __forceinline__ float row_max(float x) { x = fmaxf(x, dpp<ROR8>(x)); x = fmaxf(x, dpp<ROR4>(x)); x = fmaxf(x, dpp<ROR2>(x)); x = fmaxf(x, dpp<ROR1>(x)); return x; }
template <int N> __device__ __forceinline__ float bcast(float x) { return dpp<0x150 + N>(x); }
__device__ __forceinline__ float oct_sum(float x) { x += dpp<XOR1>(x); x += dpp<XOR2>(x); x += dpp<HMIR>(x); return x; }
__device__ __forceinline__ float oct_max(float x) { x = fmaxf(x, dpp<XOR1>(x)); x = fmaxf(x, dpp<XOR2>(x)); x = fmaxf(x, dpp<HMIR>(x)); return x; }
#define LDS_BARRIER() asm volatile("s_waitcnt lgkmcnt(0)\n\ts_barrier" ::: "memory")
#define LDS_WAIT() asm volatile("s_waitcnt lgkmcnt(0)" ::: "memory")
#define MEMFENCE() asm volatile("" ::: "memory")
constexpr int AUX_SC1 = 16;

__device__ __forceinline__ u32x4 pack4(f32x4 v, unsigned tA, unsigned tB) {
  const unsigned t0 = f2u(v[0]) + 0x800u, t1 = f2u(v[1]) + 0x800u, t2 = f2u(v[2]) + 0x800u, t3 = f2u(v[3]) + 0x800u;
  return u32x4{tA | ((t0 << 12) & 0xff000000u), (t0 >> 20) | (t1 & 0xfffff000u), (t2 >> 12) | ((t3 << 8) & 0xfff00000u), (t3 >> 24) | (tB << 8)};
}
__device__ __forceinline__ f32x4 unpack4(u32x4 d) {
  return f32x4{u2f((d[1] << 20) | ((d[0] >> 12) & 0xff000u)), u2f(d[1] & 0xfffff000u), u2f(d[2] << 12), u2f(((d[2] >> 8) & 0xfff000u) | (d[3] << 24))};
}
__device__ __forceinline__ bool tags4(u32x4 d, unsigned tA, unsigned tB) { return (d[0] & 0xffffffu) == tA && (d[3] >> 8) == tB; }
__device__ __forceinline__ f32x4 red8_16_32(f32x4 v) {
#pragma unroll
  for (int e = 0; e < 4; ++e) { float x = v[e]; x += dpp<ROR8>(x); v[e] = swap32_sum(swap16_sum(x)); }
  return v;
}

struct Args {
  const unsigned char *xA, *kA, *vA, *wqA, *woA, *xB, *kB, *vB, *wqB, *woB;
  unsigned short* out;
  unsigned char* ws;
};

__device__ __forceinline__ unsigned lds_ld(LAS unsigned char* L, int off) { return __builtin_amdgcn_readfirstlane(*(volatile LAS unsigned*)(L + off)); }
__device__ __forceinline__ void lds_st(LAS unsigned char* L, int off, unsigned v) { *(volatile LAS unsigned*)(L + off) = v; }
template <int SLEEP> __device__ __forceinline__ void lds_wait_ge(LAS unsigned char* L, int off, unsigned need) {
  for (unsigned spin = 0; lds_ld(L, off) < need && spin < LSPIN_CAP; ++spin) { if (SLEEP) __builtin_amdgcn_s_sleep(SLEEP); }
  MEMFENCE();
}
__device__ __forceinline__ void lds_arrive(LAS unsigned char* L, int off, int lane) {
  LDS_WAIT();
  if (lane == 0) __hip_atomic_fetch_add((LAS unsigned*)(L + off), 1u, __ATOMIC_RELAXED, __HIP_MEMORY_SCOPE_WORKGROUP);
}

template <int POL> __device__ __forceinline__ void dma1p(unsigned dst, unsigned voff, const unsigned char* base) {
  unsigned keep;
  if constexpr (POL == 0)
  asm volatile("s_mov_b32 %0, m0\n\ts_mov_b32 m0, %1\n\ts_nop 0\n\tglobal_load_lds_dwordx4 %2, %3 nt\n\ts_mov_b32 m0, %0\n\ts_waitcnt vmcnt(%4)"
               : "=&s"(keep) : "s"(dst), "v"(voff), "s"(base), "i"(WSTART) : "memory");
  else
  asm volatile("s_mov_b32 %0, m0\n\ts_mov_b32 m0, %1\n\ts_nop 0\n\tglobal_load_lds_dwordx4 %2, %3\n\ts_mov_b32 m0, %0\n\ts_waitcnt vmcnt(%4)"
               : "=&s"(keep) : "s"(dst), "v"(voff), "s"(base), "i"(WSTART) : "memory");
}
template <int POL> __device__ __forceinline__ void dma4p(unsigned dst, unsigned v0, unsigned v1, unsigned v2, unsigned v3, const unsigned char* base) {
  unsigned keep;
  if constexpr (POL != 0)
  asm volatile("s_mov_b32 %0, m0\n\ts_mov_b32 m0, %1\n\ts_nop 0\n\t"
               "global_load_lds_dwordx4 %2, %6\n\ts_waitcnt vmcnt(%7)\n\t"
               "global_load_lds_dwordx4 %3, %6 offset:1024\n\ts_waitcnt vmcnt(%7)\n\t"
               "global_load_lds_dwordx4 %4, %6 offset:2048\n\ts_waitcnt vmcnt(%7)\n\t"
               "global_load_lds_dwordx4 %5, %6 offset:3072\n\ts_waitcnt vmcnt(%7)\n\t"
               "s_mov_b32 m0, %0"
               : "=&s"(keep) : "s"(dst), "v"(v0), "v"(v1), "v"(v2), "v"(v3), "s"(base), "i"(WIN_LD) : "memory");
  else
  asm volatile("s_mov_b32 %0, m0\n\ts_mov_b32 m0, %1\n\ts_nop 0\n\t"
               "global_load_lds_dwordx4 %2, %6 nt\n\ts_waitcnt vmcnt(%7)\n\t"
               "global_load_lds_dwordx4 %3, %6 offset:1024 nt\n\ts_waitcnt vmcnt(%7)\n\t"
               "global_load_lds_dwordx4 %4, %6 offset:2048 nt\n\ts_waitcnt vmcnt(%7)\n\t"
               "global_load_lds_dwordx4 %5, %6 offset:3072 nt\n\ts_waitcnt vmcnt(%7)\n\t"
               "s_mov_b32 m0, %0"
               : "=&s"(keep) : "s"(dst), "v"(v0), "v"(v1), "v"(v2), "v"(v3), "s"(base), "i"(WIN_LD) : "memory");
}
__device__ __forceinline__ void dma4(unsigned dst, unsigned v0, unsigned v1, unsigned v2, unsigned v3, const unsigned char* base) { dma4p<0>(dst, v0, v1, v2, v3, base); }
template <int WL> __device__ __forceinline__ void dma4w(unsigned dst, unsigned v0, unsigned v1, unsigned v2, unsigned v3, const unsigned char* base) {
  unsigned keep;
  asm volatile("s_mov_b32 %0, m0\n\ts_mov_b32 m0, %1\n\ts_waitcnt vmcnt(%7)\n\t"
               "global_load_lds_dwordx4 %2, %6 nt\n\ts_waitcnt vmcnt(%7)\n\t"
               "global_load_lds_dwordx4 %3, %6 offset:1024 nt\n\ts_waitcnt vmcnt(%7)\n\t"
               "global_load_lds_dwordx4 %4, %6 offset:2048 nt\n\ts_waitcnt vmcnt(%7)\n\t"
               "global_load_lds_dwordx4 %5, %6 offset:3072 nt\n\t"
               "s_mov_b32 m0, %0"
               : "=&s"(keep) : "s"(dst), "v"(v0), "v"(v1), "v"(v2), "v"(v3), "s"(base), "i"(WL) : "memory");
}
template <int N> __device__ __forceinline__ void vm_wait() { asm volatile("s_waitcnt vmcnt(%0)" :: "i"(N) : "memory"); }

__device__ __forceinline__ u32x2 tile_scores(const u32x4* K, const u32x4 (&q)[4]) {
  f32x4 s = {0.f, 0.f, 0.f, 0.f};
#pragma unroll
  for (int i = 0; i < 4; ++i) s = __builtin_amdgcn_mfma_f32_16x16x32_bf16(__builtin_bit_cast(bf16x8, K[i]), __builtin_bit_cast(bf16x8, q[i]), s, 0, 0, 0);
  const unsigned r01 = cvtpk(s[0], s[1]), r23 = cvtpk(s[2], s[3]);
  return u32x2{cvtpk(lo16(r01) * SCALE, hi16(r01) * SCALE), cvtpk(lo16(r23) * SCALE, hi16(r23) * SCALE)};
}
__device__ __forceinline__ float max4(u32x2 t) { return fmaxf(fmaxf(lo16(t[0]), hi16(t[0])), fmaxf(lo16(t[1]), hi16(t[1]))); }
__device__ __forceinline__ u32x2 to_probs(u32x2 t, float m2, float& l) {
  const float p0 = __builtin_amdgcn_exp2f(fmaf(lo16(t[0]), LOG2E, -m2)), p1 = __builtin_amdgcn_exp2f(fmaf(hi16(t[0]), LOG2E, -m2));
  const float p2 = __builtin_amdgcn_exp2f(fmaf(lo16(t[1]), LOG2E, -m2)), p3 = __builtin_amdgcn_exp2f(fmaf(hi16(t[1]), LOG2E, -m2));
  l += (p0 + p1) + (p2 + p3);
  return u32x2{cvtpk(p0, p1), cvtpk(p2, p3)};
}
struct Sel { unsigned l0, l1, h0, h1; };
__device__ __forceinline__ void tile_pv(f32x4& a0, f32x4& a1, const u32x4* V, u32x2 p, const Sel& s) {
#pragma unroll
  for (int r = 0; r < 4; ++r) {
    const unsigned pk = p[r >> 1];
    const u32x2 a = {__builtin_amdgcn_perm(0u, pk, (r & 1) ? s.h0 : s.l0), __builtin_amdgcn_perm(0u, pk, (r & 1) ? s.h1 : s.l1)};
    const u32x2 v0 = {V[r][0], V[r][1]}, v1 = {V[r][2], V[r][3]};
    a0 = __builtin_amdgcn_mfma_f32_16x16x16bf16_1k(__builtin_bit_cast(s16x4, a), __builtin_bit_cast(s16x4, v0), a0, 0, 0, 0);
    a1 = __builtin_amdgcn_mfma_f32_16x16x16bf16_1k(__builtin_bit_cast(s16x4, a), __builtin_bit_cast(s16x4, v1), a1, 0, 0, 0);
  }
}

#define STAMP(i) do {} while (0)
#define WSTAMP(i) do {} while (0)
#define DUMP_STAMPS() do {} while (0)

__global__ void __launch_bounds__(NTHR, 3) fused_decode(Args a) {
  __shared__ __attribute__((aligned(16))) unsigned char lds_raw[L_END];
  LAS unsigned char* lds = (LAS unsigned char*)lds_raw;
  const int b = blockIdx.x, h = b & 7, j = b >> 3;
  const int tid = threadIdx.x, lane = tid & 63, w = __builtin_amdgcn_readfirstlane(tid >> 6);
  const int hh = lane >> 4, i16 = lane & 15, jj = lane & 3;
  const int qcb = j >> 1, qrh = j & 1, qbr = qcb >> 3;

  if (w == NC) {
    __builtin_amdgcn_s_setprio(3);
    const unsigned ring = (unsigned)(size_t)(lds + L_RING);
    int pub = 0, pslot = 0, slot = 0;
    auto publish1 = [&]() { lds_st(lds, L_FULL + 4 * pslot, (unsigned)(pub + 1)); ++pub; pslot = pslot + 1 == NS ? 0 : pslot + 1; };
    auto pre = [&](int q) {
      if (q >= NS) {
        const unsigned need = (unsigned)(q - NS + 1);
        if (lds_ld(lds, L_FREE + 4 * slot) < need) {
          vm_wait<0>();
          while (pub < q) publish1();
          lds_wait_ge<1>(lds, L_FREE + 4 * slot, need);
        }
      }
    };
    auto post = [&](int q) {
      while (q - pub >= LAG) publish1();
      slot = slot + 1 == NS ? 0 : slot + 1;
    };
    {
      const unsigned char* wq = (qbr ? a.wqB : a.wqA) + (size_t)(h * 512 + 64 * (qcb & 7)) * 2 + (size_t)qrh * 256 * 8192;
      const unsigned vo = (lane >> 3) * 8192 + (lane & 7) * 16;
#pragma unroll
      for (int q = 0; q < 4; ++q) {
#pragma unroll
        for (int m = 0; m < 8; ++m) { dma1p<0>(ring + q * SLOT + m * 1024, vo, wq + (size_t)(q * 64 + m * 8) * 8192); }
        if (q == 0) LDS_BARRIER();
        while (q - pub >= (WSTART + 7) / 8) publish1();
        slot = slot + 1 == NS ? 0 : slot + 1;
      }
    }
    const unsigned k0 = hh * 256 + ((i16 ^ (0 + hh)) * 16), k1 = hh * 256 + ((i16 ^ (4 + hh)) * 16), k2 = hh * 256 + ((i16 ^ (8 + hh)) * 16), k3 = hh * 256 + ((i16 ^ (12 + hh)) * 16);
    const unsigned vl = lane * 16;
    {
      const unsigned char* kp = a.kB + ((size_t)h * SK + (SK - WIN) + j * 128) * 256;
      const unsigned char* vp = a.vB + ((size_t)h * SK + (SK - WIN) + j * 128) * 256;
      for (int q = 4; q < 12; ++q) {
        dma4p<(POLB & 2) ? 1 : 0>(ring + slot * SLOT, k0, k1, k2, k3, kp);
        dma4p<(POLB & 4) ? 1 : 0>(ring + slot * SLOT + 4096, vl, vl, vl, vl, vp);
        kp += 4096; vp += 4096;
        post(q);
      }
      kp = a.kA + ((size_t)h * SK + j * 2048) * 256;
      unsigned o0 = k0, o1 = k1, o2 = k2, o3 = k3;
      unsigned frv = *(volatile LAS unsigned*)(lds + L_FREE + 4 * slot);
      for (int q = 12; q < 140; ++q) {
        if (q == 76) { WSTAMP(12); kp = a.vA + ((size_t)h * SK + j * 2048) * 256; o0 = vl; o1 = vl; o2 = vl; o3 = vl; }
        if (q >= NS && (unsigned)__builtin_amdgcn_readfirstlane(frv) < (unsigned)(q - NS + 1)) pre(q);
        const int nslot = slot + 1 == NS ? 0 : slot + 1;
        frv = *(volatile LAS unsigned*)(lds + L_FREE + 4 * nslot);
        dma4w<WIN_LD>(ring + slot * SLOT, o0, o1, o2, o3, kp);
        dma4w<WIN_LD>(ring + slot * SLOT + 4096, o0, o1, o2, o3, kp + 4096);
        while (q - pub >= LAG2) publish1();
        kp += 8192;
        slot = nslot;
      }
      WSTAMP(9);
      vp = (j >> 4 ? a.woB : a.woA) + (size_t)(h * 512 + 32 * (j & 15)) * 1024;
      for (int q = 140; q < 144; ++q) {
        pre(q);
        dma4w<WIN_LD>(ring + slot * SLOT, vl, vl, vl, vl, vp); dma4w<WIN_LD>(ring + slot * SLOT + 4096, vl, vl, vl, vl, vp + 4096);
        vp += 8192;
        while (q - pub >= LAG2) publish1();
        slot = slot + 1 == NS ? 0 : slot + 1;
      }
    }
    if (NSEQ - pub > 7) { vm_wait<56>(); publish1(); }
    if (NSEQ - pub > 6) { vm_wait<48>(); publish1(); }
    if (NSEQ - pub > 5) { vm_wait<40>(); publish1(); }
    if (NSEQ - pub > 4) { vm_wait<32>(); publish1(); }
    __builtin_amdgcn_s_barrier();
    if (NSEQ - pub > 3) { vm_wait<24>(); publish1(); }
    if (NSEQ - pub > 2) { vm_wait<16>(); publish1(); }
    if (NSEQ - pub > 1) { vm_wait<8>(); publish1(); }
    vm_wait<0>();
    while (pub < NSEQ) publish1();
    WSTAMP(10);
    return;
  }

  const __amdgpu_buffer_rsrc_t rs = __builtin_amdgcn_make_buffer_rsrc((void*)a.ws, 0, (int)WS_END, 0x00020000);
  const u32x2 gen = __builtin_amdgcn_raw_buffer_load_b64(rs, WS_GEN, 0, AUX_SC1);
  unsigned short xv[4];
  {
    const GAS unsigned char* xp = (const GAS unsigned char*)(qbr ? a.xB : a.xA) + 2 * (qrh * 256 + w * 32 + (lane >> 3));
#pragma unroll
    for (int i = 0; i < 4; ++i) xv[i] = *(const GAS unsigned short*)(xp + 16 * i);
  }
  if (tid < (L_RED - L_FULL) / 4) *(LAS unsigned*)(lds + L_FULL + 4 * tid) = 0u;
  LDS_BARRIER();

  lds_wait_ge<1>(lds, L_FULL + 4 * (w >> 1), (unsigned)((w >> 1) + 1));
  {
    u32x4 wv[4];
#pragma unroll
    for (int i = 0; i < 4; ++i) wv[i] = *(const LAS u32x4*)(lds + L_RING + (w >> 1) * SLOT + (w & 1) * 4096 + i * 1024 + lane * 16);
    float acc[8];
#pragma unroll
    for (int k = 0; k < 8; ++k) acc[k] = 0.f;
#pragma unroll
    for (int i = 0; i < 4; ++i) {
      const float x = lo16(xv[i]);
#pragma unroll
      for (int k = 0; k < 4; ++k) { acc[2 * k] = fmaf(x, lo16(wv[i][k]), acc[2 * k]); acc[2 * k + 1] = fmaf(x, hi16(wv[i][k]), acc[2 * k + 1]); }
    }
#pragma unroll
    for (int k = 0; k < 8; ++k) { float v = acc[k]; v += dpp<ROR8>(v); v = swap16_sum(v); v = swap32_sum(v); acc[k] = v; }
    if (lane < 8) {
      LAS f32x4* red = (LAS f32x4*)(lds + L_RED + w * 256 + lane * 32);
      red[0] = f32x4{acc[0], acc[1], acc[2], acc[3]}; red[1] = f32x4{acc[4], acc[5], acc[6], acc[7]};
    }
  }
  lds_arrive(lds, L_CNT + C_QRED, lane);
  const unsigned tagA = __builtin_amdgcn_readfirstlane(gen[0]) + 1u, tagB = __builtin_amdgcn_readfirstlane(gen[1]) + 1u;
  if (w == 0) {
    lds_wait_ge<0>(lds, L_CNT + C_QRED, NC);
    if (lane < 4) lds_st(lds, L_FREE + 4 * lane, (unsigned)(lane + 1));
    if (lane < 32) {
      f32x2 s = {0.f, 0.f};
#pragma unroll
      for (int ww = 0; ww < 8; ++ww) s += *(const LAS f32x2*)(lds + L_RED + ww * 256 + lane * 8);
      __builtin_amdgcn_raw_buffer_store_b128(u32x4{tagA, f2u(s[0]), f2u(s[1]), tagB}, rs, WS_QG + ((h * 32 + j) * 32 + lane) * 16, 0, AUX_SC1);
    }
  }
  STAMP(1);
  int slot = (4 + w) % NS;
  const unsigned kr0 = i16 * 256 + (((0 + hh) ^ i16) * 16), kr1 = i16 * 256 + (((4 + hh) ^ i16) * 16), kr2 = i16 * 256 + (((8 + hh) ^ i16) * 16), kr3 = i16 * 256 + (((12 + hh) ^ i16) * 16);
  const unsigned vr = hh * 1024 + i16 * 16;
  auto fetch = [&](int it, int kind, u32x4 (&T)[8]) {
    const unsigned seq1 = (unsigned)(4 + 8 * it + w + 1);
    lds_wait_ge<1>(lds, L_FULL + 4 * slot, seq1);
    const LAS unsigned char* sp = lds + L_RING + slot * SLOT;
    if (kind != 2) { T[0] = *(const LAS u32x4*)(sp + kr0); T[1] = *(const LAS u32x4*)(sp + kr1); T[2] = *(const LAS u32x4*)(sp + kr2); T[3] = *(const LAS u32x4*)(sp + kr3); }
    else {
#pragma unroll
      for (int r = 0; r < 4; ++r) T[r] = *(const LAS u32x4*)(sp + vr + r * 256);
    }
    if (kind == 1) { T[4] = *(const LAS u32x4*)(sp + 4096 + kr0); T[5] = *(const LAS u32x4*)(sp + 4096 + kr1); T[6] = *(const LAS u32x4*)(sp + 4096 + kr2); T[7] = *(const LAS u32x4*)(sp + 4096 + kr3); }
    else {
#pragma unroll
      for (int r = 0; r < 4; ++r) T[4 + r] = *(const LAS u32x4*)(sp + 4096 + vr + r * 256);
    }
    LDS_WAIT();
    lds_st(lds, L_FREE + 4 * slot, seq1);
    asm volatile("" : "+v"(T[0]), "+v"(T[1]), "+v"(T[2]), "+v"(T[3]), "+v"(T[4]), "+v"(T[5]), "+v"(T[6]), "+v"(T[7]) :: "memory");
    slot += 8; if (slot >= NS) slot -= NS;
  };
  u32x4 X0[8], X1[8], X2[8];
  fetch(0, 0, X0);
  if (PRE >= 2) fetch(1, 1, X1);
  if (PRE >= 3) fetch(2, 1, X2);
  {
    const unsigned off = WS_QG + ((h * 32 + 2 * (tid >> 5)) * 32 + (tid & 31)) * 16;
    u32x4 g0 = {0u, 0u, 0u, 0u}, g1 = {0u, 0u, 0u, 0u};
    bool ok = false;
    for (unsigned spin = 0; spin < SPIN_CAP; ++spin) {
      MEMFENCE();
      if (!ok) {
        g0 = __builtin_amdgcn_raw_buffer_load_b128(rs, off, 0, AUX_SC1);
        g1 = __builtin_amdgcn_raw_buffer_load_b128(rs, off + 512, 0, AUX_SC1);
        ok = g0[0] == tagA && g0[3] == tagB && g1[0] == tagA && g1[3] == tagB;
      }
      if (__all(ok)) break;
      if (QSLEEP) __builtin_amdgcn_s_sleep(QSLEEP);
    }
    *(LAS unsigned*)(lds + L_Q + tid * 4) = cvtpk(u2f(g0[1]) + u2f(g1[1]), u2f(g0[2]) + u2f(g1[2]));
  }
  lds_arrive(lds, L_CNT + C_QIN, lane);
  lds_wait_ge<0>(lds, L_CNT + C_QIN, NC);
  u32x4 qA[4], qB[4];
#pragma unroll
  for (int s = 0; s < 4; ++s) {
    qA[s] = *(const LAS u32x4*)(lds + L_Q + ((i16 >> 2) * 128 + 32 * s + 8 * hh) * 2);
    qB[s] = *(const LAS u32x4*)(lds + L_Q + 1024 + ((i16 >> 2) * 128 + 32 * s + 8 * hh) * 2);
  }
  STAMP(2);

  Sel sel;
  sel.l0 = jj == 0 ? 0x0c0c0100u : jj == 1 ? 0x01000c0cu : 0x0c0c0c0cu;  sel.h0 = jj == 0 ? 0x0c0c0302u : jj == 1 ? 0x03020c0cu : 0x0c0c0c0cu;
  sel.l1 = jj == 2 ? 0x0c0c0100u : jj == 3 ? 0x01000c0cu : 0x0c0c0c0cu;  sel.h1 = jj == 2 ? 0x0c0c0302u : jj == 3 ? 0x03020c0cu : 0x0c0c0c0cu;

  f32x4 b0 = {0.f, 0.f, 0.f, 0.f}, b1 = {0.f, 0.f, 0.f, 0.f};
  {
    const u32x2 t = tile_scores(X0, qB);
    const float m2 = swap32_max(swap16_max(max4(t))) * LOG2E;
    float l = 0.f;
    const u32x2 p = to_probs(t, m2, l);
    l = swap32_sum(swap16_sum(l));
    tile_pv(b0, b1, X0 + 4, p, sel);
    if (hh == 0 && jj == 0) *(LAS f32x2*)(lds + L_TB + ((i16 >> 2) * 8 + w) * 8) = f32x2{m2, l};
  }

  u32x2 P[16];
  float mx = -INFINITY;
#pragma unroll
  for (int i = 0; i < 8; ++i) {
    if (i == 0) { if (PRE < 2) fetch(1, 1, X1); }
    else if (i == 1) { if (PRE < 3) fetch(2, 1, X2); }
    else fetch(i + 1, 1, X1);
    const u32x4* T = i == 1 ? X2 : X1;
    P[2 * i] = tile_scores(T, qA);
    P[2 * i + 1] = tile_scores(T + 4, qA);
    mx = fmaxf(mx, fmaxf(max4(P[2 * i]), max4(P[2 * i + 1])));
  }
  STAMP(13);
  mx = swap32_max(swap16_max(mx));
  if (hh == 0 && jj == 0) *(LAS float*)(lds + L_T1 + ((i16 >> 2) * 8 + w) * 4) = mx;
  lds_arrive(lds, L_CNT + C_MAX, lane);
  lds_wait_ge<0>(lds, L_CNT + C_MAX, NC);
  {
    const f32x4 m0 = *(const LAS f32x4*)(lds + L_T1 + (i16 >> 2) * 32), m1 = *(const LAS f32x4*)(lds + L_T1 + (i16 >> 2) * 32 + 16);
    const float m2 = fmaxf(fmaxf(fmaxf(m0[0], m0[1]), fmaxf(m0[2], m0[3])), fmaxf(fmaxf(m1[0], m1[1]), fmaxf(m1[2], m1[3]))) * LOG2E;
    float l = 0.f;
#pragma unroll
    for (int t = 0; t < 16; ++t) P[t] = to_probs(P[t], m2, l);
    l = swap32_sum(swap16_sum(l));
    if (hh == 0 && jj == 0) *(LAS float*)(lds + L_T2 + ((i16 >> 2) * 8 + w) * 4) = l;
  }
  lds_arrive(lds, L_CNT + C_SUM, lane);
  lds_wait_ge<0>(lds, L_CNT + C_SUM, NC);
  const int mbr = w >> 2, mg = w & 3;
  {
    const int ww = lane & 7;
    float M, L;
    if (mbr == 0) {
      M = oct_max(*(const LAS float*)(lds + L_T1 + (mg * 8 + ww) * 4)) * LOG2E;
      L = oct_sum(*(const LAS float*)(lds + L_T2 + (mg * 8 + ww) * 4));
    } else {
      const f32x2 ml = *(const LAS f32x2*)(lds + L_TB + (mg * 8 + ww) * 8);
      M = oct_max(ml[0]);
      L = oct_sum(ml[1] * __builtin_amdgcn_exp2f(ml[0] - M));
    }
    if (lane == 0) __builtin_amdgcn_raw_buffer_store_b128(u32x4{tagA, f2u(M), f2u(L), tagB}, rs, WS_ML + (((h * 2 + mbr) * 4 + mg) * 32 + j) * 16, 0, AUX_SC1);
  }
  STAMP(14);

  f32x4 a0 = {0.f, 0.f, 0.f, 0.f}, a1 = {0.f, 0.f, 0.f, 0.f};
  float fA = 0.f, fB = 0.f;
#pragma unroll
  for (int i = 0; i < 8; ++i) {
    if (i == 4) {
      const unsigned off = WS_ML + (((h * 2 + mbr) * 4 + mg) * 32 + (lane & 31)) * 16;
      u32x4 mv = {0u, 0u, 0u, 0u};
      bool ok = false;
      for (unsigned spin = 0; spin < SPIN_CAP; ++spin) {
        MEMFENCE();
        if (!ok) { mv = __builtin_amdgcn_raw_buffer_load_b128(rs, off, 0, AUX_SC1); ok = mv[0] == tagA && mv[3] == tagB; }
        if (__all(ok)) break;
        __builtin_amdgcn_s_sleep(16);
      }
      const float M = swap16_max(row_max(u2f(mv[1])));
      const float L = swap16_sum(row_sum(u2f(mv[2]) * __builtin_amdgcn_exp2f(u2f(mv[1]) - M)));
      if (lane == 0) *(LAS f32x2*)(lds + L_FF + w * 8) = f32x2{M, 1.0f / L};
      lds_arrive(lds, L_CNT + C_FF, lane);
    }
    if (i == 7) {
      lds_wait_ge<0>(lds, L_CNT + C_FF, NC);
      const f32x4 m0 = *(const LAS f32x4*)(lds + L_T1 + hh * 32), m1 = *(const LAS f32x4*)(lds + L_T1 + hh * 32 + 16);
      const float m2 = fmaxf(fmaxf(fmaxf(m0[0], m0[1]), fmaxf(m0[2], m0[3])), fmaxf(fmaxf(m1[0], m1[1]), fmaxf(m1[2], m1[3]))) * LOG2E;
      const f32x2 FA = *(const LAS f32x2*)(lds + L_FF + hh * 8), FB = *(const LAS f32x2*)(lds + L_FF + (4 + hh) * 8);
      const float mB = *(const LAS float*)(lds + L_TB + (hh * 8 + w) * 8);
      fA = __builtin_amdgcn_exp2f(m2 - FA[0]) * FA[1];
      fB = __builtin_amdgcn_exp2f(mB - FB[0]) * FB[1];
      const int s = lane < MRG_SLOTS ? lane : 0;
      const unsigned need = (unsigned)(s + NS * ((139 - s) / NS) + 1);
      for (unsigned spin = 0; spin < LSPIN_CAP; ++spin) { MEMFENCE(); if (__all(*(volatile LAS unsigned*)(lds + L_FREE + 4 * s) >= need)) break; }
      MEMFENCE();
      LAS float* rB = (LAS float*)(lds + L_MRG) + ((8 + w) * 4 + hh) * MREC + 8 * i16;
      *(LAS f32x4*)(rB) = b0 * fB; *(LAS f32x4*)(rB + 4) = b1 * fB;
    }
    fetch(9 + i, 2, X1);
    tile_pv(a0, a1, X1, P[2 * i], sel);
    tile_pv(a0, a1, X1 + 4, P[2 * i + 1], sel);
  }
  STAMP(3);
  if (w == 7) WSTAMP(11);

  const unsigned tA = tagA & 0xffffffu, tB = tagB & 0xffffffu;
  {
    LAS float* rA = (LAS float*)(lds + L_MRG) + (w * 4 + hh) * MREC + 8 * i16;
    *(LAS f32x4*)(rA) = a0 * fA; *(LAS f32x4*)(rA + 4) = a1 * fA;
  }
  LDS_BARRIER();
  if (tid < 256) {
    const int br = tid >> 7, g = (tid >> 5) & 3, dq = tid & 31;
    const LAS float* mrg = (const LAS float*)(lds + L_MRG) + (br * 8 * 4 + g) * MREC + 4 * dq;
    f32x4 o4[8];
#pragma unroll
    for (int ww = 0; ww < 8; ++ww) o4[ww] = *(const LAS f32x4*)(mrg + ww * 4 * MREC);
    const f32x4 o = ((o4[0] + o4[1]) + (o4[2] + o4[3])) + ((o4[4] + o4[5]) + (o4[6] + o4[7]));
    const int jc = br * 16 + g * 4 + (dq >> 3), k = dq & 7;
    __builtin_amdgcn_raw_buffer_store_b128(pack4(o, tA, tB), rs, WS_I1 + (((h * 32 + jc) * 32 + j) * 8 + k) * 16, 0, AUX_SC1);
  }
  STAMP(4);

  const unsigned offA = WS_I1 + ((h * 32 + j) * 32 * 8 + (w & 3) * 64 + lane) * 16;
  u32x4 av = {0u, 0u, 0u, 0u};
  if (w >= 4) av = __builtin_amdgcn_raw_buffer_load_b128(rs, offA, 0, AUX_SC1);
  MEMFENCE();
  float W[32];
  lds_wait_ge<0>(lds, L_FULL + 4 * (143 % NS), 144u);
  {
    const LAS unsigned short* wp = (const LAS unsigned short*)(lds + L_RING + (140 % NS) * SLOT) + 64 * w + lane;
#pragma unroll
    for (int r = 0; r < 32; ++r) W[r] = u2f((unsigned)wp[r * 512] << 16);
#pragma unroll
    for (int r = 0; r < 32; r += 8) asm volatile("" : "+v"(W[r]), "+v"(W[r + 1]), "+v"(W[r + 2]), "+v"(W[r + 3]), "+v"(W[r + 4]), "+v"(W[r + 5]), "+v"(W[r + 6]), "+v"(W[r + 7]));
  }
  if (w >= 4) {
    bool ok = tags4(av, tA, tB);
    for (unsigned spin = 0; spin < SPIN_CAP && !__all(ok); ++spin) {
      MEMFENCE();
      if (!ok) { av = __builtin_amdgcn_raw_buffer_load_b128(rs, offA, 0, AUX_SC1); ok = tags4(av, tA, tB); }
    }
    if (w == 4) WSTAMP(5);
    const f32x4 o = red8_16_32(unpack4(av));
    if (lane < 8) {
#pragma unroll
      for (int e = 0; e < 4; ++e) *(LAS float*)(lds + L_H1 + ((4 * lane + e) * 4 + (w & 3)) * 4) = o[e];
    }
  }
  LDS_BARRIER();
  {
    const f32x4 ra = *(const LAS f32x4*)(lds + L_H1 + i16 * 16), rb = *(const LAS f32x4*)(lds + L_H1 + (16 + i16) * 16);
    const unsigned ob = cvtpk((ra[0] + ra[1]) + (ra[2] + ra[3]), (rb[0] + rb[1]) + (rb[2] + rb[3]));
    const float oa = lo16(ob), oc = hi16(ob);
    float y0 = 0.f, y1 = 0.f, y2 = 0.f, y3 = 0.f;
#define FM(acc, o, wv, n) "v_fmac_f32_dpp " acc ", " o ", " wv " row_newbcast:" #n " row_mask:0xf bank_mask:0xf\n\t"
#define ROWS(n0, n1, n2, n3) asm volatile("s_nop 1\n\t" \
      FM("%0", "%4", "%6", n0) FM("%1", "%5", "%7", n0) FM("%2", "%4", "%8", n1) FM("%3", "%5", "%9", n1) \
      FM("%0", "%4", "%10", n2) FM("%1", "%5", "%11", n2) FM("%2", "%4", "%12", n3) FM("%3", "%5", "%13", n3) \
      : "+v"(y0), "+v"(y1), "+v"(y2), "+v"(y3) \
      : "v"(oa), "v"(oc), "v"(W[n0]), "v"(W[16 + n0]), "v"(W[n1]), "v"(W[16 + n1]), "v"(W[n2]), "v"(W[16 + n2]), "v"(W[n3]), "v"(W[16 + n3]));
    ROWS(0, 1, 2, 3) ROWS(4, 5, 6, 7) ROWS(8, 9, 10, 11) ROWS(12, 13, 14, 15)
#undef ROWS
#undef FM
    const float y = (y0 + y1) + (y2 + y3);
    const f32x4 y4 = {y, dpp<0x55>(y), dpp<0xAA>(y), dpp<0xFF>(y)};
    const int c = (j >> 4) * 16 + 2 * w + (lane >> 5), k = (lane & 31) >> 2, pidx = h * 16 + (j & 15);
    if (jj == 0) __builtin_amdgcn_raw_buffer_store_b128(pack4(y4, tA, tB), rs, WS_I2 + ((c * 128 + pidx) * 8 + k) * 16, 0, AUX_SC1);
  }
  STAMP(6);

  if (j < 4) {
    const int c = h * 4 + j;
    const unsigned off = WS_I2 + (c * 128 + 16 * w) * 128 + lane * 16;
    u32x4 v[2] = {{0u, 0u, 0u, 0u}, {0u, 0u, 0u, 0u}};
    bool ok = false;
    for (unsigned spin = 0; spin < SPIN_CAP; ++spin) {
      MEMFENCE();
      if (!ok) {
#pragma unroll
        for (int i = 0; i < 2; ++i) v[i] = __builtin_amdgcn_raw_buffer_load_b128(rs, off + i * 1024, 0, AUX_SC1);
        ok = tags4(v[0], tA, tB) && tags4(v[1], tA, tB);
      }
      if (__all(ok)) break;
    }
    STAMP(7);
    const f32x4 s4 = red8_16_32(unpack4(v[0]) + unpack4(v[1]));
    if (lane < 8) {
#pragma unroll
      for (int e = 0; e < 4; ++e) *(LAS float*)(lds + L_F + ((4 * lane + e) * 8 + w) * 4) = s4[e];
    }
    LDS_BARRIER();
    if (tid < 32) {
      const f32x4 p0 = *(const LAS f32x4*)(lds + L_F + tid * 32), p1 = *(const LAS f32x4*)(lds + L_F + tid * 32 + 16);
      const float s = ((p0[0] + p0[1]) + (p0[2] + p0[3])) + ((p1[0] + p1[1]) + (p1[2] + p1[3]));
      const unsigned pk = cvtpk(s, dpp<XOR1>(s));
      if ((tid & 1) == 0) *(GAS unsigned*)((GAS unsigned char*)a.out + (c * 32 + tid) * 2) = pk;
      if (c == 0 && tid == 0) __builtin_amdgcn_raw_buffer_store_b64(u32x2{tagA, tagB}, rs, WS_GEN, 0, AUX_SC1);
    }
  }
  DUMP_STAMPS();
}

extern "C" void kernel_launch(void* const* d_in, const int* in_sizes, int n_in, void* d_out, int out_size, void* d_ws, size_t ws_size, hipStream_t stream) {
  Args a;
  a.xA = (const unsigned char*)d_in[0]; a.kA = (const unsigned char*)d_in[1]; a.vA = (const unsigned char*)d_in[2]; a.wqA = (const unsigned char*)d_in[3]; a.woA = (const unsigned char*)d_in[4];
  a.xB = (const unsigned char*)d_in[5]; a.kB = (const unsigned char*)d_in[6]; a.vB = (const unsigned char*)d_in[7]; a.wqB = (const unsigned char*)d_in[8]; a.woB = (const unsigned char*)d_in[9];
  a.out = (unsigned short*)d_out;
  a.ws = (unsigned char*)d_ws;
  hipLaunchKernelGGL(fused_decode, dim3(NWG), dim3(NTHR), 0, stream, a);
}
```

```cpp
#include <hip/hip_runtime.h>
#include <hip/hip_bf16.h>
#include <stdint.h>

#define GAS __attribute__((address_space(1)))
#define LAS __attribute__((address_space(3)))
typedef unsigned u32x4 __attribute__((ext_vector_type(4)));
typedef unsigned u32x2 __attribute__((ext_vector_type(2)));
typedef float f32x4 __attribute__((ext_vector_type(4)));
typedef float f32x2 __attribute__((ext_vector_type(2)));
typedef __bf16 bf16x8 __attribute__((ext_vector_type(8)));
typedef __bf16 bf16x2 __attribute__((ext_vector_type(2)));
typedef short s16x4 __attribute__((ext_vector_type(4)));

#define AMPI -1
#define AMPX 1ull
#define NS 18
#define WIN_LD 21
#define WSTART 40
#define PRE 3
#define QSLEEP 16
#define POLB 6
#define STAMPS 1

constexpr int SK = 65536, WIN = 4096;
constexpr int NWG = 256, NC = 8, NTHR = (NC + 1) * 64;
constexpr int SLOT = 8192, NSEQ = 4 + 8 + 64 + 64 + 4;
constexpr int LAG = (WIN_LD + 7) / 8;
constexpr int LAG2 = (WIN_LD + 8) / 8;
constexpr float LOG2E = 1.4426950408889634f;
constexpr float SCALE = 0.08838834764831845f;
constexpr unsigned SPIN_CAP = 1u << 20, LSPIN_CAP = 1u << 24;

constexpr unsigned WS_GEN = 0;
constexpr unsigned WS_QG = 4096;
constexpr unsigned WS_ML = WS_QG + 8 * 32 * 32 * 16;
constexpr unsigned WS_I1 = WS_ML + 256 * 128;
constexpr unsigned WS_I2 = WS_I1 + 8 * 32 * 32 * 128;
constexpr unsigned WS_END = WS_I2 + 32 * 128 * 128;
constexpr unsigned WS_STAMP = WS_END;

constexpr int L_RING = 0;
constexpr int L_FULL = NS * SLOT;
constexpr int L_FREE = L_FULL + 128;
constexpr int L_CNT = L_FREE + 128;
constexpr int L_RED = L_CNT + 64;
constexpr int L_Q = L_RED + 2048;
constexpr int L_T1 = L_Q + 2048;
constexpr int L_T2 = L_T1 + 128;
constexpr int L_TB = L_T2 + 128;
constexpr int L_FF = L_TB + 256;
constexpr int L_H1 = L_FF + 64;
constexpr int L_F = L_H1 + 512;
constexpr int L_END = L_F + 1024 + 128;
constexpr int C_QRED = 0, C_QIN = 4, C_MAX = 8, C_SUM = 12, C_FF = 16, C_MRG = 20;
constexpr int MREC = 132;
constexpr int L_MRG = 0;
constexpr int MRG_SLOTS = 5;
static_assert(L_END <= 160 * 1024, "LDS");
static_assert(2 * 8 * 4 * MREC * 4 <= MRG_SLOTS * SLOT, "merge area");
static_assert((140 % NS) >= MRG_SLOTS && (143 % NS) >= MRG_SLOTS && (140 % NS) <= (143 % NS), "wo slots contiguous and clear of the merge area");
static_assert((132 % NS) >= MRG_SLOTS && (139 % NS) >= MRG_SLOTS && (132 % NS) <= (139 % NS), "last round clear of the merge area");
static_assert(WIN_LD >= 9 && WIN_LD <= 48, "window");

__device__ __forceinline__ unsigned cvtpk(float lo, float hi) { f32x2 f = {lo, hi}; return __builtin_bit_cast(unsigned, __builtin_convertvector(f, bf16x2)); }
__device__ __forceinline__ float lo16(unsigned u) { return __builtin_bit_cast(float, u << 16); }
__device__ __forceinline__ float hi16(unsigned u) { return __builtin_bit_cast(float, u & 0xffff0000u); }
__device__ __forceinline__ float u2f(unsigned u) { return __builtin_bit_cast(float, u); }
__device__ __forceinline__ unsigned f2u(float f) { return __builtin_bit_cast(unsigned, f); }
template <int CTRL> __device__ __forceinline__ float dpp(float x) { return u2f(__builtin_amdgcn_mov_dpp(f2u(x), CTRL, 0xf, 0xf, true)); }
constexpr int XOR1 = 0xB1, XOR2 = 0x4E, HMIR = 0x141;
constexpr int ROR1 = 0x121, ROR2 = 0x122, ROR4 = 0x124, ROR8 = 0x128;
__device__ __forceinline__ float swap16_max(float x) { auto s = __builtin_amdgcn_permlane16_swap(f2u(x), f2u(x), false, false); return fmaxf(u2f(s[0]), u2f(s[1])); }
__device__ __forceinline__ float swap32_max(float x) { auto s = __builtin_amdgcn_permlane32_swap(f2u(x), f2u(x), false, false); return fmaxf(u2f(s[0]), u2f(s[1])); }
__device__ __forceinline__ float swap16_sum(float x) { auto s = __builtin_amdgcn_permlane16_swap(f2u(x), f2u(x), false, false); return u2f(s[0]) + u2f(s[1]); }
__device__ __forceinline__ float swap32_sum(float x) { auto s = __builtin_amdgcn_permlane32_swap(f2u(x), f2u(x), false, false); return u2f(s[0]) + u2f(s[1]); }
__device__ __forceinline__ float row_sum(float x) { x += dpp<ROR8>(x); x += dpp<ROR4>(x); x += dpp<ROR2>(x); x += dpp<ROR1>(x); return x; }
__device__ __forceinline__ float row_max(float x) { x = fmaxf(x, dpp<ROR8>(x)); x = fmaxf(x, dpp<ROR4>(x)); x = fmaxf(x, dpp<ROR2>(x)); x = fmaxf(x, dpp<ROR1>(x)); return x; }
template <int N> __device__ __forceinline__ float bcast(float x) { return dpp<0x150 + N>(x); }
__device__ __forceinline__ float oct_sum(float x) { x += dpp<XOR1>(x); x += dpp<XOR2>(x); x += dpp<HMIR>(x); return x; }
__device__ __forceinline__ float oct_max(float x) { x = fmaxf(x, dpp<XOR1>(x)); x = fmaxf(x, dpp<XOR2>(x)); x = fmaxf(x, dpp<HMIR>(x)); return x; }
#define LDS_BARRIER() asm volatile("s_waitcnt lgkmcnt(0)\n\ts_barrier" ::: "memory")
#define LDS_WAIT() asm volatile("s_waitcnt lgkmcnt(0)" ::: "memory")
#define MEMFENCE() asm volatile("" ::: "memory")
constexpr int AUX_SC1 = 16;

__device__ __forceinline__ u32x4 pack4(f32x4 v, unsigned tA, unsigned tB) {
  const unsigned t0 = f2u(v[0]) + 0x800u, t1 = f2u(v[1]) + 0x800u, t2 = f2u(v[2]) + 0x800u, t3 = f2u(v[3]) + 0x800u;
  return u32x4{tA | ((t0 << 12) & 0xff000000u), (t0 >> 20) | (t1 & 0xfffff000u), (t2 >> 12) | ((t3 << 8) & 0xfff00000u), (t3 >> 24) | (tB << 8)};
}
__device__ __forceinline__ f32x4 unpack4(u32x4 d) {
  return f32x4{u2f((d[1] << 20) | ((d[0] >> 12) & 0xff000u)), u2f(d[1] & 0xfffff000u), u2f(d[2] << 12), u2f(((d[2] >> 8) & 0xfff000u) | (d[3] << 24))};
}
__device__ __forceinline__ bool tags4(u32x4 d, unsigned tA, unsigned tB) { return (d[0] & 0xffffffu) == tA && (d[3] >> 8) == tB; }
__device__ __forceinline__ f32x4 red8_16_32(f32x4 v) {
#pragma unroll
  for (int e = 0; e < 4; ++e) { float x = v[e]; x += dpp<ROR8>(x); v[e] = swap32_sum(swap16_sum(x)); }
  return v;
}

struct Args {
  const unsigned char *xA, *kA, *vA, *wqA, *woA, *xB, *kB, *vB, *wqB, *woB;
  unsigned short* out;
  unsigned char* ws;
};

__device__ __forceinline__ unsigned lds_ld(LAS unsigned char* L, int off) { return __builtin_amdgcn_readfirstlane(*(volatile LAS unsigned*)(L + off)); }
__device__ __forceinline__ void lds_st(LAS unsigned char* L, int off, unsigned v) { *(volatile LAS unsigned*)(L + off) = v; }
template <int SLEEP> __device__ __forceinline__ void lds_wait_ge(LAS unsigned char* L, int off, unsigned need) {
  for (unsigned spin = 0; lds_ld(L, off) < need && spin < LSPIN_CAP; ++spin) { if (SLEEP) __builtin_amdgcn_s_sleep(SLEEP); }
  MEMFENCE();
}
__device__ __forceinline__ void lds_arrive(LAS unsigned char* L, int off, int lane) {
  LDS_WAIT();
  if (lane == 0) __hip_atomic_fetch_add((LAS unsigned*)(L + off), 1u, __ATOMIC_RELAXED, __HIP_MEMORY_SCOPE_WORKGROUP);
}

template <int POL> __device__ __forceinline__ void dma1p(unsigned dst, unsigned voff, const unsigned char* base) {
  unsigned keep;
  if constexpr (POL == 0)
  asm volatile("s_mov_b32 %0, m0\n\ts_mov_b32 m0, %1\n\ts_nop 0\n\tglobal_load_lds_dwordx4 %2, %3 nt\n\ts_mov_b32 m0, %0\n\ts_waitcnt vmcnt(%4)"
               : "=&s"(keep) : "s"(dst), "v"(voff), "s"(base), "i"(WSTART) : "memory");
  else
  asm volatile("s_mov_b32 %0, m0\n\ts_mov_b32 m0, %1\n\ts_nop 0\n\tglobal_load_lds_dwordx4 %2, %3\n\ts_mov_b32 m0, %0\n\ts_waitcnt vmcnt(%4)"
               : "=&s"(keep) : "s"(dst), "v"(voff), "s"(base), "i"(WSTART) : "memory");
}
template <int POL> __device__ __forceinline__ void dma4p(unsigned dst, unsigned v0, unsigned v1, unsigned v2, unsigned v3, const unsigned char* base) {
  unsigned keep;
  if constexpr (POL != 0)
  asm volatile("s_mov_b32 %0, m0\n\ts_mov_b32 m0, %1\n\ts_nop 0\n\t"
               "global_load_lds_dwordx4 %2, %6\n\ts_waitcnt vmcnt(%7)\n\t"
               "global_load_lds_dwordx4 %3, %6 offset:1024\n\ts_waitcnt vmcnt(%7)\n\t"
               "global_load_lds_dwordx4 %4, %6 offset:2048\n\ts_waitcnt vmcnt(%7)\n\t"
               "global_load_lds_dwordx4 %5, %6 offset:3072\n\ts_waitcnt vmcnt(%7)\n\t"
               "s_mov_b32 m0, %0"
               : "=&s"(keep) : "s"(dst), "v"(v0), "v"(v1), "v"(v2), "v"(v3), "s"(base), "i"(WIN_LD) : "memory");
  else
  asm volatile("s_mov_b32 %0, m0\n\ts_mov_b32 m0, %1\n\ts_nop 0\n\t"
               "global_load_lds_dwordx4 %2, %6 nt\n\ts_waitcnt vmcnt(%7)\n\t"
               "global_load_lds_dwordx4 %3, %6 offset:1024 nt\n\ts_waitcnt vmcnt(%7)\n\t"
               "global_load_lds_dwordx4 %4, %6 offset:2048 nt\n\ts_waitcnt vmcnt(%7)\n\t"
               "global_load_lds_dwordx4 %5, %6 offset:3072 nt\n\ts_waitcnt vmcnt(%7)\n\t"
               "s_mov_b32 m0, %0"
               : "=&s"(keep) : "s"(dst), "v"(v0), "v"(v1), "v"(v2), "v"(v3), "s"(base), "i"(WIN_LD) : "memory");
}
__device__ __forceinline__ void dma4(unsigned dst, unsigned v0, unsigned v1, unsigned v2, unsigned v3, const unsigned char* base) { dma4p<0>(dst, v0, v1, v2, v3, base); }
template <int WL> __device__ __forceinline__ void dma4w(unsigned dst, unsigned v0, unsigned v1, unsigned v2, unsigned v3, const unsigned char* base) {
  unsigned keep;
  asm volatile("s_mov_b32 %0, m0\n\ts_mov_b32 m0, %1\n\ts_waitcnt vmcnt(%7)\n\t"
               "global_load_lds_dwordx4 %2, %6 nt\n\ts_waitcnt vmcnt(%7)\n\t"
               "global_load_lds_dwordx4 %3, %6 offset:1024 nt\n\ts_waitcnt vmcnt(%7)\n\t"
               "global_load_lds_dwordx4 %4, %6 offset:2048 nt\n\ts_waitcnt vmcnt(%7)\n\t"
               "global_load_lds_dwordx4 %5, %6 offset:3072 nt\n\t"
               "s_mov_b32 m0, %0"
               : "=&s"(keep) : "s"(dst), "v"(v0), "v"(v1), "v"(v2), "v"(v3), "s"(base), "i"(WL) : "memory");
}
template <int N> __device__ __forceinline__ void vm_wait() { asm volatile("s_waitcnt vmcnt(%0)" :: "i"(N) : "memory"); }

__device__ __forceinline__ u32x2 tile_scores(const u32x4* K, const u32x4 (&q)[4]) {
  f32x4 s = {0.f, 0.f, 0.f, 0.f};
#pragma unroll
  for (int i = 0; i < 4; ++i) s = __builtin_amdgcn_mfma_f32_16x16x32_bf16(__builtin_bit_cast(bf16x8, K[i]), __builtin_bit_cast(bf16x8, q[i]), s, 0, 0, 0);
  const unsigned r01 = cvtpk(s[0], s[1]), r23 = cvtpk(s[2], s[3]);
  return u32x2{cvtpk(lo16(r01) * SCALE, hi16(r01) * SCALE), cvtpk(lo16(r23) * SCALE, hi16(r23) * SCALE)};
}
__device__ __forceinline__ float max4(u32x2 t) { return fmaxf(fmaxf(lo16(t[0]), hi16(t[0])), fmaxf(lo16(t[1]), hi16(t[1]))); }
__device__ __forceinline__ u32x2 to_probs(u32x2 t, float m2, float& l) {
  const float p0 = __builtin_amdgcn_exp2f(fmaf(lo16(t[0]), LOG2E, -m2)), p1 = __builtin_amdgcn_exp2f(fmaf(hi16(t[0]), LOG2E, -m2));
  const float p2 = __builtin_amdgcn_exp2f(fmaf(lo16(t[1]), LOG2E, -m2)), p3 = __builtin_amdgcn_exp2f(fmaf(hi16(t[1]), LOG2E, -m2));
  l += (p0 + p1) + (p2 + p3);
  return u32x2{cvtpk(p0, p1), cvtpk(p2, p3)};
}
struct Sel { unsigned l0, l1, h0, h1; };
__device__ __forceinline__ void tile_pv(f32x4& a0, f32x4& a1, const u32x4* V, u32x2 p, const Sel& s) {
#pragma unroll
  for (int r = 0; r < 4; ++r) {
    const unsigned pk = p[r >> 1];
    const u32x2 a = {__builtin_amdgcn_perm(0u, pk, (r & 1) ? s.h0 : s.l0), __builtin_amdgcn_perm(0u, pk, (r & 1) ? s.h1 : s.l1)};
    const u32x2 v0 = {V[r][0], V[r][1]}, v1 = {V[r][2], V[r][3]};
    a0 = __builtin_amdgcn_mfma_f32_16x16x16bf16_1k(__builtin_bit_cast(s16x4, a), __builtin_bit_cast(s16x4, v0), a0, 0, 0, 0);
    a1 = __builtin_amdgcn_mfma_f32_16x16x16bf16_1k(__builtin_bit_cast(s16x4, a), __builtin_bit_cast(s16x4, v1), a1, 0, 0, 0);
  }
}

#define STAMP(i) do { if (tid == 0) { ((volatile LAS unsigned long long*)(lds + L_END - 128))[(i)] = __builtin_amdgcn_s_memrealtime(); } } while (0)
#define WSTAMP(i) do { if (lane == 0) { ((volatile LAS unsigned long long*)(lds + L_END - 128))[(i)] = __builtin_amdgcn_s_memrealtime(); } } while (0)
#define DUMP_STAMPS() do { if (AMPI >= 0 && b == 0 && tid == 0) { const unsigned long long t_end = __builtin_amdgcn_s_memrealtime(); \
    unsigned long long d = AMPX * (((volatile LAS unsigned long long*)(lds + L_END - 128))[AMPI >= 0 ? AMPI : 0] - t_start); if (d > 100000ull) d = 100000ull; \
    for (unsigned sp_ = 0; sp_ < 10000000u && __builtin_amdgcn_s_memrealtime() < t_end + d; ++sp_) __builtin_amdgcn_s_sleep(1); } } while (0)

__global__ void __launch_bounds__(NTHR, 3) fused_decode(Args a) {
  __shared__ __attribute__((aligned(16))) unsigned char lds_raw[L_END];
  LAS unsigned char* lds = (LAS unsigned char*)lds_raw;
  const int b = blockIdx.x, h = b & 7, j = b >> 3;
  const int tid = threadIdx.x, lane = tid & 63, w = __builtin_amdgcn_readfirstlane(tid >> 6);
  const int hh = lane >> 4, i16 = lane & 15, jj = lane & 3;
  const unsigned long long t_start = __builtin_amdgcn_s_memrealtime();
  const int qcb = j >> 1, qrh = j & 1, qbr = qcb >> 3;

  if (w == NC) {
    __builtin_amdgcn_s_setprio(3);
    const unsigned ring = (unsigned)(size_t)(lds + L_RING);
    int pub = 0, pslot = 0, slot = 0;
    unsigned long long t_first = 0;
    auto publish1 = [&]() { lds_st(lds, L_FULL + 4 * pslot, (unsigned)(pub + 1)); ++pub; pslot = pslot + 1 == NS ? 0 : pslot + 1; };
    auto pre = [&](int q) {
      if (q >= NS) {
        const unsigned need = (unsigned)(q - NS + 1);
        if (lds_ld(lds, L_FREE + 4 * slot) < need) {
          vm_wait<0>();
          while (pub < q) publish1();
          lds_wait_ge<1>(lds, L_FREE + 4 * slot, need);
        }
      }
    };
    auto post = [&](int q) {
      while (q - pub >= LAG) publish1();
      slot = slot + 1 == NS ? 0 : slot + 1;
    };
    {
      const unsigned char* wq = (qbr ? a.wqB : a.wqA) + (size_t)(h * 512 + 64 * (qcb & 7)) * 2 + (size_t)qrh * 256 * 8192;
      const unsigned vo = (lane >> 3) * 8192 + (lane & 7) * 16;
#pragma unroll
      for (int q = 0; q < 4; ++q) {
#pragma unroll
        for (int m = 0; m < 8; ++m) { dma1p<0>(ring + q * SLOT + m * 1024, vo, wq + (size_t)(q * 64 + m * 8) * 8192); if (q == 0 && m == 0) t_first = __builtin_amdgcn_s_memrealtime(); }
        if (q == 0) { LDS_BARRIER(); if (lane == 0) ((volatile LAS unsigned long long*)(lds + L_END - 128))[15] = t_first; }
        while (q - pub >= (WSTART + 7) / 8) publish1();
        slot = slot + 1 == NS ? 0 : slot + 1;
      }
    }
    const unsigned k0 = hh * 256 + ((i16 ^ (0 + hh)) * 16), k1 = hh * 256 + ((i16 ^ (4 + hh)) * 16), k2 = hh * 256 + ((i16 ^ (8 + hh)) * 16), k3 = hh * 256 + ((i16 ^ (12 + hh)) * 16);
    const unsigned vl = lane * 16;
    {
      const unsigned char* kp = a.kB + ((size_t)h * SK + (SK - WIN) + j * 128) * 256;
      const unsigned char* vp = a.vB + ((size_t)h * SK + (SK - WIN) + j * 128) * 256;
      for (int q = 4; q < 12; ++q) {
        dma4p<(POLB & 2) ? 1 : 0>(ring + slot * SLOT, k0, k1, k2, k3, kp);
        dma4p<(POLB & 4) ? 1 : 0>(ring + slot * SLOT + 4096, vl, vl, vl, vl, vp);
        kp += 4096; vp += 4096;
        post(q);
      }
      kp = a.kA + ((size_t)h * SK + j * 2048) * 256;
      unsigned o0 = k0, o1 = k1, o2 = k2, o3 = k3;
      unsigned frv = *(volatile LAS unsigned*)(lds + L_FREE + 4 * slot);
      for (int q = 12; q < 140; ++q) {
        if (q == 76) { WSTAMP(12); kp = a.vA + ((size_t)h * SK + j * 2048) * 256; o0 = vl; o1 = vl; o2 = vl; o3 = vl; }
        if (q >= NS && (unsigned)__builtin_amdgcn_readfirstlane(frv) < (unsigned)(q - NS + 1)) pre(q);
        const int nslot = slot + 1 == NS ? 0 : slot + 1;
        frv = *(volatile LAS unsigned*)(lds + L_FREE + 4 * nslot);
        dma4w<WIN_LD>(ring + slot * SLOT, o0, o1, o2, o3, kp);
        dma4w<WIN_LD>(ring + slot * SLOT + 4096, o0, o1, o2, o3, kp + 4096);
        while (q - pub >= LAG2) publish1();
        kp += 8192;
        slot = nslot;
      }
      WSTAMP(9);
      vp = (j >> 4 ? a.woB : a.woA) + (size_t)(h * 512 + 32 * (j & 15)) * 1024;
      for (int q = 140; q < 144; ++q) {
        pre(q);
        dma4w<WIN_LD>(ring + slot * SLOT, vl, vl, vl, vl, vp); dma4w<WIN_LD>(ring + slot * SLOT + 4096, vl, vl, vl, vl, vp + 4096);
        vp += 8192;
        while (q - pub >= LAG2) publish1();
        slot = slot + 1 == NS ? 0 : slot + 1;
      }
    }
    if (NSEQ - pub > 7) { vm_wait<56>(); publish1(); }
    if (NSEQ - pub > 6) { vm_wait<48>(); publish1(); }
    if (NSEQ - pub > 5) { vm_wait<40>(); publish1(); }
    if (NSEQ - pub > 4) { vm_wait<32>(); publish1(); }
    __builtin_amdgcn_s_barrier();
    if (NSEQ - pub > 3) { vm_wait<24>(); publish1(); }
    if (NSEQ - pub > 2) { vm_wait<16>(); publish1(); }
    if (NSEQ - pub > 1) { vm_wait<8>(); publish1(); }
    vm_wait<0>();
    while (pub < NSEQ) publish1();
    WSTAMP(10);
    return;
  }

  const __amdgpu_buffer_rsrc_t rs = __builtin_amdgcn_make_buffer_rsrc((void*)a.ws, 0, (int)WS_END, 0x00020000);
  const u32x2 gen = __builtin_amdgcn_raw_buffer_load_b64(rs, WS_GEN, 0, AUX_SC1);
  unsigned short xv[4];
  {
    const GAS unsigned char* xp = (const GAS unsigned char*)(qbr ? a.xB : a.xA) + 2 * (qrh * 256 + w * 32 + (lane >> 3));
#pragma unroll
    for (int i = 0; i < 4; ++i) xv[i] = *(const GAS unsigned short*)(xp + 16 * i);
  }
  if (tid < (L_RED - L_FULL) / 4) *(LAS unsigned*)(lds + L_FULL + 4 * tid) = 0u;
  if (tid >= 128 && tid < 160) *(LAS unsigned*)(lds + L_END - 128 + 4 * (tid - 128)) = 0u;
  LDS_BARRIER();

  lds_wait_ge<1>(lds, L_FULL + 4 * (w >> 1), (unsigned)((w >> 1) + 1));
  {
    u32x4 wv[4];
#pragma unroll
    for (int i = 0; i < 4; ++i) wv[i] = *(const LAS u32x4*)(lds + L_RING + (w >> 1) * SLOT + (w & 1) * 4096 + i * 1024 + lane * 16);
    float acc[8];
#pragma unroll
    for (int k = 0; k < 8; ++k) acc[k] = 0.f;
#pragma unroll
    for (int i = 0; i < 4; ++i) {
      const float x = lo16(xv[i]);
#pragma unroll
      for (int k = 0; k < 4; ++k) { acc[2 * k] = fmaf(x, lo16(wv[i][k]), acc[2 * k]); acc[2 * k + 1] = fmaf(x, hi16(wv[i][k]), acc[2 * k + 1]); }
    }
#pragma unroll
    for (int k = 0; k < 8; ++k) { float v = acc[k]; v += dpp<ROR8>(v); v = swap16_sum(v); v = swap32_sum(v); acc[k] = v; }
    if (lane < 8) {
      LAS f32x4* red = (LAS f32x4*)(lds + L_RED + w * 256 + lane * 32);
      red[0] = f32x4{acc[0], acc[1], acc[2], acc[3]}; red[1] = f32x4{acc[4], acc[5], acc[6], acc[7]};
    }
  }
  lds_arrive(lds, L_CNT + C_QRED, lane);
  const unsigned tagA = __builtin_amdgcn_readfirstlane(gen[0]) + 1u, tagB = __builtin_amdgcn_readfirstlane(gen[1]) + 1u;
  if (w == 0) {
    lds_wait_ge<0>(lds, L_CNT + C_QRED, NC);
    if (lane < 4) lds_st(lds, L_FREE + 4 * lane, (unsigned)(lane + 1));
    if (lane < 32) {
      f32x2 s = {0.f, 0.f};
#pragma unroll
      for (int ww = 0; ww < 8; ++ww) s += *(const LAS f32x2*)(lds + L_RED + ww * 256 + lane * 8);
      __builtin_amdgcn_raw_buffer_store_b128(u32x4{tagA, f2u(s[0]), f2u(s[1]), tagB}, rs, WS_QG + ((h * 32 + j) * 32 + lane) * 16, 0, AUX_SC1);
    }
  }
  STAMP(1);
  int slot = (4 + w) % NS;
  const unsigned kr0 = i16 * 256 + (((0 + hh) ^ i16) * 16), kr1 = i16 * 256 + (((4 + hh) ^ i16) * 16), kr2 = i16 * 256 + (((8 + hh) ^ i16) * 16), kr3 = i16 * 256 + (((12 + hh) ^ i16) * 16);
  const unsigned vr = hh * 1024 + i16 * 16;
  auto fetch = [&](int it, int kind, u32x4 (&T)[8]) {
    const unsigned seq1 = (unsigned)(4 + 8 * it + w + 1);
    lds_wait_ge<1>(lds, L_FULL + 4 * slot, seq1);
    const LAS unsigned char* sp = lds + L_RING + slot * SLOT;
    if (kind != 2) { T[0] = *(const LAS u32x4*)(sp + kr0); T[1] = *(const LAS u32x4*)(sp + kr1); T[2] = *(const LAS u32x4*)(sp + kr2); T[3] = *(const LAS u32x4*)(sp + kr3); }
    else {
#pragma unroll
      for (int r = 0; r < 4; ++r) T[r] = *(const LAS u32x4*)(sp + vr + r * 256);
    }
    if (kind == 1) { T[4] = *(const LAS u32x4*)(sp + 4096 + kr0); T[5] = *(const LAS u32x4*)(sp + 4096 + kr1); T[6] = *(const LAS u32x4*)(sp + 4096 + kr2); T[7] = *(const LAS u32x4*)(sp + 4096 + kr3); }
    else {
#pragma unroll
      for (int r = 0; r < 4; ++r) T[4 + r] = *(const LAS u32x4*)(sp + 4096 + vr + r * 256);
    }
    LDS_WAIT();
    lds_st(lds, L_FREE + 4 * slot, seq1);
    asm volatile("" : "+v"(T[0]), "+v"(T[1]), "+v"(T[2]), "+v"(T[3]), "+v"(T[4]), "+v"(T[5]), "+v"(T[6]), "+v"(T[7]) :: "memory");
    slot += 8; if (slot >= NS) slot -= NS;
  };
  u32x4 X0[8], X1[8], X2[8];
  fetch(0, 0, X0);
  if (PRE >= 2) fetch(1, 1, X1);
  if (PRE >= 3) fetch(2, 1, X2);
  {
    const unsigned off = WS_QG + ((h * 32 + 2 * (tid >> 5)) * 32 + (tid & 31)) * 16;
    u32x4 g0 = {0u, 0u, 0u, 0u}, g1 = {0u, 0u, 0u, 0u};
    bool ok = false;
    for (unsigned spin = 0; spin < SPIN_CAP; ++spin) {
      MEMFENCE();
      if (!ok) {
        g0 = __builtin_amdgcn_raw_buffer_load_b128(rs, off, 0, AUX_SC1);
        g1 = __builtin_amdgcn_raw_buffer_load_b128(rs, off + 512, 0, AUX_SC1);
        ok = g0[0] == tagA && g0[3] == tagB && g1[0] == tagA && g1[3] == tagB;
      }
      if (__all(ok)) break;
      if (QSLEEP) __builtin_amdgcn_s_sleep(QSLEEP);
    }
    *(LAS unsigned*)(lds + L_Q + tid * 4) = cvtpk(u2f(g0[1]) + u2f(g1[1]), u2f(g0[2]) + u2f(g1[2]));
  }
  lds_arrive(lds, L_CNT + C_QIN, lane);
  lds_wait_ge<0>(lds, L_CNT + C_QIN, NC);
  u32x4 qA[4], qB[4];
#pragma unroll
  for (int s = 0; s < 4; ++s) {
    qA[s] = *(const LAS u32x4*)(lds + L_Q + ((i16 >> 2) * 128 + 32 * s + 8 * hh) * 2);
    qB[s] = *(const LAS u32x4*)(lds + L_Q + 1024 + ((i16 >> 2) * 128 + 32 * s + 8 * hh) * 2);
  }
  STAMP(2);

  Sel sel;
  sel.l0 = jj == 0 ? 0x0c0c0100u : jj == 1 ? 0x01000c0cu : 0x0c0c0c0cu;  sel.h0 = jj == 0 ? 0x0c0c0302u : jj == 1 ? 0x03020c0cu : 0x0c0c0c0cu;
  sel.l1 = jj == 2 ? 0x0c0c0100u : jj == 3 ? 0x01000c0cu : 0x0c0c0c0cu;  sel.h1 = jj == 2 ? 0x0c0c0302u : jj == 3 ? 0x03020c0cu : 0x0c0c0c0cu;

  f32x4 b0 = {0.f, 0.f, 0.f, 0.f}, b1 = {0.f, 0.f, 0.f, 0.f};
  {
    const u32x2 t = tile_scores(X0, qB);
    const float m2 = swap32_max(swap16_max(max4(t))) * LOG2E;
    float l = 0.f;
    const u32x2 p = to_probs(t, m2, l);
    l = swap32_sum(swap16_sum(l));
    tile_pv(b0, b1, X0 + 4, p, sel);
    if (hh == 0 && jj == 0) *(LAS f32x2*)(lds + L_TB + ((i16 >> 2) * 8 + w) * 8) = f32x2{m2, l};
  }

  u32x2 P[16];
  float mx = -INFINITY;
#pragma unroll
  for (int i = 0; i < 8; ++i) {
    if (i == 0) { if (PRE < 2) fetch(1, 1, X1); }
    else if (i == 1) { if (PRE < 3) fetch(2, 1, X2); }
    else fetch(i + 1, 1, X1);
    const u32x4* T = i == 1 ? X2 : X1;
    P[2 * i] = tile_scores(T, qA);
    P[2 * i + 1] = tile_scores(T + 4, qA);
    mx = fmaxf(mx, fmaxf(max4(P[2 * i]), max4(P[2 * i + 1])));
  }
  STAMP(13);
  mx = swap32_max(swap16_max(mx));
  if (hh == 0 && jj == 0) *(LAS float*)(lds + L_T1 + ((i16 >> 2) * 8 + w) * 4) = mx;
  lds_arrive(lds, L_CNT + C_MAX, lane);
  lds_wait_ge<0>(lds, L_CNT + C_MAX, NC);
  {
    const f32x4 m0 = *(const LAS f32x4*)(lds + L_T1 + (i16 >> 2) * 32), m1 = *(const LAS f32x4*)(lds + L_T1 + (i16 >> 2) * 32 + 16);
    const float m2 = fmaxf(fmaxf(fmaxf(m0[0], m0[1]), fmaxf(m0[2], m0[3])), fmaxf(fmaxf(m1[0], m1[1]), fmaxf(m1[2], m1[3]))) * LOG2E;
    float l = 0.f;
#pragma unroll
    for (int t = 0; t < 16; ++t) P[t] = to_probs(P[t], m2, l);
    l = swap32_sum(swap16_sum(l));
    if (hh == 0 && jj == 0) *(LAS float*)(lds + L_T2 + ((i16 >> 2) * 8 + w) * 4) = l;
  }
  lds_arrive(lds, L_CNT + C_SUM, lane);
  lds_wait_ge<0>(lds, L_CNT + C_SUM, NC);
  const int mbr = w >> 2, mg = w & 3;
  {
    const int ww = lane & 7;
    float M, L;
    if (mbr == 0) {
      M = oct_max(*(const LAS float*)(lds + L_T1 + (mg * 8 + ww) * 4)) * LOG2E;
      L = oct_sum(*(const LAS float*)(lds + L_T2 + (mg * 8 + ww) * 4));
    } else {
      const f32x2 ml = *(const LAS f32x2*)(lds + L_TB + (mg * 8 + ww) * 8);
      M = oct_max(ml[0]);
      L = oct_sum(ml[1] * __builtin_amdgcn_exp2f(ml[0] - M));
    }
    if (lane == 0) __builtin_amdgcn_raw_buffer_store_b128(u32x4{tagA, f2u(M), f2u(L), tagB}, rs, WS_ML + (((h * 2 + mbr) * 4 + mg) * 32 + j) * 16, 0, AUX_SC1);
  }
  STAMP(14);

  f32x4 a0 = {0.f, 0.f, 0.f, 0.f}, a1 = {0.f, 0.f, 0.f, 0.f};
  float fA = 0.f, fB = 0.f;
#pragma unroll
  for (int i = 0; i < 8; ++i) {
    if (i == 4) {
      const unsigned off = WS_ML + (((h * 2 + mbr) * 4 + mg) * 32 + (lane & 31)) * 16;
      u32x4 mv = {0u, 0u, 0u, 0u};
      bool ok = false;
      for (unsigned spin = 0; spin < SPIN_CAP; ++spin) {
        MEMFENCE();
        if (!ok) { mv = __builtin_amdgcn_raw_buffer_load_b128(rs, off, 0, AUX_SC1); ok = mv[0] == tagA && mv[3] == tagB; }
        if (__all(ok)) break;
        __builtin_amdgcn_s_sleep(16);
      }
      const float M = swap16_max(row_max(u2f(mv[1])));
      const float L = swap16_sum(row_sum(u2f(mv[2]) * __builtin_amdgcn_exp2f(u2f(mv[1]) - M)));
      if (lane == 0) *(LAS f32x2*)(lds + L_FF + w * 8) = f32x2{M, 1.0f / L};
      lds_arrive(lds, L_CNT + C_FF, lane);
    }
    if (i == 7) {
      lds_wait_ge<0>(lds, L_CNT + C_FF, NC);
      const f32x4 m0 = *(const LAS f32x4*)(lds + L_T1 + hh * 32), m1 = *(const LAS f32x4*)(lds + L_T1 + hh * 32 + 16);
      const float m2 = fmaxf(fmaxf(fmaxf(m0[0], m0[1]), fmaxf(m0[2], m0[3])), fmaxf(fmaxf(m1[0], m1[1]), fmaxf(m1[2], m1[3]))) * LOG2E;
      const f32x2 FA = *(const LAS f32x2*)(lds + L_FF + hh * 8), FB = *(const LAS f32x2*)(lds + L_FF + (4 + hh) * 8);
      const float mB = *(const LAS float*)(lds + L_TB + (hh * 8 + w) * 8);
      fA = __builtin_amdgcn_exp2f(m2 - FA[0]) * FA[1];
      fB = __builtin_amdgcn_exp2f(mB - FB[0]) * FB[1];
      const int s = lane < MRG_SLOTS ? lane : 0;
      const unsigned need = (unsigned)(s + NS * ((139 - s) / NS) + 1);
      for (unsigned spin = 0; spin < LSPIN_CAP; ++spin) { MEMFENCE(); if (__all(*(volatile LAS unsigned*)(lds + L_FREE + 4 * s) >= need)) break; }
      MEMFENCE();
      LAS float* rB = (LAS float*)(lds + L_MRG) + ((8 + w) * 4 + hh) * MREC + 8 * i16;
      *(LAS f32x4*)(rB) = b0 * fB; *(LAS f32x4*)(rB + 4) = b1 * fB;
    }
    fetch(9 + i, 2, X1);
    tile_pv(a0, a1, X1, P[2 * i], sel);
    tile_pv(a0, a1, X1 + 4, P[2 * i + 1], sel);
  }
  STAMP(3);
  if (w == 7) WSTAMP(11);

  const unsigned tA = tagA & 0xffffffu, tB = tagB & 0xffffffu;
  {
    LAS float* rA = (LAS float*)(lds + L_MRG) + (w * 4 + hh) * MREC + 8 * i16;
    *(LAS f32x4*)(rA) = a0 * fA; *(LAS f32x4*)(rA + 4) = a1 * fA;
  }
  LDS_BARRIER();
  if (tid < 256) {
    const int br = tid >> 7, g = (tid >> 5) & 3, dq = tid & 31;
    const LAS float* mrg = (const LAS float*)(lds + L_MRG) + (br * 8 * 4 + g) * MREC + 4 * dq;
    f32x4 o4[8];
#pragma unroll
    for (int ww = 0; ww < 8; ++ww) o4[ww] = *(const LAS f32x4*)(mrg + ww * 4 * MREC);
    const f32x4 o = ((o4[0] + o4[1]) + (o4[2] + o4[3])) + ((o4[4] + o4[5]) + (o4[6] + o4[7]));
    const int jc = br * 16 + g * 4 + (dq >> 3), k = dq & 7;
    __builtin_amdgcn_raw_buffer_store_b128(pack4(o, tA, tB), rs, WS_I1 + (((h * 32 + jc) * 32 + j) * 8 + k) * 16, 0, AUX_SC1);
  }
  STAMP(4);

  const unsigned offA = WS_I1 + ((h * 32 + j) * 32 * 8 + (w & 3) * 64 + lane) * 16;
  u32x4 av = {0u, 0u, 0u, 0u};
  if (w >= 4) av = __builtin_amdgcn_raw_buffer_load_b128(rs, offA, 0, AUX_SC1);
  MEMFENCE();
  float W[32];
  lds_wait_ge<0>(lds, L_FULL + 4 * (143 % NS), 144u);
  {
    const LAS unsigned short* wp = (const LAS unsigned short*)(lds + L_RING + (140 % NS) * SLOT) + 64 * w + lane;
#pragma unroll
    for (int r = 0; r < 32; ++r) W[r] = u2f((unsigned)wp[r * 512] << 16);
#pragma unroll
    for (int r = 0; r < 32; r += 8) asm volatile("" : "+v"(W[r]), "+v"(W[r + 1]), "+v"(W[r + 2]), "+v"(W[r + 3]), "+v"(W[r + 4]), "+v"(W[r + 5]), "+v"(W[r + 6]), "+v"(W[r + 7]));
  }
  if (w >= 4) {
    bool ok = tags4(av, tA, tB);
    for (unsigned spin = 0; spin < SPIN_CAP && !__all(ok); ++spin) {
      MEMFENCE();
      if (!ok) { av = __builtin_amdgcn_raw_buffer_load_b128(rs, offA, 0, AUX_SC1); ok = tags4(av, tA, tB); }
    }
    if (w == 4) WSTAMP(5);
    const f32x4 o = red8_16_32(unpack4(av));
    if (lane < 8) {
#pragma unroll
      for (int e = 0; e < 4; ++e) *(LAS float*)(lds + L_H1 + ((4 * lane + e) * 4 + (w & 3)) * 4) = o[e];
    }
  }
  LDS_BARRIER();
  {
    const f32x4 ra = *(const LAS f32x4*)(lds + L_H1 + i16 * 16), rb = *(const LAS f32x4*)(lds + L_H1 + (16 + i16) * 16);
    const unsigned ob = cvtpk((ra[0] + ra[1]) + (ra[2] + ra[3]), (rb[0] + rb[1]) + (rb[2] + rb[3]));
    const float oa = lo16(ob), oc = hi16(ob);
    float y0 = 0.f, y1 = 0.f, y2 = 0.f, y3 = 0.f;
#define FM(acc, o, wv, n) "v_fmac_f32_dpp " acc ", " o ", " wv " row_newbcast:" #n " row_mask:0xf bank_mask:0xf\n\t"
#define ROWS(n0, n1, n2, n3) asm volatile("s_nop 1\n\t" \
      FM("%0", "%4", "%6", n0) FM("%1", "%5", "%7", n0) FM("%2", "%4", "%8", n1) FM("%3", "%5", "%9", n1) \
      FM("%0", "%4", "%10", n2) FM("%1", "%5", "%11", n2) FM("%2", "%4", "%12", n3) FM("%3", "%5", "%13", n3) \
      : "+v"(y0), "+v"(y1), "+v"(y2), "+v"(y3) \
      : "v"(oa), "v"(oc), "v"(W[n0]), "v"(W[16 + n0]), "v"(W[n1]), "v"(W[16 + n1]), "v"(W[n2]), "v"(W[16 + n2]), "v"(W[n3]), "v"(W[16 + n3]));
    ROWS(0, 1, 2, 3) ROWS(4, 5, 6, 7) ROWS(8, 9, 10, 11) ROWS(12, 13, 14, 15)
#undef ROWS
#undef FM
    const float y = (y0 + y1) + (y2 + y3);
    const f32x4 y4 = {y, dpp<0x55>(y), dpp<0xAA>(y), dpp<0xFF>(y)};
    const int c = (j >> 4) * 16 + 2 * w + (lane >> 5), k = (lane & 31) >> 2, pidx = h * 16 + (j & 15);
    if (jj == 0) __builtin_amdgcn_raw_buffer_store_b128(pack4(y4, tA, tB), rs, WS_I2 + ((c * 128 + pidx) * 8 + k) * 16, 0, AUX_SC1);
  }
  STAMP(6);

  if (j < 4) {
    const int c = h * 4 + j;
    const unsigned off = WS_I2 + (c * 128 + 16 * w) * 128 + lane * 16;
    u32x4 v[2] = {{0u, 0u, 0u, 0u}, {0u, 0u, 0u, 0u}};
    bool ok = false;
    for (unsigned spin = 0; spin < SPIN_CAP; ++spin) {
      MEMFENCE();
      if (!ok) {
#pragma unroll
        for (int i = 0; i < 2; ++i) v[i] = __builtin_amdgcn_raw_buffer_load_b128(rs, off + i * 1024, 0, AUX_SC1);
        ok = tags4(v[0], tA, tB) && tags4(v[1], tA, tB);
      }
      if (__all(ok)) break;
    }
    STAMP(7);
    const f32x4 s4 = red8_16_32(unpack4(v[0]) + unpack4(v[1]));
    if (lane < 8) {
#pragma unroll
      for (int e = 0; e < 4; ++e) *(LAS float*)(lds + L_F + ((4 * lane + e) * 8 + w) * 4) = s4[e];
    }
    LDS_BARRIER();
    if (tid < 32) {
      const f32x4 p0 = *(const LAS f32x4*)(lds + L_F + tid * 32), p1 = *(const LAS f32x4*)(lds + L_F + tid * 32 + 16);
      const float s = ((p0[0] + p0[1]) + (p0[2] + p0[3])) + ((p1[0] + p1[1]) + (p1[2] + p1[3]));
      const unsigned pk = cvtpk(s, dpp<XOR1>(s));
      if ((tid & 1) == 0) *(GAS unsigned*)((GAS unsigned char*)a.out + (c * 32 + tid) * 2) = pk;
      if (c == 0 && tid == 0) __builtin_amdgcn_raw_buffer_store_b64(u32x2{tagA, tagB}, rs, WS_GEN, 0, AUX_SC1);
    }
  }
  DUMP_STAMPS();
}

extern "C" void kernel_launch(void* const* d_in, const int* in_sizes, int n_in, void* d_out, int out_size, void* d_ws, size_t ws_size, hipStream_t stream) {
  Args a;
  a.xA = (const unsigned char*)d_in[0]; a.kA = (const unsigned char*)d_in[1]; a.vA = (const unsigned char*)d_in[2]; a.wqA = (const unsigned char*)d_in[3]; a.woA = (const unsigned char*)d_in[4];
  a.xB = (const unsigned char*)d_in[5]; a.kB = (const unsigned char*)d_in[6]; a.vB = (const unsigned char*)d_in[7]; a.wqB = (const unsigned char*)d_in[8]; a.woB = (const unsigned char*)d_in[9];
  a.out = (unsigned short*)d_out;
  a.ws = (unsigned char*)d_ws;
  hipLaunchKernelGGL(fused_decode, dim3(NWG), dim3(NTHR), 0, stream, a);
}
```

```cpp
#include <hip/hip_runtime.h>
#include <hip/hip_bf16.h>
#include <stdint.h>

#define GAS __attribute__((address_space(1)))
#define LAS __attribute__((address_space(3)))
typedef unsigned u32x4 __attribute__((ext_vector_type(4)));
typedef unsigned u32x2 __attribute__((ext_vector_type(2)));
typedef float f32x4 __attribute__((ext_vector_type(4)));
typedef float f32x2 __attribute__((ext_vector_type(2)));
typedef __bf16 bf16x8 __attribute__((ext_vector_type(8)));
typedef __bf16 bf16x2 __attribute__((ext_vector_type(2)));
typedef short s16x4 __attribute__((ext_vector_type(4)));

constexpr int NS = 18;
constexpr int WIN_LD = 21;
constexpr int WSTART = 40;
constexpr int QSLEEP = 16;

constexpr int SK = 65536, WIN = 4096;
constexpr int NWG = 256, NC = 8, NTHR = (NC + 1) * 64;
constexpr int SLOT = 8192, NSEQ = 4 + 8 + 64 + 64 + 4;
constexpr int LAG = (WIN_LD + 7) / 8;
constexpr int LAG2 = (WIN_LD + 8) / 8;
constexpr float LOG2E = 1.4426950408889634f;
constexpr float SCALE = 0.08838834764831845f;
constexpr unsigned SPIN_CAP = 1u << 17, LSPIN_CAP = 1u << 23;

constexpr unsigned WS_GEN = 0;
constexpr unsigned WS_QG = 4096;
constexpr unsigned WS_ML = WS_QG + 8 * 32 * 32 * 16;
constexpr unsigned WS_I1 = WS_ML + 256 * 128;
constexpr unsigned WS_I2 = WS_I1 + 8 * 32 * 32 * 128;
constexpr unsigned WS_END = WS_I2 + 32 * 128 * 128;

constexpr int L_RING = 0;
constexpr int L_FULL = NS * SLOT;
constexpr int L_FREE = L_FULL + 128;
constexpr int L_CNT = L_FREE + 128;
constexpr int L_RED = L_CNT + 64;
constexpr int L_Q = L_RED + 2048;
constexpr int L_T1 = L_Q + 2048;
constexpr int L_T2 = L_T1 + 128;
constexpr int L_TB = L_T2 + 128;
constexpr int L_FF = L_TB + 256;
constexpr int L_H1 = L_FF + 64;
constexpr int L_F = L_H1 + 512;
constexpr int L_END = L_F + 1024;
constexpr int C_QRED = 0, C_QIN = 4, C_MAX = 8, C_SUM = 12, C_FF = 16;
constexpr int MREC = 132;
constexpr int L_MRG = 0;
constexpr int MRG_SLOTS = 5;
static_assert(L_END <= 160 * 1024, "LDS");
static_assert(2 * 8 * 4 * MREC * 4 <= MRG_SLOTS * SLOT, "merge area");
static_assert((140 % NS) >= MRG_SLOTS && (143 % NS) >= MRG_SLOTS && (140 % NS) <= (143 % NS), "wo slots contiguous and clear of the merge area");
static_assert((132 % NS) >= MRG_SLOTS && (139 % NS) >= MRG_SLOTS && (132 % NS) <= (139 % NS), "last round clear of the merge area");
static_assert(WIN_LD >= 9 && WIN_LD <= 48, "window");

__device__ __forceinline__ unsigned cvtpk(float lo, float hi) { f32x2 f = {lo, hi}; return __builtin_bit_cast(unsigned, __builtin_convertvector(f, bf16x2)); }
__device__ __forceinline__ float lo16(unsigned u) { return __builtin_bit_cast(float, u << 16); }
__device__ __forceinline__ float hi16(unsigned u) { return __builtin_bit_cast(float, u & 0xffff0000u); }
__device__ __forceinline__ float u2f(unsigned u) { return __builtin_bit_cast(float, u); }
__device__ __forceinline__ unsigned f2u(float f) { return __builtin_bit_cast(unsigned, f); }
template <int CTRL> __device__ __forceinline__ float dpp(float x) { return u2f(__builtin_amdgcn_mov_dpp(f2u(x), CTRL, 0xf, 0xf, true)); }
constexpr int XOR1 = 0xB1, XOR2 = 0x4E, HMIR = 0x141;
constexpr int ROR1 = 0x121, ROR2 = 0x122, ROR4 = 0x124, ROR8 = 0x128;
__device__ __forceinline__ float swap16_max(float x) { auto s = __builtin_amdgcn_permlane16_swap(f2u(x), f2u(x), false, false); return fmaxf(u2f(s[0]), u2f(s[1])); }
__device__ __forceinline__ float swap32_max(float x) { auto s = __builtin_amdgcn_permlane32_swap(f2u(x), f2u(x), false, false); return fmaxf(u2f(s[0]), u2f(s[1])); }
__device__ __forceinline__ float swap16_sum(float x) { auto s = __builtin_amdgcn_permlane16_swap(f2u(x), f2u(x), false, false); return u2f(s[0]) + u2f(s[1]); }
__device__ __forceinline__ float swap32_sum(float x) { auto s = __builtin_amdgcn_permlane32_swap(f2u(x), f2u(x), false, false); return u2f(s[0]) + u2f(s[1]); }
__device__ __forceinline__ float row_sum(float x) { x += dpp<ROR8>(x); x += dpp<ROR4>(x); x += dpp<ROR2>(x); x += dpp<ROR1>(x); return x; }
__device__ __forceinline__ float row_max(float x) { x = fmaxf(x, dpp<ROR8>(x)); x = fmaxf(x, dpp<ROR4>(x)); x = fmaxf(x, dpp<ROR2>(x)); x = fmaxf(x, dpp<ROR1>(x)); return x; }
__device__ __forceinline__ float oct_sum(float x) { x += dpp<XOR1>(x); x += dpp<XOR2>(x); x += dpp<HMIR>(x); return x; }
__device__ __forceinline__ float oct_max(float x) { x = fmaxf(x, dpp<XOR1>(x)); x = fmaxf(x, dpp<XOR2>(x)); x = fmaxf(x, dpp<HMIR>(x)); return x; }
#define LDS_BARRIER() asm volatile("s_waitcnt lgkmcnt(0)\n\ts_barrier" ::: "memory")
#define LDS_WAIT() asm volatile("s_waitcnt lgkmcnt(0)" ::: "memory")
#define MEMFENCE() asm volatile("" ::: "memory")
constexpr int AUX_SC1 = 16;

__device__ __forceinline__ u32x4 pack4(f32x4 v, unsigned tA, unsigned tB) {
  const unsigned t0 = f2u(v[0]) + 0x800u, t1 = f2u(v[1]) + 0x800u, t2 = f2u(v[2]) + 0x800u, t3 = f2u(v[3]) + 0x800u;
  return u32x4{tA | ((t0 << 12) & 0xff000000u), (t0 >> 20) | (t1 & 0xfffff000u), (t2 >> 12) | ((t3 << 8) & 0xfff00000u), (t3 >> 24) | (tB << 8)};
}
__device__ __forceinline__ f32x4 unpack4(u32x4 d) {
  return f32x4{u2f((d[1] << 20) | ((d[0] >> 12) & 0xff000u)), u2f(d[1] & 0xfffff000u), u2f(d[2] << 12), u2f(((d[2] >> 8) & 0xfff000u) | (d[3] << 24))};
}
__device__ __forceinline__ bool tags4(u32x4 d, unsigned tA, unsigned tB) { return (d[0] & 0xffffffu) == tA && (d[3] >> 8) == tB; }
__device__ __forceinline__ f32x4 red8_16_32(f32x4 v) {
#pragma unroll
  for (int e = 0; e < 4; ++e) { float x = v[e]; x += dpp<ROR8>(x); v[e] = swap32_sum(swap16_sum(x)); }
  return v;
}

struct Args {
  const unsigned char *xA, *kA, *vA, *wqA, *woA, *xB, *kB, *vB, *wqB, *woB;
  unsigned short* out;
  unsigned char* ws;
};

__device__ __forceinline__ unsigned lds_ld(LAS unsigned char* L, int off) { return __builtin_amdgcn_readfirstlane(*(volatile LAS unsigned*)(L + off)); }
__device__ __forceinline__ void lds_st(LAS unsigned char* L, int off, unsigned v) { *(volatile LAS unsigned*)(L + off) = v; }
template <int SLEEP> __device__ __forceinline__ void lds_wait_ge(LAS unsigned char* L, int off, unsigned need) {
  for (unsigned spin = 0; lds_ld(L, off) < need && spin < LSPIN_CAP; ++spin) { if (SLEEP) __builtin_amdgcn_s_sleep(SLEEP); }
  MEMFENCE();
}
__device__ __forceinline__ void lds_arrive(LAS unsigned char* L, int off, int lane) {
  LDS_WAIT();
  if (lane == 0) __hip_atomic_fetch_add((LAS unsigned*)(L + off), 1u, __ATOMIC_RELAXED, __HIP_MEMORY_SCOPE_WORKGROUP);
}

__device__ __forceinline__ void dma1(unsigned dst, unsigned voff, const unsigned char* base) {
  unsigned keep;
  asm volatile("s_mov_b32 %0, m0\n\ts_mov_b32 m0, %1\n\ts_nop 0\n\tglobal_load_lds_dwordx4 %2, %3 nt\n\ts_mov_b32 m0, %0\n\ts_waitcnt vmcnt(%4)"
               : "=&s"(keep) : "s"(dst), "v"(voff), "s"(base), "i"(WSTART) : "memory");
}
__device__ __forceinline__ void dma4d(unsigned dst, unsigned v0, unsigned v1, unsigned v2, unsigned v3, const unsigned char* base) {
  unsigned keep;
  asm volatile("s_mov_b32 %0, m0\n\ts_mov_b32 m0, %1\n\ts_nop 0\n\t"
               "global_load_lds_dwordx4 %2, %6\n\ts_waitcnt vmcnt(%7)\n\t"
               "global_load_lds_dwordx4 %3, %6 offset:1024\n\ts_waitcnt vmcnt(%7)\n\t"
               "global_load_lds_dwordx4 %4, %6 offset:2048\n\ts_waitcnt vmcnt(%7)\n\t"
               "global_load_lds_dwordx4 %5, %6 offset:3072\n\ts_waitcnt vmcnt(%7)\n\t"
               "s_mov_b32 m0, %0"
               : "=&s"(keep) : "s"(dst), "v"(v0), "v"(v1), "v"(v2), "v"(v3), "s"(base), "i"(WIN_LD) : "memory");
}
template <int WL> __device__ __forceinline__ void dma4w(unsigned dst, unsigned v0, unsigned v1, unsigned v2, unsigned v3, const unsigned char* base) {
  unsigned keep;
  asm volatile("s_mov_b32 %0, m0\n\ts_mov_b32 m0, %1\n\ts_waitcnt vmcnt(%7)\n\t"
               "global_load_lds_dwordx4 %2, %6 nt\n\ts_waitcnt vmcnt(%7)\n\t"
               "global_load_lds_dwordx4 %3, %6 offset:1024 nt\n\ts_waitcnt vmcnt(%7)\n\t"
               "global_load_lds_dwordx4 %4, %6 offset:2048 nt\n\ts_waitcnt vmcnt(%7)\n\t"
               "global_load_lds_dwordx4 %5, %6 offset:3072 nt\n\t"
               "s_mov_b32 m0, %0"
               : "=&s"(keep) : "s"(dst), "v"(v0), "v"(v1), "v"(v2), "v"(v3), "s"(base), "i"(WL) : "memory");
}
template <int N> __device__ __forceinline__ void vm_wait() { asm volatile("s_waitcnt vmcnt(%0)" :: "i"(N) : "memory"); }

__device__ __forceinline__ u32x2 tile_scores(const u32x4* K, const u32x4 (&q)[4]) {
  f32x4 s = {0.f, 0.f, 0.f, 0.f};
#pragma unroll
  for (int i = 0; i < 4; ++i) s = __builtin_amdgcn_mfma_f32_16x16x32_bf16(__builtin_bit_cast(bf16x8, K[i]), __builtin_bit_cast(bf16x8, q[i]), s, 0, 0, 0);
  const unsigned r01 = cvtpk(s[0], s[1]), r23 = cvtpk(s[2], s[3]);
  return u32x2{cvtpk(lo16(r01) * SCALE, hi16(r01) * SCALE), cvtpk(lo16(r23) * SCALE, hi16(r23) * SCALE)};
}
__device__ __forceinline__ float max4(u32x2 t) { return fmaxf(fmaxf(lo16(t[0]), hi16(t[0])), fmaxf(lo16(t[1]), hi16(t[1]))); }
__device__ __forceinline__ u32x2 to_probs(u32x2 t, float m2, float& l) {
  const float p0 = __builtin_amdgcn_exp2f(fmaf(lo16(t[0]), LOG2E, -m2)), p1 = __builtin_amdgcn_exp2f(fmaf(hi16(t[0]), LOG2E, -m2));
  const float p2 = __builtin_amdgcn_exp2f(fmaf(lo16(t[1]), LOG2E, -m2)), p3 = __builtin_amdgcn_exp2f(fmaf(hi16(t[1]), LOG2E, -m2));
  l += (p0 + p1) + (p2 + p3);
  return u32x2{cvtpk(p0, p1), cvtpk(p2, p3)};
}
struct Sel { unsigned l0, l1, h0, h1; };
__device__ __forceinline__ void tile_pv(f32x4& a0, f32x4& a1, const u32x4* V, u32x2 p, const Sel& s) {
#pragma unroll
  for (int r = 0; r < 4; ++r) {
    const unsigned pk = p[r >> 1];
    const u32x2 a = {__builtin_amdgcn_perm(0u, pk, (r & 1) ? s.h0 : s.l0), __builtin_amdgcn_perm(0u, pk, (r & 1) ? s.h1 : s.l1)};
    const u32x2 v0 = {V[r][0], V[r][1]}, v1 = {V[r][2], V[r][3]};
    a0 = __builtin_amdgcn_mfma_f32_16x16x16bf16_1k(__builtin_bit_cast(s16x4, a), __builtin_bit_cast(s16x4, v0), a0, 0, 0, 0);
    a1 = __builtin_amdgcn_mfma_f32_16x16x16bf16_1k(__builtin_bit_cast(s16x4, a), __builtin_bit_cast(s16x4, v1), a1, 0, 0, 0);
  }
}

__global__ void __launch_bounds__(NTHR, 3) fused_decode(Args a) {
  __shared__ __attribute__((aligned(16))) unsigned char lds_raw[L_END];
  LAS unsigned char* lds = (LAS unsigned char*)lds_raw;
  const int b = blockIdx.x, h = b & 7, j = b >> 3;
  const int tid = threadIdx.x, lane = tid & 63, w = __builtin_amdgcn_readfirstlane(tid >> 6);
  const int hh = lane >> 4, i16 = lane & 15, jj = lane & 3;
  const int qcb = j >> 1, qrh = j & 1, qbr = qcb >> 3;

  if (w == NC) {
    __builtin_amdgcn_s_setprio(3);
    const unsigned ring = (unsigned)(size_t)(lds + L_RING);
    int pub = 0, pslot = 0, slot = 0;
    auto publish1 = [&]() { lds_st(lds, L_FULL + 4 * pslot, (unsigned)(pub + 1)); ++pub; pslot = pslot + 1 == NS ? 0 : pslot + 1; };
    auto pre = [&](int q) {
      if (q >= NS) {
        const unsigned need = (unsigned)(q - NS + 1);
        if (lds_ld(lds, L_FREE + 4 * slot) < need) {
          vm_wait<0>();
          while (pub < q) publish1();
          lds_wait_ge<1>(lds, L_FREE + 4 * slot, need);
        }
      }
    };
    auto post = [&](int q) {
      while (q - pub >= LAG) publish1();
      slot = slot + 1 == NS ? 0 : slot + 1;
    };
    {
      const unsigned char* wq = (qbr ? a.wqB : a.wqA) + (size_t)(h * 512 + 64 * (qcb & 7)) * 2 + (size_t)qrh * 256 * 8192;
      const unsigned vo = (lane >> 3) * 8192 + (lane & 7) * 16;
#pragma unroll
      for (int q = 0; q < 4; ++q) {
#pragma unroll
        for (int m = 0; m < 8; ++m) dma1(ring + q * SLOT + m * 1024, vo, wq + (size_t)(q * 64 + m * 8) * 8192);
        if (q == 0) LDS_BARRIER();
        while (q - pub >= (WSTART + 7) / 8) publish1();
        slot = slot + 1 == NS ? 0 : slot + 1;
      }
    }
    const unsigned k0 = hh * 256 + ((i16 ^ (0 + hh)) * 16), k1 = hh * 256 + ((i16 ^ (4 + hh)) * 16), k2 = hh * 256 + ((i16 ^ (8 + hh)) * 16), k3 = hh * 256 + ((i16 ^ (12 + hh)) * 16);
    const unsigned vl = lane * 16;
    {
      const unsigned char* kp = a.kB + ((size_t)h * SK + (SK - WIN) + j * 128) * 256;
      const unsigned char* vp = a.vB + ((size_t)h * SK + (SK - WIN) + j * 128) * 256;
      for (int q = 4; q < 12; ++q) {
        dma4d(ring + slot * SLOT, k0, k1, k2, k3, kp);
        dma4d(ring + slot * SLOT + 4096, vl, vl, vl, vl, vp);
        kp += 4096; vp += 4096;
        post(q);
      }
      kp = a.kA + ((size_t)h * SK + j * 2048) * 256;
      unsigned o0 = k0, o1 = k1, o2 = k2, o3 = k3;
      unsigned frv = *(volatile LAS unsigned*)(lds + L_FREE + 4 * slot);
      for (int q = 12; q < 140; ++q) {
        if (q == 76) { kp = a.vA + ((size_t)h * SK + j * 2048) * 256; o0 = vl; o1 = vl; o2 = vl; o3 = vl; }
        if (q >= NS && (unsigned)__builtin_amdgcn_readfirstlane(frv) < (unsigned)(q - NS + 1)) pre(q);
        const int nslot = slot + 1 == NS ? 0 : slot + 1;
        frv = *(volatile LAS unsigned*)(lds + L_FREE + 4 * nslot);
        dma4w<WIN_LD>(ring + slot * SLOT, o0, o1, o2, o3, kp);
        dma4w<WIN_LD>(ring + slot * SLOT + 4096, o0, o1, o2, o3, kp + 4096);
        while (q - pub >= LAG2) publish1();
        kp += 8192;
        slot = nslot;
      }
      vp = (j >> 4 ? a.woB : a.woA) + (size_t)(h * 512 + 32 * (j & 15)) * 1024;
      for (int q = 140; q < 144; ++q) {
        pre(q);
        dma4w<WIN_LD>(ring + slot * SLOT, vl, vl, vl, vl, vp); dma4w<WIN_LD>(ring + slot * SLOT + 4096, vl, vl, vl, vl, vp + 4096);
        vp += 8192;
        while (q - pub >= LAG2) publish1();
        slot = slot + 1 == NS ? 0 : slot + 1;
      }
    }
    if (NSEQ - pub > 7) { vm_wait<56>(); publish1(); }
    if (NSEQ - pub > 6) { vm_wait<48>(); publish1(); }
    if (NSEQ - pub > 5) { vm_wait<40>(); publish1(); }
    if (NSEQ - pub > 4) { vm_wait<32>(); publish1(); }
    __builtin_amdgcn_s_barrier();
    if (NSEQ - pub > 3) { vm_wait<24>(); publish1(); }
    if (NSEQ - pub > 2) { vm_wait<16>(); publish1(); }
    if (NSEQ - pub > 1) { vm_wait<8>(); publish1(); }
    vm_wait<0>();
    while (pub < NSEQ) publish1();
    return;
  }

  const __amdgpu_buffer_rsrc_t rs = __builtin_amdgcn_make_buffer_rsrc((void*)a.ws, 0, (int)WS_END, 0x00020000);
  const u32x2 gen = __builtin_amdgcn_raw_buffer_load_b64(rs, WS_GEN, 0, AUX_SC1);
  unsigned short xv[4];
  {
    const GAS unsigned char* xp = (const GAS unsigned char*)(qbr ? a.xB : a.xA) + 2 * (qrh * 256 + w * 32 + (lane >> 3));
#pragma unroll
    for (int i = 0; i < 4; ++i) xv[i] = *(const GAS unsigned short*)(xp + 16 * i);
  }
  if (tid < (L_RED - L_FULL) / 4) *(LAS unsigned*)(lds + L_FULL + 4 * tid) = 0u;
  LDS_BARRIER();

  lds_wait_ge<1>(lds, L_FULL + 4 * (w >> 1), (unsigned)((w >> 1) + 1));
  {
    u32x4 wv[4];
#pragma unroll
    for (int i = 0; i < 4; ++i) wv[i] = *(const LAS u32x4*)(lds + L_RING + (w >> 1) * SLOT + (w & 1) * 4096 + i * 1024 + lane * 16);
    float acc[8];
#pragma unroll
    for (int k = 0; k < 8; ++k) acc[k] = 0.f;
#pragma unroll
    for (int i = 0; i < 4; ++i) {
      const float x = lo16(xv[i]);
#pragma unroll
      for (int k = 0; k < 4; ++k) { acc[2 * k] = fmaf(x, lo16(wv[i][k]), acc[2 * k]); acc[2 * k + 1] = fmaf(x, hi16(wv[i][k]), acc[2 * k + 1]); }
    }
#pragma unroll
    for (int k = 0; k < 8; ++k) { float v = acc[k]; v += dpp<ROR8>(v); v = swap16_sum(v); v = swap32_sum(v); acc[k] = v; }
    if (lane < 8) {
      LAS f32x4* red = (LAS f32x4*)(lds + L_RED + w * 256 + lane * 32);
      red[0] = f32x4{acc[0], acc[1], acc[2], acc[3]}; red[1] = f32x4{acc[4], acc[5], acc[6], acc[7]};
    }
  }
  lds_arrive(lds, L_CNT + C_QRED, lane);
  const unsigned tagA = __builtin_amdgcn_readfirstlane(gen[0]) + 1u, tagB = __builtin_amdgcn_readfirstlane(gen[1]) + 1u;
  if (w == 0) {
    lds_wait_ge<0>(lds, L_CNT + C_QRED, NC);
    if (lane < 4) lds_st(lds, L_FREE + 4 * lane, (unsigned)(lane + 1));
    if (lane < 32) {
      f32x2 s = {0.f, 0.f};
#pragma unroll
      for (int ww = 0; ww < 8; ++ww) s += *(const LAS f32x2*)(lds + L_RED + ww * 256 + lane * 8);
      __builtin_amdgcn_raw_buffer_store_b128(u32x4{tagA, f2u(s[0]), f2u(s[1]), tagB}, rs, WS_QG + ((h * 32 + j) * 32 + lane) * 16, 0, AUX_SC1);
    }
  }
  int slot = (4 + w) % NS;
  const unsigned kr0 = i16 * 256 + (((0 + hh) ^ i16) * 16), kr1 = i16 * 256 + (((4 + hh) ^ i16) * 16), kr2 = i16 * 256 + (((8 + hh) ^ i16) * 16), kr3 = i16 * 256 + (((12 + hh) ^ i16) * 16);
  const unsigned vr = hh * 1024 + i16 * 16;
  auto fetch = [&](int it, int kind, u32x4 (&T)[8]) {
    const unsigned seq1 = (unsigned)(4 + 8 * it + w + 1);
    lds_wait_ge<1>(lds, L_FULL + 4 * slot, seq1);
    const LAS unsigned char* sp = lds + L_RING + slot * SLOT;
    if (kind != 2) { T[0] = *(const LAS u32x4*)(sp + kr0); T[1] = *(const LAS u32x4*)(sp + kr1); T[2] = *(const LAS u32x4*)(sp + kr2); T[3] = *(const LAS u32x4*)(sp + kr3); }
    else {
#pragma unroll
      for (int r = 0; r < 4; ++r) T[r] = *(const LAS u32x4*)(sp + vr + r * 256);
    }
    if (kind == 1) { T[4] = *(const LAS u32x4*)(sp + 4096 + kr0); T[5] = *(const LAS u32x4*)(sp + 4096 + kr1); T[6] = *(const LAS u32x4*)(sp + 4096 + kr2); T[7] = *(const LAS u32x4*)(sp + 4096 + kr3); }
    else {
#pragma unroll
      for (int r = 0; r < 4; ++r) T[4 + r] = *(const LAS u32x4*)(sp + 4096 + vr + r * 256);
    }
    LDS_WAIT();
    lds_st(lds, L_FREE + 4 * slot, seq1);
    asm volatile("" : "+v"(T[0]), "+v"(T[1]), "+v"(T[2]), "+v"(T[3]), "+v"(T[4]), "+v"(T[5]), "+v"(T[6]), "+v"(T[7]) :: "memory");
    slot += 8; if (slot >= NS) slot -= NS;
  };
  u32x4 X0[8], X1[8], X2[8];
  fetch(0, 0, X0);
  fetch(1, 1, X1);
  fetch(2, 1, X2);
  {
    const unsigned off = WS_QG + ((h * 32 + 2 * (tid >> 5)) * 32 + (tid & 31)) * 16;
    u32x4 g0 = {0u, 0u, 0u, 0u}, g1 = {0u, 0u, 0u, 0u};
    bool ok = false;
    for (unsigned spin = 0; spin < SPIN_CAP; ++spin) {
      MEMFENCE();
      if (!ok) {
        g0 = __builtin_amdgcn_raw_buffer_load_b128(rs, off, 0, AUX_SC1);
        g1 = __builtin_amdgcn_raw_buffer_load_b128(rs, off + 512, 0, AUX_SC1);
        ok = g0[0] == tagA && g0[3] == tagB && g1[0] == tagA && g1[3] == tagB;
      }
      if (__all(ok)) break;
      __builtin_amdgcn_s_sleep(QSLEEP);
    }
    *(LAS unsigned*)(lds + L_Q + tid * 4) = cvtpk(u2f(g0[1]) + u2f(g1[1]), u2f(g0[2]) + u2f(g1[2]));
  }
  lds_arrive(lds, L_CNT + C_QIN, lane);
  lds_wait_ge<0>(lds, L_CNT + C_QIN, NC);
  u32x4 qA[4], qB[4];
#pragma unroll
  for (int s = 0; s < 4; ++s) {
    qA[s] = *(const LAS u32x4*)(lds + L_Q + ((i16 >> 2) * 128 + 32 * s + 8 * hh) * 2);
    qB[s] = *(const LAS u32x4*)(lds + L_Q + 1024 + ((i16 >> 2) * 128 + 32 * s + 8 * hh) * 2);
  }

  Sel sel;
  sel.l0 = jj == 0 ? 0x0c0c0100u : jj == 1 ? 0x01000c0cu : 0x0c0c0c0cu;  sel.h0 = jj == 0 ? 0x0c0c0302u : jj == 1 ? 0x03020c0cu : 0x0c0c0c0cu;
  sel.l1 = jj == 2 ? 0x0c0c0100u : jj == 3 ? 0x01000c0cu : 0x0c0c0c0cu;  sel.h1 = jj == 2 ? 0x0c0c0302u : jj == 3 ? 0x03020c0cu : 0x0c0c0c0cu;

  f32x4 b0 = {0.f, 0.f, 0.f, 0.f}, b1 = {0.f, 0.f, 0.f, 0.f};
  {
    const u32x2 t = tile_scores(X0, qB);
    const float m2 = swap32_max(swap16_max(max4(t))) * LOG2E;
    float l = 0.f;
    const u32x2 p = to_probs(t, m2, l);
    l = swap32_sum(swap16_sum(l));
    tile_pv(b0, b1, X0 + 4, p, sel);
    if (hh == 0 && jj == 0) *(LAS f32x2*)(lds + L_TB + ((i16 >> 2) * 8 + w) * 8) = f32x2{m2, l};
  }

  u32x2 P[16];
  float mx = -INFINITY;
#pragma unroll
  for (int i = 0; i < 8; ++i) {
    if (i >= 2) fetch(i + 1, 1, X1);
    const u32x4* T = i == 1 ? X2 : X1;
    P[2 * i] = tile_scores(T, qA);
    P[2 * i + 1] = tile_scores(T + 4, qA);
    mx = fmaxf(mx, fmaxf(max4(P[2 * i]), max4(P[2 * i + 1])));
  }
  mx = swap32_max(swap16_max(mx));
  if (hh == 0 && jj == 0) *(LAS float*)(lds + L_T1 + ((i16 >> 2) * 8 + w) * 4) = mx;
  lds_arrive(lds, L_CNT + C_MAX, lane);
  lds_wait_ge<0>(lds, L_CNT + C_MAX, NC);
  {
    const f32x4 m0 = *(const LAS f32x4*)(lds + L_T1 + (i16 >> 2) * 32), m1 = *(const LAS f32x4*)(lds + L_T1 + (i16 >> 2) * 32 + 16);
    const float m2 = fmaxf(fmaxf(fmaxf(m0[0], m0[1]), fmaxf(m0[2], m0[3])), fmaxf(fmaxf(m1[0], m1[1]), fmaxf(m1[2], m1[3]))) * LOG2E;
    float l = 0.f;
#pragma unroll
    for (int t = 0; t < 16; ++t) P[t] = to_probs(P[t], m2, l);
    l = swap32_sum(swap16_sum(l));
    if (hh == 0 && jj == 0) *(LAS float*)(lds + L_T2 + ((i16 >> 2) * 8 + w) * 4) = l;
  }
  lds_arrive(lds, L_CNT + C_SUM, lane);
  lds_wait_ge<0>(lds, L_CNT + C_SUM, NC);
  const int mbr = w >> 2, mg = w & 3;
  {
    const int ww = lane & 7;
    float M, L;
    if (mbr == 0) {
      M = oct_max(*(const LAS float*)(lds + L_T1 + (mg * 8 + ww) * 4)) * LOG2E;
      L = oct_sum(*(const LAS float*)(lds + L_T2 + (mg * 8 + ww) * 4));
    } else {
      const f32x2 ml = *(const LAS f32x2*)(lds + L_TB + (mg * 8 + ww) * 8);
      M = oct_max(ml[0]);
      L = oct_sum(ml[1] * __builtin_amdgcn_exp2f(ml[0] - M));
    }
    if (lane == 0) __builtin_amdgcn_raw_buffer_store_b128(u32x4{tagA, f2u(M), f2u(L), tagB}, rs, WS_ML + (((h * 2 + mbr) * 4 + mg) * 32 + j) * 16, 0, AUX_SC1);
  }

  f32x4 a0 = {0.f, 0.f, 0.f, 0.f}, a1 = {0.f, 0.f, 0.f, 0.f};
  float fA = 0.f, fB = 0.f;
#pragma unroll
  for (int i = 0; i < 8; ++i) {
    if (i == 4) {
      const unsigned off = WS_ML + (((h * 2 + mbr) * 4 + mg) * 32 + (lane & 31)) * 16;
      u32x4 mv = {0u, 0u, 0u, 0u};
      bool ok = false;
      for (unsigned spin = 0; spin < SPIN_CAP; ++spin) {
        MEMFENCE();
        if (!ok) { mv = __builtin_amdgcn_raw_buffer_load_b128(rs, off, 0, AUX_SC1); ok = mv[0] == tagA && mv[3] == tagB; }
        if (__all(ok)) break;
        __builtin_amdgcn_s_sleep(16);
      }
      const float M = swap16_max(row_max(u2f(mv[1])));
      const float L = swap16_sum(row_sum(u2f(mv[2]) * __builtin_amdgcn_exp2f(u2f(mv[1]) - M)));
      if (lane == 0) *(LAS f32x2*)(lds + L_FF + w * 8) = f32x2{M, 1.0f / L};
      lds_arrive(lds, L_CNT + C_FF, lane);
    }
    if (i == 7) {
      lds_wait_ge<0>(lds, L_CNT + C_FF, NC);
      const f32x4 m0 = *(const LAS f32x4*)(lds + L_T1 + hh * 32), m1 = *(const LAS f32x4*)(lds + L_T1 + hh * 32 + 16);
      const float m2 = fmaxf(fmaxf(fmaxf(m0[0], m0[1]), fmaxf(m0[2], m0[3])), fmaxf(fmaxf(m1[0], m1[1]), fmaxf(m1[2], m1[3]))) * LOG2E;
      const f32x2 FA = *(const LAS f32x2*)(lds + L_FF + hh * 8), FB = *(const LAS f32x2*)(lds + L_FF + (4 + hh) * 8);
      const float mB = *(const LAS float*)(lds + L_TB + (hh * 8 + w) * 8);
      fA = __builtin_amdgcn_exp2f(m2 - FA[0]) * FA[1];
      fB = __builtin_amdgcn_exp2f(mB - FB[0]) * FB[1];
      const int s = lane < MRG_SLOTS ? lane : 0;
      const unsigned need = (unsigned)(s + NS * ((139 - s) / NS) + 1);
      for (unsigned spin = 0; spin < LSPIN_CAP; ++spin) { MEMFENCE(); if (__all(*(volatile LAS unsigned*)(lds + L_FREE + 4 * s) >= need)) break; }
      MEMFENCE();
      LAS float* rB = (LAS float*)(lds + L_MRG) + ((8 + w) * 4 + hh) * MREC + 8 * i16;
      *(LAS f32x4*)(rB) = b0 * fB; *(LAS f32x4*)(rB + 4) = b1 * fB;
    }
    fetch(9 + i, 2, X1);
    tile_pv(a0, a1, X1, P[2 * i], sel);
    tile_pv(a0, a1, X1 + 4, P[2 * i + 1], sel);
  }

  const unsigned tA = tagA & 0xffffffu, tB = tagB & 0xffffffu;
  {
    LAS float* rA = (LAS float*)(lds + L_MRG) + (w * 4 + hh) * MREC + 8 * i16;
    *(LAS f32x4*)(rA) = a0 * fA; *(LAS f32x4*)(rA + 4) = a1 * fA;
  }
  LDS_BARRIER();
  if (tid < 256) {
    const int br = tid >> 7, g = (tid >> 5) & 3, dq = tid & 31;
    const LAS float* mrg = (const LAS float*)(lds + L_MRG) + (br * 8 * 4 + g) * MREC + 4 * dq;
    f32x4 o4[8];
#pragma unroll
    for (int ww = 0; ww < 8; ++ww) o4[ww] = *(const LAS f32x4*)(mrg + ww * 4 * MREC);
    const f32x4 o = ((o4[0] + o4[1]) + (o4[2] + o4[3])) + ((o4[4] + o4[5]) + (o4[6] + o4[7]));
    const int jc = br * 16 + g * 4 + (dq >> 3), k = dq & 7;
    __builtin_amdgcn_raw_buffer_store_b128(pack4(o, tA, tB), rs, WS_I1 + (((h * 32 + jc) * 32 + j) * 8 + k) * 16, 0, AUX_SC1);
  }

  const unsigned offA = WS_I1 + ((h * 32 + j) * 32 * 8 + (w & 3) * 64 + lane) * 16;
  u32x4 av = {0u, 0u, 0u, 0u};
  if (w >= 4) av = __builtin_amdgcn_raw_buffer_load_b128(rs, offA, 0, AUX_SC1);
  MEMFENCE();
  float W[32];
  lds_wait_ge<0>(lds, L_FULL + 4 * (143 % NS), 144u);
  {
    const LAS unsigned short* wp = (const LAS unsigned short*)(lds + L_RING + (140 % NS) * SLOT) + 64 * w + lane;
#pragma unroll
    for (int r = 0; r < 32; ++r) W[r] = u2f((unsigned)wp[r * 512] << 16);
#pragma unroll
    for (int r = 0; r < 32; r += 8) asm volatile("" : "+v"(W[r]), "+v"(W[r + 1]), "+v"(W[r + 2]), "+v"(W[r + 3]), "+v"(W[r + 4]), "+v"(W[r + 5]), "+v"(W[r + 6]), "+v"(W[r + 7]));
  }
  if (w >= 4) {
    bool ok = tags4(av, tA, tB);
    for (unsigned spin = 0; spin < SPIN_CAP && !__all(ok); ++spin) {
      MEMFENCE();
      if (!ok) { av = __builtin_amdgcn_raw_buffer_load_b128(rs, offA, 0, AUX_SC1); ok = tags4(av, tA, tB); }
    }
    const f32x4 o = red8_16_32(unpack4(av));
    if (lane < 8) {
#pragma unroll
      for (int e = 0; e < 4; ++e) *(LAS float*)(lds + L_H1 + ((4 * lane + e) * 4 + (w & 3)) * 4) = o[e];
    }
  }
  LDS_BARRIER();
  {
    const f32x4 ra = *(const LAS f32x4*)(lds + L_H1 + i16 * 16), rb = *(const LAS f32x4*)(lds + L_H1 + (16 + i16) * 16);
    const unsigned ob = cvtpk((ra[0] + ra[1]) + (ra[2] + ra[3]), (rb[0] + rb[1]) + (rb[2] + rb[3]));
    const float oa = lo16(ob), oc = hi16(ob);
    float y0 = 0.f, y1 = 0.f, y2 = 0.f, y3 = 0.f;
#define FM(acc, o, wv, n) "v_fmac_f32_dpp " acc ", " o ", " wv " row_newbcast:" #n " row_mask:0xf bank_mask:0xf\n\t"
#define ROWS(n0, n1, n2, n3) asm volatile("s_nop 1\n\t" \
      FM("%0", "%4", "%6", n0) FM("%1", "%5", "%7", n0) FM("%2", "%4", "%8", n1) FM("%3", "%5", "%9", n1) \
      FM("%0", "%4", "%10", n2) FM("%1", "%5", "%11", n2) FM("%2", "%4", "%12", n3) FM("%3", "%5", "%13", n3) \
      : "+v"(y0), "+v"(y1), "+v"(y2), "+v"(y3) \
      : "v"(oa), "v"(oc), "v"(W[n0]), "v"(W[16 + n0]), "v"(W[n1]), "v"(W[16 + n1]), "v"(W[n2]), "v"(W[16 + n2]), "v"(W[n3]), "v"(W[16 + n3]));
    ROWS(0, 1, 2, 3) ROWS(4, 5, 6, 7) ROWS(8, 9, 10, 11) ROWS(12, 13, 14, 15)
#undef ROWS
#undef FM
    const float y = (y0 + y1) + (y2 + y3);
    const f32x4 y4 = {y, dpp<0x55>(y), dpp<0xAA>(y), dpp<0xFF>(y)};
    const int c = (j >> 4) * 16 + 2 * w + (lane >> 5), k = (lane & 31) >> 2, pidx = h * 16 + (j & 15);
    if (jj == 0) __builtin_amdgcn_raw_buffer_store_b128(pack4(y4, tA, tB), rs, WS_I2 + ((c * 128 + pidx) * 8 + k) * 16, 0, AUX_SC1);
  }

  if (j < 4) {
    const int c = h * 4 + j;
    const unsigned off = WS_I2 + (c * 128 + 16 * w) * 128 + lane * 16;
    u32x4 v[2] = {{0u, 0u, 0u, 0u}, {0u, 0u, 0u, 0u}};
    bool ok = false;
    for (unsigned spin = 0; spin < SPIN_CAP; ++spin) {
      MEMFENCE();
      if (!ok) {
#pragma unroll
        for (int i = 0; i < 2; ++i) v[i] = __builtin_amdgcn_raw_buffer_load_b128(rs, off + i * 1024, 0, AUX_SC1);
        ok = tags4(v[0], tA, tB) && tags4(v[1], tA, tB);
      }
      if (__all(ok)) break;
    }
    const f32x4 s4 = red8_16_32(unpack4(v[0]) + unpack4(v[1]));
    if (lane < 8) {
#pragma unroll
      for (int e = 0; e < 4; ++e) *(LAS float*)(lds + L_F + ((4 * lane + e) * 8 + w) * 4) = s4[e];
    }
    LDS_BARRIER();
    if (tid < 32) {
      const f32x4 p0 = *(const LAS f32x4*)(lds + L_F + tid * 32), p1 = *(const LAS f32x4*)(lds + L_F + tid * 32 + 16);
      const float s = ((p0[0] + p0[1]) + (p0[2] + p0[3])) + ((p1[0] + p1[1]) + (p1[2] + p1[3]));
      const unsigned pk = cvtpk(s, dpp<XOR1>(s));
      if ((tid & 1) == 0) *(GAS unsigned*)((GAS unsigned char*)a.out + (c * 32 + tid) * 2) = pk;
      if (c == 0 && tid == 0) __builtin_amdgcn_raw_buffer_store_b64(u32x2{tagA, tagB}, rs, WS_GEN, 0, 0);
    }
  }
}

extern "C" void kernel_launch(void* const* d_in, const int* in_sizes, int n_in, void* d_out, int out_size, void* d_ws, size_t ws_size, hipStream_t stream) {
  Args a;
  a.xA = (const unsigned char*)d_in[0]; a.kA = (const unsigned char*)d_in[1]; a.vA = (const unsigned char*)d_in[2]; a.wqA = (const unsigned char*)d_in[3]; a.woA = (const unsigned char*)d_in[4];
  a.xB = (const unsigned char*)d_in[5]; a.kB = (const unsigned char*)d_in[6]; a.vB = (const unsigned char*)d_in[7]; a.wqB = (const unsigned char*)d_in[8]; a.woB = (const unsigned char*)d_in[9];
  a.out = (unsigned short*)d_out;
  a.ws = (unsigned char*)d_ws;
  hipLaunchKernelGGL(fused_decode, dim3(NWG), dim3(NTHR), 0, stream, a);
}
```

```cpp
#include <hip/hip_runtime.h>
#include <hip/hip_bf16.h>
#include <stdint.h>

#define GAS __attribute__((address_space(1)))
#define LAS __attribute__((address_space(3)))
typedef unsigned u32x4 __attribute__((ext_vector_type(4)));
typedef unsigned u32x2 __attribute__((ext_vector_type(2)));
typedef float f32x4 __attribute__((ext_vector_type(4)));
typedef float f32x2 __attribute__((ext_vector_type(2)));
typedef __bf16 bf16x8 __attribute__((ext_vector_type(8)));
typedef __bf16 bf16x2 __attribute__((ext_vector_type(2)));
typedef short s16x4 __attribute__((ext_vector_type(4)));

#define WPOL ""
#define ITOUCH 3
constexpr int NS = 18;
constexpr int WIN_LD = 21;
constexpr int WSTART = 40;
constexpr int QSLEEP = 16;

constexpr int SK = 65536, WIN = 4096;
constexpr int NWG = 256, NC = 8, NTHR = (NC + 1) * 64;
constexpr int SLOT = 8192, NSEQ = 4 + 8 + 64 + 64 + 4;
constexpr int LAG = (WIN_LD + 7) / 8;
constexpr int LAG2 = (WIN_LD + 8) / 8;
constexpr float LOG2E = 1.4426950408889634f;
constexpr float SCALE = 0.08838834764831845f;
constexpr unsigned SPIN_CAP = 1u << 17, LSPIN_CAP = 1u << 23;

constexpr unsigned WS_GEN = 0;
constexpr unsigned WS_QG = 4096;
constexpr unsigned WS_ML = WS_QG + 8 * 32 * 32 * 16;
constexpr unsigned WS_I1 = WS_ML + 256 * 128;
constexpr unsigned WS_I2 = WS_I1 + 8 * 32 * 32 * 128;
constexpr unsigned WS_END = WS_I2 + 32 * 128 * 128;

constexpr int L_RING = 0;
constexpr int L_FULL = NS * SLOT;
constexpr int L_FREE = L_FULL + 128;
constexpr int L_CNT = L_FREE + 128;
constexpr int L_RED = L_CNT + 64;
constexpr int L_Q = L_RED + 2048;
constexpr int L_T1 = L_Q + 2048;
constexpr int L_T2 = L_T1 + 128;
constexpr int L_TB = L_T2 + 128;
constexpr int L_FF = L_TB + 256;
constexpr int L_H1 = L_FF + 64;
constexpr int L_F = L_H1 + 512;
constexpr int L_END = L_F + 1024;
constexpr int C_QRED = 0, C_QIN = 4, C_MAX = 8, C_SUM = 12, C_FF = 16;
constexpr int MREC = 132;
constexpr int L_MRG = 0;
constexpr int MRG_SLOTS = 5;
static_assert(L_END <= 160 * 1024, "LDS");
static_assert(2 * 8 * 4 * MREC * 4 <= MRG_SLOTS * SLOT, "merge area");
static_assert((140 % NS) >= MRG_SLOTS && (143 % NS) >= MRG_SLOTS && (140 % NS) <= (143 % NS), "wo slots contiguous and clear of the merge area");
static_assert((132 % NS) >= MRG_SLOTS && (139 % NS) >= MRG_SLOTS && (132 % NS) <= (139 % NS), "last round clear of the merge area");
static_assert(WIN_LD >= 9 && WIN_LD <= 48, "window");

__device__ __forceinline__ unsigned cvtpk(float lo, float hi) { f32x2 f = {lo, hi}; return __builtin_bit_cast(unsigned, __builtin_convertvector(f, bf16x2)); }
__device__ __forceinline__ float lo16(unsigned u) { return __builtin_bit_cast(float, u << 16); }
__device__ __forceinline__ float hi16(unsigned u) { return __builtin_bit_cast(float, u & 0xffff0000u); }
__device__ __forceinline__ float u2f(unsigned u) { return __builtin_bit_cast(float, u); }
__device__ __forceinline__ unsigned f2u(float f) { return __builtin_bit_cast(unsigned, f); }
template <int CTRL> __device__ __forceinline__ float dpp(float x) { return u2f(__builtin_amdgcn_mov_dpp(f2u(x), CTRL, 0xf, 0xf, true)); }
constexpr int XOR1 = 0xB1, XOR2 = 0x4E, HMIR = 0x141;
constexpr int ROR1 = 0x121, ROR2 = 0x122, ROR4 = 0x124, ROR8 = 0x128;
__device__ __forceinline__ float swap16_max(float x) { auto s = __builtin_amdgcn_permlane16_swap(f2u(x), f2u(x), false, false); return fmaxf(u2f(s[0]), u2f(s[1])); }
__device__ __forceinline__ float swap32_max(float x) { auto s = __builtin_amdgcn_permlane32_swap(f2u(x), f2u(x), false, false); return fmaxf(u2f(s[0]), u2f(s[1])); }
__device__ __forceinline__ float swap16_sum(float x) { auto s = __builtin_amdgcn_permlane16_swap(f2u(x), f2u(x), false, false); return u2f(s[0]) + u2f(s[1]); }
__device__ __forceinline__ float swap32_sum(float x) { auto s = __builtin_amdgcn_permlane32_swap(f2u(x), f2u(x), false, false); return u2f(s[0]) + u2f(s[1]); }
__device__ __forceinline__ float row_sum(float x) { x += dpp<ROR8>(x); x += dpp<ROR4>(x); x += dpp<ROR2>(x); x += dpp<ROR1>(x); return x; }
__device__ __forceinline__ float row_max(float x) { x = fmaxf(x, dpp<ROR8>(x)); x = fmaxf(x, dpp<ROR4>(x)); x = fmaxf(x, dpp<ROR2>(x)); x = fmaxf(x, dpp<ROR1>(x)); return x; }
__device__ __forceinline__ float oct_sum(float x) { x += dpp<XOR1>(x); x += dpp<XOR2>(x); x += dpp<HMIR>(x); return x; }
__device__ __forceinline__ float oct_max(float x) { x = fmaxf(x, dpp<XOR1>(x)); x = fmaxf(x, dpp<XOR2>(x)); x = fmaxf(x, dpp<HMIR>(x)); return x; }
#define LDS_BARRIER() asm volatile("s_waitcnt lgkmcnt(0)\n\ts_barrier" ::: "memory")
#define LDS_WAIT() asm volatile("s_waitcnt lgkmcnt(0)" ::: "memory")
#define MEMFENCE() asm volatile("" ::: "memory")
constexpr int AUX_SC1 = 16;

__device__ __forceinline__ u32x4 pack4(f32x4 v, unsigned tA, unsigned tB) {
  const unsigned t0 = f2u(v[0]) + 0x800u, t1 = f2u(v[1]) + 0x800u, t2 = f2u(v[2]) + 0x800u, t3 = f2u(v[3]) + 0x800u;
  return u32x4{tA | ((t0 << 12) & 0xff000000u), (t0 >> 20) | (t1 & 0xfffff000u), (t2 >> 12) | ((t3 << 8) & 0xfff00000u), (t3 >> 24) | (tB << 8)};
}
__device__ __forceinline__ f32x4 unpack4(u32x4 d) {
  return f32x4{u2f((d[1] << 20) | ((d[0] >> 12) & 0xff000u)), u2f(d[1] & 0xfffff000u), u2f(d[2] << 12), u2f(((d[2] >> 8) & 0xfff000u) | (d[3] << 24))};
}
__device__ __forceinline__ bool tags4(u32x4 d, unsigned tA, unsigned tB) { return (d[0] & 0xffffffu) == tA && (d[3] >> 8) == tB; }
__device__ __forceinline__ f32x4 red8_16_32(f32x4 v) {
#pragma unroll
  for (int e = 0; e < 4; ++e) { float x = v[e]; x += dpp<ROR8>(x); v[e] = swap32_sum(swap16_sum(x)); }
  return v;
}

struct Args {
  const unsigned char *xA, *kA, *vA, *wqA, *woA, *xB, *kB, *vB, *wqB, *woB;
  unsigned short* out;
  unsigned char* ws;
};

__device__ __forceinline__ unsigned lds_ld(LAS unsigned char* L, int off) { return __builtin_amdgcn_readfirstlane(*(volatile LAS unsigned*)(L + off)); }
__device__ __forceinline__ void lds_st(LAS unsigned char* L, int off, unsigned v) { *(volatile LAS unsigned*)(L + off) = v; }
template <int SLEEP> __device__ __forceinline__ void lds_wait_ge(LAS unsigned char* L, int off, unsigned need) {
  for (unsigned spin = 0; lds_ld(L, off) < need && spin < LSPIN_CAP; ++spin) { if (SLEEP) __builtin_amdgcn_s_sleep(SLEEP); }
  MEMFENCE();
}
__device__ __forceinline__ void lds_arrive(LAS unsigned char* L, int off, int lane) {
  LDS_WAIT();
  if (lane == 0) __hip_atomic_fetch_add((LAS unsigned*)(L + off), 1u, __ATOMIC_RELAXED, __HIP_MEMORY_SCOPE_WORKGROUP);
}

__device__ __forceinline__ void dma1(unsigned dst, unsigned voff, const unsigned char* base) {
  unsigned keep;
  asm volatile("s_mov_b32 %0, m0\n\ts_mov_b32 m0, %1\n\ts_nop 0\n\tglobal_load_lds_dwordx4 %2, %3 nt\n\ts_mov_b32 m0, %0\n\ts_waitcnt vmcnt(%4)"
               : "=&s"(keep) : "s"(dst), "v"(voff), "s"(base), "i"(WSTART) : "memory");
}
__device__ __forceinline__ void dma4d(unsigned dst, unsigned v0, unsigned v1, unsigned v2, unsigned v3, const unsigned char* base) {
  unsigned keep;
  asm volatile("s_mov_b32 %0, m0\n\ts_mov_b32 m0, %1\n\ts_nop 0\n\t"
               "global_load_lds_dwordx4 %2, %6" WPOL "\n\ts_waitcnt vmcnt(%7)\n\t"
               "global_load_lds_dwordx4 %3, %6 offset:1024" WPOL "\n\ts_waitcnt vmcnt(%7)\n\t"
               "global_load_lds_dwordx4 %4, %6 offset:2048" WPOL "\n\ts_waitcnt vmcnt(%7)\n\t"
               "global_load_lds_dwordx4 %5, %6 offset:3072" WPOL "\n\ts_waitcnt vmcnt(%7)\n\t"
               "s_mov_b32 m0, %0"
               : "=&s"(keep) : "s"(dst), "v"(v0), "v"(v1), "v"(v2), "v"(v3), "s"(base), "i"(WIN_LD) : "memory");
}
template <int WL> __device__ __forceinline__ void dma4w(unsigned dst, unsigned v0, unsigned v1, unsigned v2, unsigned v3, const unsigned char* base) {
  unsigned keep;
  asm volatile("s_mov_b32 %0, m0\n\ts_mov_b32 m0, %1\n\ts_waitcnt vmcnt(%7)\n\t"
               "global_load_lds_dwordx4 %2, %6 nt\n\ts_waitcnt vmcnt(%7)\n\t"
               "global_load_lds_dwordx4 %3, %6 offset:1024 nt\n\ts_waitcnt vmcnt(%7)\n\t"
               "global_load_lds_dwordx4 %4, %6 offset:2048 nt\n\ts_waitcnt vmcnt(%7)\n\t"
               "global_load_lds_dwordx4 %5, %6 offset:3072 nt\n\t"
               "s_mov_b32 m0, %0"
               : "=&s"(keep) : "s"(dst), "v"(v0), "v"(v1), "v"(v2), "v"(v3), "s"(base), "i"(WL) : "memory");
}
template <int N> __device__ __forceinline__ void vm_wait() { asm volatile("s_waitcnt vmcnt(%0)" :: "i"(N) : "memory"); }

__device__ __forceinline__ u32x2 tile_scores(const u32x4* K, const u32x4 (&q)[4]) {
  f32x4 s = {0.f, 0.f, 0.f, 0.f};
#pragma unroll
  for (int i = 0; i < 4; ++i) s = __builtin_amdgcn_mfma_f32_16x16x32_bf16(__builtin_bit_cast(bf16x8, K[i]), __builtin_bit_cast(bf16x8, q[i]), s, 0, 0, 0);
  const unsigned r01 = cvtpk(s[0], s[1]), r23 = cvtpk(s[2], s[3]);
  return u32x2{cvtpk(lo16(r01) * SCALE, hi16(r01) * SCALE), cvtpk(lo16(r23) * SCALE, hi16(r23) * SCALE)};
}
__device__ __forceinline__ float max4(u32x2 t) { return fmaxf(fmaxf(lo16(t[0]), hi16(t[0])), fmaxf(lo16(t[1]), hi16(t[1]))); }
__device__ __forceinline__ u32x2 to_probs(u32x2 t, float m2, float& l) {
  const float p0 = __builtin_amdgcn_exp2f(fmaf(lo16(t[0]), LOG2E, -m2)), p1 = __builtin_amdgcn_exp2f(fmaf(hi16(t[0]), LOG2E, -m2));
  const float p2 = __builtin_amdgcn_exp2f(fmaf(lo16(t[1]), LOG2E, -m2)), p3 = __builtin_amdgcn_exp2f(fmaf(hi16(t[1]), LOG2E, -m2));
  l += (p0 + p1) + (p2 + p3);
  return u32x2{cvtpk(p0, p1), cvtpk(p2, p3)};
}
struct Sel { unsigned l0, l1, h0, h1; };
__device__ __forceinline__ void tile_pv(f32x4& a0, f32x4& a1, const u32x4* V, u32x2 p, const Sel& s) {
#pragma unroll
  for (int r = 0; r < 4; ++r) {
    const unsigned pk = p[r >> 1];
    const u32x2 a = {__builtin_amdgcn_perm(0u, pk, (r & 1) ? s.h0 : s.l0), __builtin_amdgcn_perm(0u, pk, (r & 1) ? s.h1 : s.l1)};
    const u32x2 v0 = {V[r][0], V[r][1]}, v1 = {V[r][2], V[r][3]};
    a0 = __builtin_amdgcn_mfma_f32_16x16x16bf16_1k(__builtin_bit_cast(s16x4, a), __builtin_bit_cast(s16x4, v0), a0, 0, 0, 0);
    a1 = __builtin_amdgcn_mfma_f32_16x16x16bf16_1k(__builtin_bit_cast(s16x4, a), __builtin_bit_cast(s16x4, v1), a1, 0, 0, 0);
  }
}

__global__ void __launch_bounds__(NTHR, 3) fused_decode(Args a) {
  __shared__ __attribute__((aligned(16))) unsigned char lds_raw[L_END];
  LAS unsigned char* lds = (LAS unsigned char*)lds_raw;
  const int b = blockIdx.x, h = b & 7, j = b >> 3;
  const int tid = threadIdx.x, lane = tid & 63, w = __builtin_amdgcn_readfirstlane(tid >> 6);
  const int hh = lane >> 4, i16 = lane & 15, jj = lane & 3;
  const int qcb = j >> 1, qrh = j & 1, qbr = qcb >> 3;

  if (w == NC) {
    __builtin_amdgcn_s_setprio(3);
    const unsigned ring = (unsigned)(size_t)(lds + L_RING);
    int pub = 0, pslot = 0, slot = 0;
    auto publish1 = [&]() { lds_st(lds, L_FULL + 4 * pslot, (unsigned)(pub + 1)); ++pub; pslot = pslot + 1 == NS ? 0 : pslot + 1; };
    auto pre = [&](int q) {
      if (q >= NS) {
        const unsigned need = (unsigned)(q - NS + 1);
        if (lds_ld(lds, L_FREE + 4 * slot) < need) {
          vm_wait<0>();
          while (pub < q) publish1();
          lds_wait_ge<1>(lds, L_FREE + 4 * slot, need);
        }
      }
    };
    auto post = [&](int q) {
      while (q - pub >= LAG) publish1();
      slot = slot + 1 == NS ? 0 : slot + 1;
    };
    {
      const unsigned char* wq = (qbr ? a.wqB : a.wqA) + (size_t)(h * 512 + 64 * (qcb & 7)) * 2 + (size_t)qrh * 256 * 8192;
      const unsigned vo = (lane >> 3) * 8192 + (lane & 7) * 16;
#pragma unroll
      for (int q = 0; q < 4; ++q) {
#pragma unroll
        for (int m = 0; m < 8; ++m) dma1(ring + q * SLOT + m * 1024, vo, wq + (size_t)(q * 64 + m * 8) * 8192);
        if (q == 0) LDS_BARRIER();
        while (q - pub >= (WSTART + 7) / 8) publish1();
        slot = slot + 1 == NS ? 0 : slot + 1;
      }
    }
    const unsigned k0 = hh * 256 + ((i16 ^ (0 + hh)) * 16), k1 = hh * 256 + ((i16 ^ (4 + hh)) * 16), k2 = hh * 256 + ((i16 ^ (8 + hh)) * 16), k3 = hh * 256 + ((i16 ^ (12 + hh)) * 16);
    const unsigned vl = lane * 16;
    {
      const unsigned char* kp = a.kB + ((size_t)h * SK + (SK - WIN) + j * 128) * 256;
      const unsigned char* vp = a.vB + ((size_t)h * SK + (SK - WIN) + j * 128) * 256;
      for (int q = 4; q < 12; ++q) {
        dma4d(ring + slot * SLOT, k0, k1, k2, k3, kp);
        dma4d(ring + slot * SLOT + 4096, vl, vl, vl, vl, vp);
        kp += 4096; vp += 4096;
        post(q);
      }
      kp = a.kA + ((size_t)h * SK + j * 2048) * 256;
      unsigned o0 = k0, o1 = k1, o2 = k2, o3 = k3;
      unsigned frv = *(volatile LAS unsigned*)(lds + L_FREE + 4 * slot);
      for (int q = 12; q < 140; ++q) {
        if (q == 76) { kp = a.vA + ((size_t)h * SK + j * 2048) * 256; o0 = vl; o1 = vl; o2 = vl; o3 = vl; }
        if (q >= NS && (unsigned)__builtin_amdgcn_readfirstlane(frv) < (unsigned)(q - NS + 1)) pre(q);
        const int nslot = slot + 1 == NS ? 0 : slot + 1;
        frv = *(volatile LAS unsigned*)(lds + L_FREE + 4 * nslot);
        dma4w<WIN_LD>(ring + slot * SLOT, o0, o1, o2, o3, kp);
        dma4w<WIN_LD>(ring + slot * SLOT + 4096, o0, o1, o2, o3, kp + 4096);
        while (q - pub >= LAG2) publish1();
        kp += 8192;
        slot = nslot;
      }
      vp = (j >> 4 ? a.woB : a.woA) + (size_t)(h * 512 + 32 * (j & 15)) * 1024;
      for (int q = 140; q < 144; ++q) {
        pre(q);
        dma4w<WIN_LD>(ring + slot * SLOT, vl, vl, vl, vl, vp); dma4w<WIN_LD>(ring + slot * SLOT + 4096, vl, vl, vl, vl, vp + 4096);
        vp += 8192;
        while (q - pub >= LAG2) publish1();
        slot = slot + 1 == NS ? 0 : slot + 1;
      }
    }
    if (NSEQ - pub > 7) { vm_wait<56>(); publish1(); }
    if (NSEQ - pub > 6) { vm_wait<48>(); publish1(); }
    if (NSEQ - pub > 5) { vm_wait<40>(); publish1(); }
    if (NSEQ - pub > 4) { vm_wait<32>(); publish1(); }
    __builtin_amdgcn_s_barrier();
    if (NSEQ - pub > 3) { vm_wait<24>(); publish1(); }
    if (NSEQ - pub > 2) { vm_wait<16>(); publish1(); }
    if (NSEQ - pub > 1) { vm_wait<8>(); publish1(); }
    vm_wait<0>();
    while (pub < NSEQ) publish1();
    return;
  }

  const __amdgpu_buffer_rsrc_t rs = __builtin_amdgcn_make_buffer_rsrc((void*)a.ws, 0, (int)WS_END, 0x00020000);
  const u32x2 gen = __builtin_amdgcn_raw_buffer_load_b64(rs, WS_GEN, 0, AUX_SC1);
  unsigned short xv[4];
  {
    const GAS unsigned char* xp = (const GAS unsigned char*)(qbr ? a.xB : a.xA) + 2 * (qrh * 256 + w * 32 + (lane >> 3));
#pragma unroll
    for (int i = 0; i < 4; ++i) xv[i] = *(const GAS unsigned short*)(xp + 16 * i);
  }
  unsigned itouch = 0;
  if (w == 1 && lane < 32) itouch = __builtin_amdgcn_raw_buffer_load_b32(rs, WS_I1 + ((h * 32 + j) * 32 + lane) * 128, 0, AUX_SC1);
  if (j < 4 && (w == 2 || w == 3)) itouch = __builtin_amdgcn_raw_buffer_load_b32(rs, WS_I2 + ((h * 4 + j) * 128 + (w - 2) * 64 + lane) * 128, 0, AUX_SC1);
  if (tid < (L_RED - L_FULL) / 4) *(LAS unsigned*)(lds + L_FULL + 4 * tid) = 0u;
  LDS_BARRIER();

  lds_wait_ge<1>(lds, L_FULL + 4 * (w >> 1), (unsigned)((w >> 1) + 1));
  {
    u32x4 wv[4];
#pragma unroll
    for (int i = 0; i < 4; ++i) wv[i] = *(const LAS u32x4*)(lds + L_RING + (w >> 1) * SLOT + (w & 1) * 4096 + i * 1024 + lane * 16);
    float acc[8];
#pragma unroll
    for (int k = 0; k < 8; ++k) acc[k] = 0.f;
#pragma unroll
    for (int i = 0; i < 4; ++i) {
      const float x = lo16(xv[i]);
#pragma unroll
      for (int k = 0; k < 4; ++k) { acc[2 * k] = fmaf(x, lo16(wv[i][k]), acc[2 * k]); acc[2 * k + 1] = fmaf(x, hi16(wv[i][k]), acc[2 * k + 1]); }
    }
#pragma unroll
    for (int k = 0; k < 8; ++k) { float v = acc[k]; v += dpp<ROR8>(v); v = swap16_sum(v); v = swap32_sum(v); acc[k] = v; }
    if (lane < 8) {
      LAS f32x4* red = (LAS f32x4*)(lds + L_RED + w * 256 + lane * 32);
      red[0] = f32x4{acc[0], acc[1], acc[2], acc[3]}; red[1] = f32x4{acc[4], acc[5], acc[6], acc[7]};
    }
  }
  lds_arrive(lds, L_CNT + C_QRED, lane);
  const unsigned tagA = __builtin_amdgcn_readfirstlane(gen[0]) + 1u, tagB = __builtin_amdgcn_readfirstlane(gen[1]) + 1u;
  if (w == 0) {
    lds_wait_ge<0>(lds, L_CNT + C_QRED, NC);
    if (lane < 4) lds_st(lds, L_FREE + 4 * lane, (unsigned)(lane + 1));
    if (lane < 32) {
      f32x2 s = {0.f, 0.f};
#pragma unroll
      for (int ww = 0; ww < 8; ++ww) s += *(const LAS f32x2*)(lds + L_RED + ww * 256 + lane * 8);
      __builtin_amdgcn_raw_buffer_store_b128(u32x4{tagA, f2u(s[0]), f2u(s[1]), tagB}, rs, WS_QG + ((h * 32 + j) * 32 + lane) * 16, 0, AUX_SC1);
    }
  }
  int slot = (4 + w) % NS;
  const unsigned kr0 = i16 * 256 + (((0 + hh) ^ i16) * 16), kr1 = i16 * 256 + (((4 + hh) ^ i16) * 16), kr2 = i16 * 256 + (((8 + hh) ^ i16) * 16), kr3 = i16 * 256 + (((12 + hh) ^ i16) * 16);
  const unsigned vr = hh * 1024 + i16 * 16;
  auto fetch = [&](int it, int kind, u32x4 (&T)[8]) {
    const unsigned seq1 = (unsigned)(4 + 8 * it + w + 1);
    lds_wait_ge<1>(lds, L_FULL + 4 * slot, seq1);
    const LAS unsigned char* sp = lds + L_RING + slot * SLOT;
    if (kind != 2) { T[0] = *(const LAS u32x4*)(sp + kr0); T[1] = *(const LAS u32x4*)(sp + kr1); T[2] = *(const LAS u32x4*)(sp + kr2); T[3] = *(const LAS u32x4*)(sp + kr3); }
    else {
#pragma unroll
      for (int r = 0; r < 4; ++r) T[r] = *(const LAS u32x4*)(sp + vr + r * 256);
    }
    if (kind == 1) { T[4] = *(const LAS u32x4*)(sp + 4096 + kr0); T[5] = *(const LAS u32x4*)(sp + 4096 + kr1); T[6] = *(const LAS u32x4*)(sp + 4096 + kr2); T[7] = *(const LAS u32x4*)(sp + 4096 + kr3); }
    else {
#pragma unroll
      for (int r = 0; r < 4; ++r) T[4 + r] = *(const LAS u32x4*)(sp + 4096 + vr + r * 256);
    }
    LDS_WAIT();
    lds_st(lds, L_FREE + 4 * slot, seq1);
    asm volatile("" : "+v"(T[0]), "+v"(T[1]), "+v"(T[2]), "+v"(T[3]), "+v"(T[4]), "+v"(T[5]), "+v"(T[6]), "+v"(T[7]) :: "memory");
    slot += 8; if (slot >= NS) slot -= NS;
  };
  u32x4 X0[8], X1[8], X2[8];
  fetch(0, 0, X0);
  fetch(1, 1, X1);
  fetch(2, 1, X2);
  {
    const unsigned off = WS_QG + ((h * 32 + 2 * (tid >> 5)) * 32 + (tid & 31)) * 16;
    u32x4 g0 = {0u, 0u, 0u, 0u}, g1 = {0u, 0u, 0u, 0u};
    bool ok = false;
    for (unsigned spin = 0; spin < SPIN_CAP; ++spin) {
      MEMFENCE();
      if (!ok) {
        g0 = __builtin_amdgcn_raw_buffer_load_b128(rs, off, 0, AUX_SC1);
        g1 = __builtin_amdgcn_raw_buffer_load_b128(rs, off + 512, 0, AUX_SC1);
        ok = g0[0] == tagA && g0[3] == tagB && g1[0] == tagA && g1[3] == tagB;
      }
      if (__all(ok)) break;
      __builtin_amdgcn_s_sleep(QSLEEP);
    }
    *(LAS unsigned*)(lds + L_Q + tid * 4) = cvtpk(u2f(g0[1]) + u2f(g1[1]), u2f(g0[2]) + u2f(g1[2]));
  }
  lds_arrive(lds, L_CNT + C_QIN, lane);
  lds_wait_ge<0>(lds, L_CNT + C_QIN, NC);
  u32x4 qA[4], qB[4];
#pragma unroll
  for (int s = 0; s < 4; ++s) {
    qA[s] = *(const LAS u32x4*)(lds + L_Q + ((i16 >> 2) * 128 + 32 * s + 8 * hh) * 2);
    qB[s] = *(const LAS u32x4*)(lds + L_Q + 1024 + ((i16 >> 2) * 128 + 32 * s + 8 * hh) * 2);
  }

  asm volatile("" :: "v"(itouch));
  Sel sel;
  sel.l0 = jj == 0 ? 0x0c0c0100u : jj == 1 ? 0x01000c0cu : 0x0c0c0c0cu;  sel.h0 = jj == 0 ? 0x0c0c0302u : jj == 1 ? 0x03020c0cu : 0x0c0c0c0cu;
  sel.l1 = jj == 2 ? 0x0c0c0100u : jj == 3 ? 0x01000c0cu : 0x0c0c0c0cu;  sel.h1 = jj == 2 ? 0x0c0c0302u : jj == 3 ? 0x03020c0cu : 0x0c0c0c0cu;

  f32x4 b0 = {0.f, 0.f, 0.f, 0.f}, b1 = {0.f, 0.f, 0.f, 0.f};
  {
    const u32x2 t = tile_scores(X0, qB);
    const float m2 = swap32_max(swap16_max(max4(t))) * LOG2E;
    float l = 0.f;
    const u32x2 p = to_probs(t, m2, l);
    l = swap32_sum(swap16_sum(l));
    tile_pv(b0, b1, X0 + 4, p, sel);
    if (hh == 0 && jj == 0) *(LAS f32x2*)(lds + L_TB + ((i16 >> 2) * 8 + w) * 8) = f32x2{m2, l};
  }

  u32x2 P[16];
  float mx = -INFINITY;
#pragma unroll
  for (int i = 0; i < 8; ++i) {
    if (i >= 2) fetch(i + 1, 1, X1);
    const u32x4* T = i == 1 ? X2 : X1;
    P[2 * i] = tile_scores(T, qA);
    P[2 * i + 1] = tile_scores(T + 4, qA);
    mx = fmaxf(mx, fmaxf(max4(P[2 * i]), max4(P[2 * i + 1])));
  }
  mx = swap32_max(swap16_max(mx));
  if (hh == 0 && jj == 0) *(LAS float*)(lds + L_T1 + ((i16 >> 2) * 8 + w) * 4) = mx;
  lds_arrive(lds, L_CNT + C_MAX, lane);
  lds_wait_ge<0>(lds, L_CNT + C_MAX, NC);
  {
    const f32x4 m0 = *(const LAS f32x4*)(lds + L_T1 + (i16 >> 2) * 32), m1 = *(const LAS f32x4*)(lds + L_T1 + (i16 >> 2) * 32 + 16);
    const float m2 = fmaxf(fmaxf(fmaxf(m0[0], m0[1]), fmaxf(m0[2], m0[3])), fmaxf(fmaxf(m1[0], m1[1]), fmaxf(m1[2], m1[3]))) * LOG2E;
    float l = 0.f;
#pragma unroll
    for (int t = 0; t < 16; ++t) P[t] = to_probs(P[t], m2, l);
    l = swap32_sum(swap16_sum(l));
    if (hh == 0 && jj == 0) *(LAS float*)(lds + L_T2 + ((i16 >> 2) * 8 + w) * 4) = l;
  }
  lds_arrive(lds, L_CNT + C_SUM, lane);
  lds_wait_ge<0>(lds, L_CNT + C_SUM, NC);
  const int mbr = w >> 2, mg = w & 3;
  {
    const int ww = lane & 7;
    float M, L;
    if (mbr == 0) {
      M = oct_max(*(const LAS float*)(lds + L_T1 + (mg * 8 + ww) * 4)) * LOG2E;
      L = oct_sum(*(const LAS float*)(lds + L_T2 + (mg * 8 + ww) * 4));
    } else {
      const f32x2 ml = *(const LAS f32x2*)(lds + L_TB + (mg * 8 + ww) * 8);
      M = oct_max(ml[0]);
      L = oct_sum(ml[1] * __builtin_amdgcn_exp2f(ml[0] - M));
    }
    if (lane == 0) __builtin_amdgcn_raw_buffer_store_b128(u32x4{tagA, f2u(M), f2u(L), tagB}, rs, WS_ML + (((h * 2 + mbr) * 4 + mg) * 32 + j) * 16, 0, AUX_SC1);
  }

  f32x4 a0 = {0.f, 0.f, 0.f, 0.f}, a1 = {0.f, 0.f, 0.f, 0.f};
  float fA = 0.f, fB = 0.f;
#pragma unroll
  for (int i = 0; i < 8; ++i) {
    if (i == 4) {
      const unsigned off = WS_ML + (((h * 2 + mbr) * 4 + mg) * 32 + (lane & 31)) * 16;
      u32x4 mv = {0u, 0u, 0u, 0u};
      bool ok = false;
      for (unsigned spin = 0; spin < SPIN_CAP; ++spin) {
        MEMFENCE();
        if (!ok) { mv = __builtin_amdgcn_raw_buffer_load_b128(rs, off, 0, AUX_SC1); ok = mv[0] == tagA && mv[3] == tagB; }
        if (__all(ok)) break;
        __builtin_amdgcn_s_sleep(16);
      }
      const float M = swap16_max(row_max(u2f(mv[1])));
      const float L = swap16_sum(row_sum(u2f(mv[2]) * __builtin_amdgcn_exp2f(u2f(mv[1]) - M)));
      if (lane == 0) *(LAS f32x2*)(lds + L_FF + w * 8) = f32x2{M, 1.0f / L};
      lds_arrive(lds, L_CNT + C_FF, lane);
    }
    if (i == 7) {
      lds_wait_ge<0>(lds, L_CNT + C_FF, NC);
      const f32x4 m0 = *(const LAS f32x4*)(lds + L_T1 + hh * 32), m1 = *(const LAS f32x4*)(lds + L_T1 + hh * 32 + 16);
      const float m2 = fmaxf(fmaxf(fmaxf(m0[0], m0[1]), fmaxf(m0[2], m0[3])), fmaxf(fmaxf(m1[0], m1[1]), fmaxf(m1[2], m1[3]))) * LOG2E;
      const f32x2 FA = *(const LAS f32x2*)(lds + L_FF + hh * 8), FB = *(const LAS f32x2*)(lds + L_FF + (4 + hh) * 8);
      const float mB = *(const LAS float*)(lds + L_TB + (hh * 8 + w) * 8);
      fA = __builtin_amdgcn_exp2f(m2 - FA[0]) * FA[1];
      fB = __builtin_amdgcn_exp2f(mB - FB[0]) * FB[1];
      const int s = lane < MRG_SLOTS ? lane : 0;
      const unsigned need = (unsigned)(s + NS * ((139 - s) / NS) + 1);
      for (unsigned spin = 0; spin < LSPIN_CAP; ++spin) { MEMFENCE(); if (__all(*(volatile LAS unsigned*)(lds + L_FREE + 4 * s) >= need)) break; }
      MEMFENCE();
      LAS float* rB = (LAS float*)(lds + L_MRG) + ((8 + w) * 4 + hh) * MREC + 8 * i16;
      *(LAS f32x4*)(rB) = b0 * fB; *(LAS f32x4*)(rB + 4) = b1 * fB;
    }
    fetch(9 + i, 2, X1);
    tile_pv(a0, a1, X1, P[2 * i], sel);
    tile_pv(a0, a1, X1 + 4, P[2 * i + 1], sel);
  }

  const unsigned tA = tagA & 0xffffffu, tB = tagB & 0xffffffu;
  {
    LAS float* rA = (LAS float*)(lds + L_MRG) + (w * 4 + hh) * MREC + 8 * i16;
    *(LAS f32x4*)(rA) = a0 * fA; *(LAS f32x4*)(rA + 4) = a1 * fA;
  }
  LDS_BARRIER();
  if (tid < 256) {
    const int br = tid >> 7, g = (tid >> 5) & 3, dq = tid & 31;
    const LAS float* mrg = (const LAS float*)(lds + L_MRG) + (br * 8 * 4 + g) * MREC + 4 * dq;
    f32x4 o4[8];
#pragma unroll
    for (int ww = 0; ww < 8; ++ww) o4[ww] = *(const LAS f32x4*)(mrg + ww * 4 * MREC);
    const f32x4 o = ((o4[0] + o4[1]) + (o4[2] + o4[3])) + ((o4[4] + o4[5]) + (o4[6] + o4[7]));
    const int jc = br * 16 + g * 4 + (dq >> 3), k = dq & 7;
    __builtin_amdgcn_raw_buffer_store_b128(pack4(o, tA, tB), rs, WS_I1 + (((h * 32 + jc) * 32 + j) * 8 + k) * 16, 0, AUX_SC1);
  }

  const unsigned offA = WS_I1 + ((h * 32 + j) * 32 * 8 + (w & 3) * 64 + lane) * 16;
  u32x4 av = {0u, 0u, 0u, 0u};
  if (w >= 4) av = __builtin_amdgcn_raw_buffer_load_b128(rs, offA, 0, AUX_SC1);
  MEMFENCE();
  float W[32];
  lds_wait_ge<0>(lds, L_FULL + 4 * (143 % NS), 144u);
  {
    const LAS unsigned short* wp = (const LAS unsigned short*)(lds + L_RING + (140 % NS) * SLOT) + 64 * w + lane;
#pragma unroll
    for (int r = 0; r < 32; ++r) W[r] = u2f((unsigned)wp[r * 512] << 16);
#pragma unroll
    for (int r = 0; r < 32; r += 8) asm volatile("" : "+v"(W[r]), "+v"(W[r + 1]), "+v"(W[r + 2]), "+v"(W[r + 3]), "+v"(W[r + 4]), "+v"(W[r + 5]), "+v"(W[r + 6]), "+v"(W[r + 7]));
  }
  if (w >= 4) {
    bool ok = tags4(av, tA, tB);
    for (unsigned spin = 0; spin < SPIN_CAP && !__all(ok); ++spin) {
      MEMFENCE();
      if (!ok) { av = __builtin_amdgcn_raw_buffer_load_b128(rs, offA, 0, AUX_SC1); ok = tags4(av, tA, tB); }
    }
    const f32x4 o = red8_16_32(unpack4(av));
    if (lane < 8) {
#pragma unroll
      for (int e = 0; e < 4; ++e) *(LAS float*)(lds + L_H1 + ((4 * lane + e) * 4 + (w & 3)) * 4) = o[e];
    }
  }
  LDS_BARRIER();
  {
    const f32x4 ra = *(const LAS f32x4*)(lds + L_H1 + i16 * 16), rb = *(const LAS f32x4*)(lds + L_H1 + (16 + i16) * 16);
    const unsigned ob = cvtpk((ra[0] + ra[1]) + (ra[2] + ra[3]), (rb[0] + rb[1]) + (rb[2] + rb[3]));
    const float oa = lo16(ob), oc = hi16(ob);
    float y0 = 0.f, y1 = 0.f, y2 = 0.f, y3 = 0.f;
#define FM(acc, o, wv, n) "v_fmac_f32_dpp " acc ", " o ", " wv " row_newbcast:" #n " row_mask:0xf bank_mask:0xf\n\t"
#define ROWS(n0, n1, n2, n3) asm volatile("s_nop 1\n\t" \
      FM("%0", "%4", "%6", n0) FM("%1", "%5", "%7", n0) FM("%2", "%4", "%8", n1) FM("%3", "%5", "%9", n1) \
      FM("%0", "%4", "%10", n2) FM("%1", "%5", "%11", n2) FM("%2", "%4", "%12", n3) FM("%3", "%5", "%13", n3) \
      : "+v"(y0), "+v"(y1), "+v"(y2), "+v"(y3) \
      : "v"(oa), "v"(oc), "v"(W[n0]), "v"(W[16 + n0]), "v"(W[n1]), "v"(W[16 + n1]), "v"(W[n2]), "v"(W[16 + n2]), "v"(W[n3]), "v"(W[16 + n3]));
    ROWS(0, 1, 2, 3) ROWS(4, 5, 6, 7) ROWS(8, 9, 10, 11) ROWS(12, 13, 14, 15)
#undef ROWS
#undef FM
    const float y = (y0 + y1) + (y2 + y3);
    const f32x4 y4 = {y, dpp<0x55>(y), dpp<0xAA>(y), dpp<0xFF>(y)};
    const int c = (j >> 4) * 16 + 2 * w + (lane >> 5), k = (lane & 31) >> 2, pidx = h * 16 + (j & 15);
    if (jj == 0) __builtin_amdgcn_raw_buffer_store_b128(pack4(y4, tA, tB), rs, WS_I2 + ((c * 128 + pidx) * 8 + k) * 16, 0, AUX_SC1);
  }

  if (j < 4) {
    const int c = h * 4 + j;
    const unsigned off = WS_I2 + (c * 128 + 16 * w) * 128 + lane * 16;
    u32x4 v[2] = {{0u, 0u, 0u, 0u}, {0u, 0u, 0u, 0u}};
    bool ok = false;
    for (unsigned spin = 0; spin < SPIN_CAP; ++spin) {
      MEMFENCE();
      if (!ok) {
#pragma unroll
        for (int i = 0; i < 2; ++i) v[i] = __builtin_amdgcn_raw_buffer_load_b128(rs, off + i * 1024, 0, AUX_SC1);
        ok = tags4(v[0], tA, tB) && tags4(v[1], tA, tB);
      }
      if (__all(ok)) break;
    }
    const f32x4 s4 = red8_16_32(unpack4(v[0]) + unpack4(v[1]));
    if (lane < 8) {
#pragma unroll
      for (int e = 0; e < 4; ++e) *(LAS float*)(lds + L_F + ((4 * lane + e) * 8 + w) * 4) = s4[e];
    }
    LDS_BARRIER();
    if (tid < 32) {
      const f32x4 p0 = *(const LAS f32x4*)(lds + L_F + tid * 32), p1 = *(const LAS f32x4*)(lds + L_F + tid * 32 + 16);
      const float s = ((p0[0] + p0[1]) + (p0[2] + p0[3])) + ((p1[0] + p1[1]) + (p1[2] + p1[3]));
      const unsigned pk = cvtpk(s, dpp<XOR1>(s));
      if ((tid & 1) == 0) *(GAS unsigned*)((GAS unsigned char*)a.out + (c * 32 + tid) * 2) = pk;
      if (c == 0 && tid == 0) __builtin_amdgcn_raw_buffer_store_b64(u32x2{tagA, tagB}, rs, WS_GEN, 0, 0);
    }
  }
}

extern "C" void kernel_launch(void* const* d_in, const int* in_sizes, int n_in, void* d_out, int out_size, void* d_ws, size_t ws_size, hipStream_t stream) {
  Args a;
  a.xA = (const unsigned char*)d_in[0]; a.kA = (const unsigned char*)d_in[1]; a.vA = (const unsigned char*)d_in[2]; a.wqA = (const unsigned char*)d_in[3]; a.woA = (const unsigned char*)d_in[4];
  a.xB = (const unsigned char*)d_in[5]; a.kB = (const unsigned char*)d_in[6]; a.vB = (const unsigned char*)d_in[7]; a.wqB = (const unsigned char*)d_in[8]; a.woB = (const unsigned char*)d_in[9];
  a.out = (unsigned short*)d_out;
  a.ws = (unsigned char*)d_ws;
  hipLaunchKernelGGL(fused_decode, dim3(NWG), dim3(NTHR), 0, stream, a);
}
```

```cpp
#include <hip/hip_runtime.h>
#include <hip/hip_bf16.h>
#include <stdint.h>

#define GAS __attribute__((address_space(1)))
#define LAS __attribute__((address_space(3)))
typedef unsigned u32x4 __attribute__((ext_vector_type(4)));
typedef unsigned u32x2 __attribute__((ext_vector_type(2)));
typedef float f32x4 __attribute__((ext_vector_type(4)));
typedef float f32x2 __attribute__((ext_vector_type(2)));
typedef __bf16 bf16x8 __attribute__((ext_vector_type(8)));
typedef __bf16 bf16x2 __attribute__((ext_vector_type(2)));
typedef short s16x4 __attribute__((ext_vector_type(4)));

constexpr int NS = 18;
constexpr int WIN_LD = 21;
constexpr int WSTART = 40;
constexpr int QSLEEP = 16;

constexpr int SK = 65536, WIN = 4096;
constexpr int NWG = 256, NC = 8, NTHR = (NC + 1) * 64;
constexpr int SLOT = 8192, NSEQ = 4 + 8 + 64 + 64 + 4;
constexpr int LAG = (WIN_LD + 7) / 8;
constexpr int LAG2 = (WIN_LD + 8) / 8;
constexpr float LOG2E = 1.4426950408889634f;
constexpr float SCALE = 0.08838834764831845f;
constexpr unsigned SPIN_CAP = 1u << 17, LSPIN_CAP = 1u << 23;

constexpr unsigned WS_GEN = 0;
constexpr unsigned WS_QG = 4096;
constexpr unsigned WS_ML = WS_QG + 8 * 32 * 32 * 16;
constexpr unsigned WS_I1 = WS_ML + 256 * 128;
constexpr unsigned WS_I2 = WS_I1 + 8 * 32 * 32 * 128;
constexpr unsigned WS_END = WS_I2 + 32 * 128 * 128;

constexpr int L_RING = 0;
constexpr int L_FULL = NS * SLOT;
constexpr int L_FREE = L_FULL + 128;
constexpr int L_CNT = L_FREE + 128;
constexpr int L_RED = L_CNT + 64;
constexpr int L_Q = L_RED + 2048;
constexpr int L_T1 = L_Q + 2048;
constexpr int L_T2 = L_T1 + 128;
constexpr int L_TB = L_T2 + 128;
constexpr int L_FF = L_TB + 256;
constexpr int L_H1 = L_FF + 64;
constexpr int L_F = L_H1 + 512;
constexpr int L_END = L_F + 1024;
constexpr int C_QRED = 0, C_QIN = 4, C_MAX = 8, C_SUM = 12, C_FF = 16;
constexpr int MREC = 132;
constexpr int L_MRG = 0;
constexpr int MRG_SLOTS = 5;
static_assert(L_END <= 160 * 1024, "LDS");
static_assert(2 * 8 * 4 * MREC * 4 <= MRG_SLOTS * SLOT, "merge area");
static_assert((140 % NS) >= MRG_SLOTS && (143 % NS) >= MRG_SLOTS && (140 % NS) <= (143 % NS), "wo slots contiguous and clear of the merge area");
static_assert((132 % NS) >= MRG_SLOTS && (139 % NS) >= MRG_SLOTS && (132 % NS) <= (139 % NS), "last round clear of the merge area");
static_assert(WIN_LD >= 9 && WIN_LD <= 48, "window");

__device__ __forceinline__ unsigned cvtpk(float lo, float hi) { f32x2 f = {lo, hi}; return __builtin_bit_cast(unsigned, __builtin_convertvector(f, bf16x2)); }
__device__ __forceinline__ float lo16(unsigned u) { return __builtin_bit_cast(float, u << 16); }
__device__ __forceinline__ float hi16(unsigned u) { return __builtin_bit_cast(float, u & 0xffff0000u); }
__device__ __forceinline__ float u2f(unsigned u) { return __builtin_bit_cast(float, u); }
__device__ __forceinline__ unsigned f2u(float f) { return __builtin_bit_cast(unsigned, f); }
template <int CTRL> __device__ __forceinline__ float dpp(float x) { return u2f(__builtin_amdgcn_mov_dpp(f2u(x), CTRL, 0xf, 0xf, true)); }
constexpr int XOR1 = 0xB1, XOR2 = 0x4E, HMIR = 0x141;
constexpr int ROR1 = 0x121, ROR2 = 0x122, ROR4 = 0x124, ROR8 = 0x128;
__device__ __forceinline__ float swap16_max(float x) { auto s = __builtin_amdgcn_permlane16_swap(f2u(x), f2u(x), false, false); return fmaxf(u2f(s[0]), u2f(s[1])); }
__device__ __forceinline__ float swap32_max(float x) { auto s = __builtin_amdgcn_permlane32_swap(f2u(x), f2u(x), false, false); return fmaxf(u2f(s[0]), u2f(s[1])); }
__device__ __forceinline__ float swap16_sum(float x) { auto s = __builtin_amdgcn_permlane16_swap(f2u(x), f2u(x), false, false); return u2f(s[0]) + u2f(s[1]); }
__device__ __forceinline__ float swap32_sum(float x) { auto s = __builtin_amdgcn_permlane32_swap(f2u(x), f2u(x), false, false); return u2f(s[0]) + u2f(s[1]); }
__device__ __forceinline__ float row_sum(float x) { x += dpp<ROR8>(x); x += dpp<ROR4>(x); x += dpp<ROR2>(x); x += dpp<ROR1>(x); return x; }
__device__ __forceinline__ float row_max(float x) { x = fmaxf(x, dpp<ROR8>(x)); x = fmaxf(x, dpp<ROR4>(x)); x = fmaxf(x, dpp<ROR2>(x)); x = fmaxf(x, dpp<ROR1>(x)); return x; }
__device__ __forceinline__ float oct_sum(float x) { x += dpp<XOR1>(x); x += dpp<XOR2>(x); x += dpp<HMIR>(x); return x; }
__device__ __forceinline__ float oct_max(float x) { x = fmaxf(x, dpp<XOR1>(x)); x = fmaxf(x, dpp<XOR2>(x)); x = fmaxf(x, dpp<HMIR>(x)); return x; }
#define LDS_BARRIER() asm volatile("s_waitcnt lgkmcnt(0)\n\ts_barrier" ::: "memory")
#define LDS_WAIT() asm volatile("s_waitcnt lgkmcnt(0)" ::: "memory")
#define MEMFENCE() asm volatile("" ::: "memory")
constexpr int AUX_SC1 = 16;

__device__ __forceinline__ u32x4 pack4(f32x4 v, unsigned tA, unsigned tB) {
  const unsigned t0 = f2u(v[0]) + 0x800u, t1 = f2u(v[1]) + 0x800u, t2 = f2u(v[2]) + 0x800u, t3 = f2u(v[3]) + 0x800u;
  return u32x4{tA | ((t0 << 12) & 0xff000000u), (t0 >> 20) | (t1 & 0xfffff000u), (t2 >> 12) | ((t3 << 8) & 0xfff00000u), (t3 >> 24) | (tB << 8)};
}
__device__ __forceinline__ f32x4 unpack4(u32x4 d) {
  return f32x4{u2f((d[1] << 20) | ((d[0] >> 12) & 0xff000u)), u2f(d[1] & 0xfffff000u), u2f(d[2] << 12), u2f(((d[2] >> 8) & 0xfff000u) | (d[3] << 24))};
}
__device__ __forceinline__ bool tags4(u32x4 d, unsigned tA, unsigned tB) { return (d[0] & 0xffffffu) == tA && (d[3] >> 8) == tB; }
__device__ __forceinline__ f32x4 red8_16_32(f32x4 v) {
#pragma unroll
  for (int e = 0; e < 4; ++e) { float x = v[e]; x += dpp<ROR8>(x); v[e] = swap32_sum(swap16_sum(x)); }
  return v;
}

struct Args {
  const unsigned char *xA, *kA, *vA, *wqA, *woA, *xB, *kB, *vB, *wqB, *woB;
  unsigned short* out;
  unsigned char* ws;
};

__device__ __forceinline__ unsigned lds_ld(LAS unsigned char* L, int off) { return __builtin_amdgcn_readfirstlane(*(volatile LAS unsigned*)(L + off)); }
__device__ __forceinline__ void lds_st(LAS unsigned char* L, int off, unsigned v) { *(volatile LAS unsigned*)(L + off) = v; }
template <int SLEEP> __device__ __forceinline__ void lds_wait_ge(LAS unsigned char* L, int off, unsigned need) {
  for (unsigned spin = 0; lds_ld(L, off) < need && spin < LSPIN_CAP; ++spin) { if (SLEEP) __builtin_amdgcn_s_sleep(SLEEP); }
  MEMFENCE();
}
__device__ __forceinline__ void lds_arrive(LAS unsigned char* L, int off, int lane) {
  LDS_WAIT();
  if (lane == 0) __hip_atomic_fetch_add((LAS unsigned*)(L + off), 1u, __ATOMIC_RELAXED, __HIP_MEMORY_SCOPE_WORKGROUP);
}

__device__ __forceinline__ void dma1(unsigned dst, unsigned voff, const unsigned char* base) {
  unsigned keep;
  asm volatile("s_mov_b32 %0, m0\n\ts_mov_b32 m0, %1\n\ts_nop 0\n\tglobal_load_lds_dwordx4 %2, %3 nt\n\ts_mov_b32 m0, %0\n\ts_waitcnt vmcnt(%4)"
               : "=&s"(keep) : "s"(dst), "v"(voff), "s"(base), "i"(WSTART) : "memory");
}
__device__ __forceinline__ void dma4d(unsigned dst, unsigned v0, unsigned v1, unsigned v2, unsigned v3, const unsigned char* base) {
  unsigned keep;
  asm volatile("s_mov_b32 %0, m0\n\ts_mov_b32 m0, %1\n\ts_nop 0\n\t"
               "global_load_lds_dwordx4 %2, %6\n\ts_waitcnt vmcnt(%7)\n\t"
               "global_load_lds_dwordx4 %3, %6 offset:1024\n\ts_waitcnt vmcnt(%7)\n\t"
               "global_load_lds_dwordx4 %4, %6 offset:2048\n\ts_waitcnt vmcnt(%7)\n\t"
               "global_load_lds_dwordx4 %5, %6 offset:3072\n\ts_waitcnt vmcnt(%7)\n\t"
               "s_mov_b32 m0, %0"
               : "=&s"(keep) : "s"(dst), "v"(v0), "v"(v1), "v"(v2), "v"(v3), "s"(base), "i"(WIN_LD) : "memory");
}
template <int WL> __device__ __forceinline__ void dma4w(unsigned dst, unsigned v0, unsigned v1, unsigned v2, unsigned v3, const unsigned char* base) {
  unsigned keep;
  asm volatile("s_mov_b32 %0, m0\n\ts_mov_b32 m0, %1\n\ts_waitcnt vmcnt(%7)\n\t"
               "global_load_lds_dwordx4 %2, %6 nt\n\ts_waitcnt vmcnt(%7)\n\t"
               "global_load_lds_dwordx4 %3, %6 offset:1024 nt\n\ts_waitcnt vmcnt(%7)\n\t"
               "global_load_lds_dwordx4 %4, %6 offset:2048 nt\n\ts_waitcnt vmcnt(%7)\n\t"
               "global_load_lds_dwordx4 %5, %6 offset:3072 nt\n\t"
               "s_mov_b32 m0, %0"
               : "=&s"(keep) : "s"(dst), "v"(v0), "v"(v1), "v"(v2), "v"(v3), "s"(base), "i"(WL) : "memory");
}
template <int N> __device__ __forceinline__ void vm_wait() { asm volatile("s_waitcnt vmcnt(%0)" :: "i"(N) : "memory"); }

__device__ __forceinline__ u32x2 tile_scores(const u32x4* K, const u32x4 (&q)[4]) {
  f32x4 s = {0.f, 0.f, 0.f, 0.f};
#pragma unroll
  for (int i = 0; i < 4; ++i) s = __builtin_amdgcn_mfma_f32_16x16x32_bf16(__builtin_bit_cast(bf16x8, K[i]), __builtin_bit_cast(bf16x8, q[i]), s, 0, 0, 0);
  const unsigned r01 = cvtpk(s[0], s[1]), r23 = cvtpk(s[2], s[3]);
  return u32x2{cvtpk(lo16(r01) * SCALE, hi16(r01) * SCALE), cvtpk(lo16(r23) * SCALE, hi16(r23) * SCALE)};
}
__device__ __forceinline__ float max4(u32x2 t) { return fmaxf(fmaxf(lo16(t[0]), hi16(t[0])), fmaxf(lo16(t[1]), hi16(t[1]))); }
__device__ __forceinline__ u32x2 to_probs(u32x2 t, float m2, float& l) {
  const float p0 = __builtin_amdgcn_exp2f(fmaf(lo16(t[0]), LOG2E, -m2)), p1 = __builtin_amdgcn_exp2f(fmaf(hi16(t[0]), LOG2E, -m2));
  const float p2 = __builtin_amdgcn_exp2f(fmaf(lo16(t[1]), LOG2E, -m2)), p3 = __builtin_amdgcn_exp2f(fmaf(hi16(t[1]), LOG2E, -m2));
  l += (p0 + p1) + (p2 + p3);
  return u32x2{cvtpk(p0, p1), cvtpk(p2, p3)};
}
struct Sel { unsigned l0, l1, h0, h1; };
__device__ __forceinline__ void tile_pv(f32x4& a0, f32x4& a1, const u32x4* V, u32x2 p, const Sel& s) {
#pragma unroll
  for (int r = 0; r < 4; ++r) {
    const unsigned pk = p[r >> 1];
    const u32x2 a = {__builtin_amdgcn_perm(0u, pk, (r & 1) ? s.h0 : s.l0), __builtin_amdgcn_perm(0u, pk, (r & 1) ? s.h1 : s.l1)};
    const u32x2 v0 = {V[r][0], V[r][1]}, v1 = {V[r][2], V[r][3]};
    a0 = __builtin_amdgcn_mfma_f32_16x16x16bf16_1k(__builtin_bit_cast(s16x4, a), __builtin_bit_cast(s16x4, v0), a0, 0, 0, 0);
    a1 = __builtin_amdgcn_mfma_f32_16x16x16bf16_1k(__builtin_bit_cast(s16x4, a), __builtin_bit_cast(s16x4, v1), a1, 0, 0, 0);
  }
}

__global__ void __launch_bounds__(NTHR, 3) fused_decode(Args a) {
  __shared__ __attribute__((aligned(16))) unsigned char lds_raw[L_END];
  LAS unsigned char* lds = (LAS unsigned char*)lds_raw;
  const int b = blockIdx.x, h = b & 7, j = b >> 3;
  const int tid = threadIdx.x, lane = tid & 63, w = __builtin_amdgcn_readfirstlane(tid >> 6);
  const int hh = lane >> 4, i16 = lane & 15, jj = lane & 3;
  const int qcb = j >> 1, qrh = j & 1, qbr = qcb >> 3;

  if (w == NC) {
    __builtin_amdgcn_s_setprio(3);
    const unsigned ring = (unsigned)(size_t)(lds + L_RING);
    int pub = 0, pslot = 0, slot = 0;
    auto publish1 = [&]() { lds_st(lds, L_FULL + 4 * pslot, (unsigned)(pub + 1)); ++pub; pslot = pslot + 1 == NS ? 0 : pslot + 1; };
    auto pre = [&](int q) {
      if (q >= NS) {
        const unsigned need = (unsigned)(q - NS + 1);
        if (lds_ld(lds, L_FREE + 4 * slot) < need) {
          vm_wait<0>();
          while (pub < q) publish1();
          lds_wait_ge<1>(lds, L_FREE + 4 * slot, need);
        }
      }
    };
    auto post = [&](int q) {
      while (q - pub >= LAG) publish1();
      slot = slot + 1 == NS ? 0 : slot + 1;
    };
    {
      const unsigned char* wq = (qbr ? a.wqB : a.wqA) + (size_t)(h * 512 + 64 * (qcb & 7)) * 2 + (size_t)qrh * 256 * 8192;
      const unsigned vo = (lane >> 3) * 8192 + (lane & 7) * 16;
#pragma unroll
      for (int q = 0; q < 4; ++q) {
#pragma unroll
        for (int m = 0; m < 8; ++m) dma1(ring + q * SLOT + m * 1024, vo, wq + (size_t)(q * 64 + m * 8) * 8192);
        if (q == 0) LDS_BARRIER();
        while (q - pub >= (WSTART + 7) / 8) publish1();
        slot = slot + 1 == NS ? 0 : slot + 1;
      }
    }
    const unsigned k0 = hh * 256 + ((i16 ^ (0 + hh)) * 16), k1 = hh * 256 + ((i16 ^ (4 + hh)) * 16), k2 = hh * 256 + ((i16 ^ (8 + hh)) * 16), k3 = hh * 256 + ((i16 ^ (12 + hh)) * 16);
    const unsigned vl = lane * 16;
    {
      const unsigned char* kp = a.kB + ((size_t)h * SK + (SK - WIN) + j * 128) * 256;
      const unsigned char* vp = a.vB + ((size_t)h * SK + (SK - WIN) + j * 128) * 256;
      for (int q = 4; q < 12; ++q) {
        dma4d(ring + slot * SLOT, k0, k1, k2, k3, kp);
        dma4d(ring + slot * SLOT + 4096, vl, vl, vl, vl, vp);
        kp += 4096; vp += 4096;
        post(q);
      }
      kp = a.kA + ((size_t)h * SK + j * 2048) * 256;
      unsigned o0 = k0, o1 = k1, o2 = k2, o3 = k3;
      unsigned frv = *(volatile LAS unsigned*)(lds + L_FREE + 4 * slot);
      for (int q = 12; q < 140; ++q) {
        if (q == 76) { kp = a.vA + ((size_t)h * SK + j * 2048) * 256; o0 = vl; o1 = vl; o2 = vl; o3 = vl; }
        if (q >= NS && (unsigned)__builtin_amdgcn_readfirstlane(frv) < (unsigned)(q - NS + 1)) pre(q);
        const int nslot = slot + 1 == NS ? 0 : slot + 1;
        frv = *(volatile LAS unsigned*)(lds + L_FREE + 4 * nslot);
        dma4w<WIN_LD>(ring + slot * SLOT, o0, o1, o2, o3, kp);
        dma4w<WIN_LD>(ring + slot * SLOT + 4096, o0, o1, o2, o3, kp + 4096);
        while (q - pub >= LAG2) publish1();
        kp += 8192;
        slot = nslot;
      }
      vp = (j >> 4 ? a.woB : a.woA) + (size_t)(h * 512 + 32 * (j & 15)) * 1024;
      for (int q = 140; q < 144; ++q) {
        pre(q);
        dma4w<WIN_LD>(ring + slot * SLOT, vl, vl, vl, vl, vp); dma4w<WIN_LD>(ring + slot * SLOT + 4096, vl, vl, vl, vl, vp + 4096);
        vp += 8192;
        while (q - pub >= LAG2) publish1();
        slot = slot + 1 == NS ? 0 : slot + 1;
      }
    }
    if (NSEQ - pub > 7) { vm_wait<56>(); publish1(); }
    if (NSEQ - pub > 6) { vm_wait<48>(); publish1(); }
    if (NSEQ - pub > 5) { vm_wait<40>(); publish1(); }
    if (NSEQ - pub > 4) { vm_wait<32>(); publish1(); }
    __builtin_amdgcn_s_barrier();
    if (NSEQ - pub > 3) { vm_wait<24>(); publish1(); }
    if (NSEQ - pub > 2) { vm_wait<16>(); publish1(); }
    if (NSEQ - pub > 1) { vm_wait<8>(); publish1(); }
    vm_wait<0>();
    while (pub < NSEQ) publish1();
    return;
  }

  const __amdgpu_buffer_rsrc_t rs = __builtin_amdgcn_make_buffer_rsrc((void*)a.ws, 0, (int)WS_END, 0x00020000);
  const u32x2 gen = __builtin_amdgcn_raw_buffer_load_b64(rs, WS_GEN, 0, AUX_SC1);
  unsigned short xv[4];
  {
    const GAS unsigned char* xp = (const GAS unsigned char*)(qbr ? a.xB : a.xA) + 2 * (qrh * 256 + w * 32 + (lane >> 3));
#pragma unroll
    for (int i = 0; i < 4; ++i) xv[i] = *(const GAS unsigned short*)(xp + 16 * i);
  }
  if (tid < (L_RED - L_FULL) / 4) *(LAS unsigned*)(lds + L_FULL + 4 * tid) = 0u;
  LDS_BARRIER();

  lds_wait_ge<1>(lds, L_FULL + 4 * (w >> 1), (unsigned)((w >> 1) + 1));
  {
    u32x4 wv[4];
#pragma unroll
    for (int i = 0; i < 4; ++i) wv[i] = *(const LAS u32x4*)(lds + L_RING + (w >> 1) * SLOT + (w & 1) * 4096 + i * 1024 + lane * 16);
    float acc[8];
#pragma unroll
    for (int k = 0; k < 8; ++k) acc[k] = 0.f;
#pragma unroll
    for (int i = 0; i < 4; ++i) {
      const float x = lo16(xv[i]);
#pragma unroll
      for (int k = 0; k < 4; ++k) { acc[2 * k] = fmaf(x, lo16(wv[i][k]), acc[2 * k]); acc[2 * k + 1] = fmaf(x, hi16(wv[i][k]), acc[2 * k + 1]); }
    }
#pragma unroll
    for (int k = 0; k < 8; ++k) { float v = acc[k]; v += dpp<ROR8>(v); v = swap16_sum(v); v = swap32_sum(v); acc[k] = v; }
    if (lane < 8) {
      LAS f32x4* red = (LAS f32x4*)(lds + L_RED + w * 256 + lane * 32);
      red[0] = f32x4{acc[0], acc[1], acc[2], acc[3]}; red[1] = f32x4{acc[4], acc[5], acc[6], acc[7]};
    }
  }
  lds_arrive(lds, L_CNT + C_QRED, lane);
  const unsigned tagA = __builtin_amdgcn_readfirstlane(gen[0]) + 1u, tagB = __builtin_amdgcn_readfirstlane(gen[1]) + 1u;
  if (w == 0) {
    lds_wait_ge<0>(lds, L_CNT + C_QRED, NC);
    if (lane < 4) lds_st(lds, L_FREE + 4 * lane, (unsigned)(lane + 1));
    if (lane < 32) {
      f32x2 s = {0.f, 0.f};
#pragma unroll
      for (int ww = 0; ww < 8; ++ww) s += *(const LAS f32x2*)(lds + L_RED + ww * 256 + lane * 8);
      __builtin_amdgcn_raw_buffer_store_b128(u32x4{tagA, f2u(s[0]), f2u(s[1]), tagB}, rs, WS_QG + ((h * 32 + j) * 32 + lane) * 16, 0, AUX_SC1);
    }
  }
  int slot = (4 + w) % NS;
  const unsigned kr0 = i16 * 256 + (((0 + hh) ^ i16) * 16), kr1 = i16 * 256 + (((4 + hh) ^ i16) * 16), kr2 = i16 * 256 + (((8 + hh) ^ i16) * 16), kr3 = i16 * 256 + (((12 + hh) ^ i16) * 16);
  const unsigned vr = hh * 1024 + i16 * 16;
  auto fetch = [&](int it, int kind, u32x4 (&T)[8]) {
    const unsigned seq1 = (unsigned)(4 + 8 * it + w + 1);
    lds_wait_ge<1>(lds, L_FULL + 4 * slot, seq1);
    const LAS unsigned char* sp = lds + L_RING + slot * SLOT;
    if (kind != 2) { T[0] = *(const LAS u32x4*)(sp + kr0); T[1] = *(const LAS u32x4*)(sp + kr1); T[2] = *(const LAS u32x4*)(sp + kr2); T[3] = *(const LAS u32x4*)(sp + kr3); }
    else {
#pragma unroll
      for (int r = 0; r < 4; ++r) T[r] = *(const LAS u32x4*)(sp + vr + r * 256);
    }
    if (kind == 1) { T[4] = *(const LAS u32x4*)(sp + 4096 + kr0); T[5] = *(const LAS u32x4*)(sp + 4096 + kr1); T[6] = *(const LAS u32x4*)(sp + 4096 + kr2); T[7] = *(const LAS u32x4*)(sp + 4096 + kr3); }
    else {
#pragma unroll
      for (int r = 0; r < 4; ++r) T[4 + r] = *(const LAS u32x4*)(sp + 4096 + vr + r * 256);
    }
    LDS_WAIT();
    lds_st(lds, L_FREE + 4 * slot, seq1);
    asm volatile("" : "+v"(T[0]), "+v"(T[1]), "+v"(T[2]), "+v"(T[3]), "+v"(T[4]), "+v"(T[5]), "+v"(T[6]), "+v"(T[7]) :: "memory");
    slot += 8; if (slot >= NS) slot -= NS;
  };
  u32x4 X0[8], X1[8], X2[8];
  fetch(0, 0, X0);
  fetch(1, 1, X1);
  fetch(2, 1, X2);
  {
    const unsigned off = WS_QG + ((h * 32 + 2 * (tid >> 5)) * 32 + (tid & 31)) * 16;
    u32x4 g0 = {0u, 0u, 0u, 0u}, g1 = {0u, 0u, 0u, 0u};
    bool ok = false;
    for (unsigned spin = 0; spin < SPIN_CAP; ++spin) {
      MEMFENCE();
      if (!ok) {
        g0 = __builtin_amdgcn_raw_buffer_load_b128(rs, off, 0, AUX_SC1);
        g1 = __builtin_amdgcn_raw_buffer_load_b128(rs, off + 512, 0, AUX_SC1);
        ok = g0[0] == tagA && g0[3] == tagB && g1[0] == tagA && g1[3] == tagB;
      }
      if (__all(ok)) break;
      __builtin_amdgcn_s_sleep(QSLEEP);
    }
    *(LAS unsigned*)(lds + L_Q + tid * 4) = cvtpk(u2f(g0[1]) + u2f(g1[1]), u2f(g0[2]) + u2f(g1[2]));
  }
  lds_arrive(lds, L_CNT + C_QIN, lane);
  lds_wait_ge<0>(lds, L_CNT + C_QIN, NC);
  u32x4 qA[4], qB[4];
#pragma unroll
  for (int s = 0; s < 4; ++s) {
    qA[s] = *(const LAS u32x4*)(lds + L_Q + ((i16 >> 2) * 128 + 32 * s + 8 * hh) * 2);
    qB[s] = *(const LAS u32x4*)(lds + L_Q + 1024 + ((i16 >> 2) * 128 + 32 * s + 8 * hh) * 2);
  }

  Sel sel;
  sel.l0 = jj == 0 ? 0x0c0c0100u : jj == 1 ? 0x01000c0cu : 0x0c0c0c0cu;  sel.h0 = jj == 0 ? 0x0c0c0302u : jj == 1 ? 0x03020c0cu : 0x0c0c0c0cu;
  sel.l1 = jj == 2 ? 0x0c0c0100u : jj == 3 ? 0x01000c0cu : 0x0c0c0c0cu;  sel.h1 = jj == 2 ? 0x0c0c0302u : jj == 3 ? 0x03020c0cu : 0x0c0c0c0cu;

  f32x4 b0 = {0.f, 0.f, 0.f, 0.f}, b1 = {0.f, 0.f, 0.f, 0.f};
  {
    const u32x2 t = tile_scores(X0, qB);
    const float m2 = swap32_max(swap16_max(max4(t))) * LOG2E;
    float l = 0.f;
    const u32x2 p = to_probs(t, m2, l);
    l = swap32_sum(swap16_sum(l));
    tile_pv(b0, b1, X0 + 4, p, sel);
    if (hh == 0 && jj == 0) *(LAS f32x2*)(lds + L_TB + ((i16 >> 2) * 8 + w) * 8) = f32x2{m2, l};
  }

  u32x2 P[16];
  float mx = -INFINITY;
#pragma unroll
  for (int i = 0; i < 8; ++i) {
    if (i >= 2) fetch(i + 1, 1, X1);
    const u32x4* T = i == 1 ? X2 : X1;
    P[2 * i] = tile_scores(T, qA);
    P[2 * i + 1] = tile_scores(T + 4, qA);
    mx = fmaxf(mx, fmaxf(max4(P[2 * i]), max4(P[2 * i + 1])));
  }
  mx = swap32_max(swap16_max(mx));
  if (hh == 0 && jj == 0) *(LAS float*)(lds + L_T1 + ((i16 >> 2) * 8 + w) * 4) = mx;
  lds_arrive(lds, L_CNT + C_MAX, lane);
  lds_wait_ge<0>(lds, L_CNT + C_MAX, NC);
  {
    const f32x4 m0 = *(const LAS f32x4*)(lds + L_T1 + (i16 >> 2) * 32), m1 = *(const LAS f32x4*)(lds + L_T1 + (i16 >> 2) * 32 + 16);
    const float m2 = fmaxf(fmaxf(fmaxf(m0[0], m0[1]), fmaxf(m0[2], m0[3])), fmaxf(fmaxf(m1[0], m1[1]), fmaxf(m1[2], m1[3]))) * LOG2E;
    float l = 0.f;
#pragma unroll
    for (int t = 0; t < 16; ++t) P[t] = to_probs(P[t], m2, l);
    l = swap32_sum(swap16_sum(l));
    if (hh == 0 && jj == 0) *(LAS float*)(lds + L_T2 + ((i16 >> 2) * 8 + w) * 4) = l;
  }
  lds_arrive(lds, L_CNT + C_SUM, lane);
  lds_wait_ge<0>(lds, L_CNT + C_SUM, NC);
  const int mbr = w >> 2, mg = w & 3;
  {
    const int ww = lane & 7;
    float M, L;
    if (mbr == 0) {
      M = oct_max(*(const LAS float*)(lds + L_T1 + (mg * 8 + ww) * 4)) * LOG2E;
      L = oct_sum(*(const LAS float*)(lds + L_T2 + (mg * 8 + ww) * 4));
    } else {
      const f32x2 ml = *(const LAS f32x2*)(lds + L_TB + (mg * 8 + ww) * 8);
      M = oct_max(ml[0]);
      L = oct_sum(ml[1] * __builtin_amdgcn_exp2f(ml[0] - M));
    }
    if (lane == 0) __builtin_amdgcn_raw_buffer_store_b128(u32x4{tagA, f2u(M), f2u(L), tagB}, rs, WS_ML + (((h * 2 + mbr) * 4 + mg) * 32 + j) * 16, 0, AUX_SC1);
  }

  f32x4 a0 = {0.f, 0.f, 0.f, 0.f}, a1 = {0.f, 0.f, 0.f, 0.f};
  float fA = 0.f, fB = 0.f;
#pragma unroll
  for (int i = 0; i < 8; ++i) {
    if (i == 4) {
      const unsigned off = WS_ML + (((h * 2 + mbr) * 4 + mg) * 32 + (lane & 31)) * 16;
      u32x4 mv = {0u, 0u, 0u, 0u};
      bool ok = false;
      for (unsigned spin = 0; spin < SPIN_CAP; ++spin) {
        MEMFENCE();
        if (!ok) { mv = __builtin_amdgcn_raw_buffer_load_b128(rs, off, 0, AUX_SC1); ok = mv[0] == tagA && mv[3] == tagB; }
        if (__all(ok)) break;
        __builtin_amdgcn_s_sleep(16);
      }
      const float M = swap16_max(row_max(u2f(mv[1])));
      const float L = swap16_sum(row_sum(u2f(mv[2]) * __builtin_amdgcn_exp2f(u2f(mv[1]) - M)));
      if (lane == 0) *(LAS f32x2*)(lds + L_FF + w * 8) = f32x2{M, 1.0f / L};
      lds_arrive(lds, L_CNT + C_FF, lane);
    }
    if (i == 7) {
      lds_wait_ge<0>(lds, L_CNT + C_FF, NC);
      const f32x4 m0 = *(const LAS f32x4*)(lds + L_T1 + hh * 32), m1 = *(const LAS f32x4*)(lds + L_T1 + hh * 32 + 16);
      const float m2 = fmaxf(fmaxf(fmaxf(m0[0], m0[1]), fmaxf(m0[2], m0[3])), fmaxf(fmaxf(m1[0], m1[1]), fmaxf(m1[2], m1[3]))) * LOG2E;
      const f32x2 FA = *(const LAS f32x2*)(lds + L_FF + hh * 8), FB = *(const LAS f32x2*)(lds + L_FF + (4 + hh) * 8);
      const float mB = *(const LAS float*)(lds + L_TB + (hh * 8 + w) * 8);
      fA = __builtin_amdgcn_exp2f(m2 - FA[0]) * FA[1];
      fB = __builtin_amdgcn_exp2f(mB - FB[0]) * FB[1];
      const int s = lane < MRG_SLOTS ? lane : 0;
      const unsigned need = (unsigned)(s + NS * ((139 - s) / NS) + 1);
      for (unsigned spin = 0; spin < LSPIN_CAP; ++spin) { MEMFENCE(); if (__all(*(volatile LAS unsigned*)(lds + L_FREE + 4 * s) >= need)) break; }
      MEMFENCE();
      LAS float* rB = (LAS float*)(lds + L_MRG) + ((8 + w) * 4 + hh) * MREC + 8 * i16;
      *(LAS f32x4*)(rB) = b0 * fB; *(LAS f32x4*)(rB + 4) = b1 * fB;
    }
    fetch(9 + i, 2, X1);
    tile_pv(a0, a1, X1, P[2 * i], sel);
    tile_pv(a0, a1, X1 + 4, P[2 * i + 1], sel);
  }

  const unsigned tA = tagA & 0xffffffu, tB = tagB & 0xffffffu;
  {
    LAS float* rA = (LAS float*)(lds + L_MRG) + (w * 4 + hh) * MREC + 8 * i16;
    *(LAS f32x4*)(rA) = a0 * fA; *(LAS f32x4*)(rA + 4) = a1 * fA;
  }
  LDS_BARRIER();
  if (tid < 256) {
    const int br = tid >> 7, g = (tid >> 5) & 3, dq = tid & 31;
    const LAS float* mrg = (const LAS float*)(lds + L_MRG) + (br * 8 * 4 + g) * MREC + 4 * dq;
    f32x4 o4[8];
#pragma unroll
    for (int ww = 0; ww < 8; ++ww) o4[ww] = *(const LAS f32x4*)(mrg + ww * 4 * MREC);
    const f32x4 o = ((o4[0] + o4[1]) + (o4[2] + o4[3])) + ((o4[4] + o4[5]) + (o4[6] + o4[7]));
    const int jc = br * 16 + g * 4 + (dq >> 3), k = dq & 7;
    __builtin_amdgcn_raw_buffer_store_b128(pack4(o, tA, tB), rs, WS_I1 + (((h * 32 + jc) * 32 + j) * 8 + k) * 16, 0, AUX_SC1);
  }

  const unsigned offA = WS_I1 + ((h * 32 + j) * 32 * 8 + (w & 3) * 64 + lane) * 16;
  u32x4 av = {0u, 0u, 0u, 0u};
  if (w >= 4) av = __builtin_amdgcn_raw_buffer_load_b128(rs, offA, 0, AUX_SC1);
  MEMFENCE();
  float W[32];
  lds_wait_ge<0>(lds, L_FULL + 4 * (143 % NS), 144u);
  {
    const LAS unsigned short* wp = (const LAS unsigned short*)(lds + L_RING + (140 % NS) * SLOT) + 64 * w + lane;
#pragma unroll
    for (int r = 0; r < 32; ++r) W[r] = u2f((unsigned)wp[r * 512] << 16);
#pragma unroll
    for (int r = 0; r < 32; r += 8) asm volatile("" : "+v"(W[r]), "+v"(W[r + 1]), "+v"(W[r + 2]), "+v"(W[r + 3]), "+v"(W[r + 4]), "+v"(W[r + 5]), "+v"(W[r + 6]), "+v"(W[r + 7]));
  }
  if (w >= 4) {
    bool ok = tags4(av, tA, tB);
    for (unsigned spin = 0; spin < SPIN_CAP && !__all(ok); ++spin) {
      MEMFENCE();
      if (!ok) { av = __builtin_amdgcn_raw_buffer_load_b128(rs, offA, 0, AUX_SC1); ok = tags4(av, tA, tB); }
    }
    const f32x4 o = red8_16_32(unpack4(av));
    if (lane < 8) {
#pragma unroll
      for (int e = 0; e < 4; ++e) *(LAS float*)(lds + L_H1 + ((4 * lane + e) * 4 + (w & 3)) * 4) = o[e];
    }
  }
  LDS_BARRIER();
  {
    const f32x4 ra = *(const LAS f32x4*)(lds + L_H1 + i16 * 16), rb = *(const LAS f32x4*)(lds + L_H1 + (16 + i16) * 16);
    const unsigned ob = cvtpk((ra[0] + ra[1]) + (ra[2] + ra[3]), (rb[0] + rb[1]) + (rb[2] + rb[3]));
    const float oa = lo16(ob), oc = hi16(ob);
    float y0 = 0.f, y1 = 0.f, y2 = 0.f, y3 = 0.f;
#define FM(acc, o, wv, n) "v_fmac_f32_dpp " acc ", " o ", " wv " row_newbcast:" #n " row_mask:0xf bank_mask:0xf\n\t"
#define ROWS(n0, n1, n2, n3) asm volatile("s_nop 1\n\t" \
      FM("%0", "%4", "%6", n0) FM("%1", "%5", "%7", n0) FM("%2", "%4", "%8", n1) FM("%3", "%5", "%9", n1) \
      FM("%0", "%4", "%10", n2) FM("%1", "%5", "%11", n2) FM("%2", "%4", "%12", n3) FM("%3", "%5", "%13", n3) \
      : "+v"(y0), "+v"(y1), "+v"(y2), "+v"(y3) \
      : "v"(oa), "v"(oc), "v"(W[n0]), "v"(W[16 + n0]), "v"(W[n1]), "v"(W[16 + n1]), "v"(W[n2]), "v"(W[16 + n2]), "v"(W[n3]), "v"(W[16 + n3]));
    ROWS(0, 1, 2, 3) ROWS(4, 5, 6, 7) ROWS(8, 9, 10, 11) ROWS(12, 13, 14, 15)
#undef ROWS
#undef FM
    const float y = (y0 + y1) + (y2 + y3);
    const f32x4 y4 = {y, dpp<0x55>(y), dpp<0xAA>(y), dpp<0xFF>(y)};
    const int c = (j >> 4) * 16 + 2 * w + (lane >> 5), k = (lane & 31) >> 2, pidx = h * 16 + (j & 15);
    if (jj == 0) __builtin_amdgcn_raw_buffer_store_b128(pack4(y4, tA, tB), rs, WS_I2 + ((c * 128 + pidx) * 8 + k) * 16, 0, AUX_SC1);
  }

  if (j < 4) {
    const int c = h * 4 + j;
    const unsigned off = WS_I2 + (c * 128 + 16 * w) * 128 + lane * 16;
    u32x4 v[2] = {{0u, 0u, 0u, 0u}, {0u, 0u, 0u, 0u}};
    bool ok = false;
    for (unsigned spin = 0; spin < SPIN_CAP; ++spin) {
      MEMFENCE();
      if (!ok) {
#pragma unroll
        for (int i = 0; i < 2; ++i) v[i] = __builtin_amdgcn_raw_buffer_load_b128(rs, off + i * 1024, 0, AUX_SC1);
        ok = tags4(v[0], tA, tB) && tags4(v[1], tA, tB);
      }
      if (__all(ok)) break;
    }
    const f32x4 s4 = red8_16_32(unpack4(v[0]) + unpack4(v[1]));
    if (lane < 8) {
#pragma unroll
      for (int e = 0; e < 4; ++e) *(LAS float*)(lds + L_F + ((4 * lane + e) * 8 + w) * 4) = s4[e];
    }
    LDS_BARRIER();
    if (tid < 32) {
      const f32x4 p0 = *(const LAS f32x4*)(lds + L_F + tid * 32), p1 = *(const LAS f32x4*)(lds + L_F + tid * 32 + 16);
      const float s = ((p0[0] + p0[1]) + (p0[2] + p0[3])) + ((p1[0] + p1[1]) + (p1[2] + p1[3]));
      const unsigned pk = cvtpk(s, dpp<XOR1>(s));
      if ((tid & 1) == 0) *(GAS unsigned*)((GAS unsigned char*)a.out + (c * 32 + tid) * 2) = pk;
      if (c == 0 && tid == 0) __builtin_amdgcn_raw_buffer_store_b64(u32x2{tagA, tagB}, rs, WS_GEN, 0, AUX_SC1);
    }
  }
}

extern "C" void kernel_launch(void* const* d_in, const int* in_sizes, int n_in, void* d_out, int out_size, void* d_ws, size_t ws_size, hipStream_t stream) {
  Args a;
  a.xA = (const unsigned char*)d_in[0]; a.kA = (const unsigned char*)d_in[1]; a.vA = (const unsigned char*)d_in[2]; a.wqA = (const unsigned char*)d_in[3]; a.woA = (const unsigned char*)d_in[4];
  a.xB = (const unsigned char*)d_in[5]; a.kB = (const unsigned char*)d_in[6]; a.vB = (const unsigned char*)d_in[7]; a.wqB = (const unsigned char*)d_in[8]; a.woB = (const unsigned char*)d_in[9];
  a.out = (unsigned short*)d_out;
  a.ws = (unsigned char*)d_ws;
  hipLaunchKernelGGL(fused_decode, dim3(NWG), dim3(NTHR), 0, stream, a);
}
```
